```python
import torch

def _softmax(x, axis=-1):
    return torch.softmax(x, dim=axis)
def _gelu(x):
    return torch.nn.functional.gelu(x, approximate="tanh")
def _silu(x):
    return torch.nn.functional.silu(x)
def _sigmoid(x):
    return torch.sigmoid(x)
def _layernorm(x, axis=-1, eps=1e-5):
    m = x.mean(dim=axis, keepdim=True)
    v = x.var(dim=axis, keepdim=True, unbiased=False)
    return (x - m) / torch.sqrt(v + eps)
def _scatter_add(tgt, idx, src, axis=0):
    return tgt.clone().index_add_(axis, idx, src)
def _segment_sum(src, seg, n):
    out = torch.zeros((n,) + src.shape[1:], dtype=src.dtype)
    return out.index_add_(0, seg, src)
def _pad(x, pw):
    flat = tuple(v for pair in reversed(pw) for v in pair)
    return torch.nn.functional.pad(x, flat)

kernel_name = 'concat_heads__ssm_conv_s4d__linear_attn_block__prompt'

def setup_inputs():

    _w = (lambda a: (lambda t: t.to(torch.bfloat16) if t.is_floating_point() else t)(torch.as_tensor(a)))
    def _A_setup():
        Bsz, L, D, N = 1, 2048, 448, 256
        return {
            "u": _w((lambda *s: torch.randn(*s, dtype=torch.bfloat16) * 0.05)(Bsz, L, D)),
            "log_A_real": _w((lambda *s: torch.randn(*s, dtype=torch.bfloat16) * 6.4)(D, N) * 0.5),
            "A_imag": _w((lambda *s: torch.randn(*s, dtype=torch.bfloat16) * 0.05)(D, N) * 3.14),
            "C_real": _w((lambda *s: torch.randn(*s, dtype=torch.bfloat16) * 0.2)(D, N) / N ** 0.5),
            "C_imag": _w((lambda *s: torch.randn(*s, dtype=torch.bfloat16) * 0.2)(D, N) / N ** 0.5),
            "log_dt": _w((lambda *s: torch.randn(*s, dtype=torch.bfloat16) * 0.05)(D) * 0.5 - 4.0),
            "D_skip": _w((lambda *s: torch.randn(*s, dtype=torch.bfloat16) * 0.05)(D)),
        }
    _A_in = _A_setup()

    def _B_setup():
        B, S, D = 1, 2048, 448
        return {
            "x": _w((lambda *s: torch.randn(*s, dtype=torch.bfloat16) * 0.05)(B, S, D)),
            "wqkv": _w((lambda *s: torch.randn(*s, dtype=torch.bfloat16) * 0.8)(D, 3 * D)),
            "wo": _w((lambda *s: torch.randn(*s, dtype=torch.bfloat16) * 0.05)(D, D)),
        }
    _B_in = _B_setup()

    return {
        "A_u": _A_in["u"],
        "A_log_A_real": _A_in["log_A_real"],
        "A_A_imag": _A_in["A_imag"],
        "A_C_real": _A_in["C_real"],
        "A_C_imag": _A_in["C_imag"],
        "A_log_dt": _A_in["log_dt"],
        "A_D_skip": _A_in["D_skip"],
        "B_x": _B_in["x"],
        "B_wqkv": _B_in["wqkv"],
        "B_wo": _B_in["wo"],
    }

def reference(A_u, A_log_A_real, A_A_imag, A_C_real, A_C_imag, A_log_dt, A_D_skip, B_x, B_wqkv, B_wo):
    def _A_ref(u, log_A_real, A_imag, C_real, C_imag, log_dt, D_skip):
        L = u.shape[1]
        dt = torch.exp(log_dt)
        A = -torch.exp(log_A_real) + 1j * A_imag
        C = (C_real + 1j * C_imag) * dt[:, None]
        pos = (lambda x, dt: x.to(dt))(torch.arange(L), log_dt.dtype)
        P = torch.exp((A * dt[:, None])[:, :, None] * pos[None, None, :])
        K = 2.0 * torch.einsum("dn,dnl->dl", C, P).real
        L2 = 2 * L
        u_t = (lambda x, *d: x.permute(*d))(u, 0, 2, 1)
        Kf = (lambda x, *a, **k: torch.fft.rfft(x.float(), *a, **k))(K, L2)
        Uf = (lambda x, *a, **k: torch.fft.rfft(x.float(), *a, **k))(u_t, L2)
        y = (lambda x, *a, **k: torch.fft.irfft(x, *a, **k).to(torch.bfloat16))(Uf * Kf[None, :, :], L2)[:, :, :L]
        y = (lambda x, *d: x.permute(*d))(y, 0, 2, 1)
        return y + u * D_skip[None, None, :]

    def _B_ref(x, wqkv, wo):
        B, S, D = x.shape
        H = 16; Dh = D // H
        qkv = (lambda x, *d: x.permute(*d))((x @ wqkv).reshape(B, S, 3, H, Dh), 2, 0, 3, 1, 4)
        q, k, v = qkv[0], qkv[1], qkv[2]
        phi = lambda t: torch.where(t > 0, t + 1, torch.exp(t)) + 1e-6
        pq, pk = phi(q), phi(k)
        kv = torch.einsum("bhsd,bhse->bhde", pk, v)
        z = torch.einsum("bhsd->bhd", pk)
        num = torch.einsum("bhqd,bhde->bhqe", pq, kv)
        den = torch.einsum("bhqd,bhd->bhq", pq, z)[..., None] + 1e-6
        o = num / den
        return (lambda x, *d: x.permute(*d))(o, 0, 2, 1, 3).reshape(B, S, D) @ wo

    a = _A_ref(A_u, A_log_A_real, A_A_imag, A_C_real, A_C_imag, A_log_dt, A_D_skip)
    b = _B_ref(B_x, B_wqkv, B_wo)
    return torch.concatenate([a, b], axis=-1)
```

```cpp
#include <hip/hip_runtime.h>
#include <hip/hip_bf16.h>
#include <stdint.h>

typedef unsigned short u16;
__device__ __forceinline__ float bf2f(u16 v) { return __uint_as_float(((unsigned)v) << 16); }
__device__ __forceinline__ u16 f2bf(float f) {
    __hip_bfloat16 b = __float2bfloat16(f);
    return *reinterpret_cast<u16*>(&b);
}
__device__ __forceinline__ float rbf(float f) { return bf2f(f2bf(f)); }

constexpr int L = 2048, D = 448, N = 256, H = 16, DH = 28;

__global__ void nv_kgen(const u16* logA, const u16* Aim, const u16* Cre, const u16* Cim, const u16* logdt, float* K) {
    int d = blockIdx.y; int l = blockIdx.x * 256 + threadIdx.x;
    float dt = rbf(expf(bf2f(logdt[d])));
    float p = rbf((float)l);
    float acc = 0.f;
    for (int n = 0; n < N; ++n) {
        float ar = -rbf(expf(bf2f(logA[d * N + n]))) * dt;
        float ai = bf2f(Aim[d * N + n]) * dt;
        float cr = bf2f(Cre[d * N + n]) * dt, ci = bf2f(Cim[d * N + n]) * dt;
        float m = expf(ar * p); float s, c; sincosf(ai * p, &s, &c);
        acc += m * (cr * c - ci * s);
    }
    K[d * L + l] = 2.f * acc;
}
__global__ void nv_conv(const u16* u, const float* K, const u16* Dskip, u16* out) {
    int d = blockIdx.x * 64 + (threadIdx.x & 63); int l = blockIdx.y * 4 + (threadIdx.x >> 6);
    float acc = 0.f;
    for (int j = 0; j <= l; ++j) acc += K[d * L + j] * bf2f(u[(l - j) * D + d]);
    float y = rbf(acc);
    float s = rbf(bf2f(u[l * D + d]) * bf2f(Dskip[d]));
    out[l * (2 * D) + d] = f2bf(y + s);
}
__global__ void nv_qkv(const u16* x, const u16* w, u16* qkv) {
    int c = blockIdx.x * 64 + (threadIdx.x & 63); int s = blockIdx.y * 4 + (threadIdx.x >> 6);
    float acc = 0.f;
    for (int k = 0; k < D; ++k) acc += bf2f(x[s * D + k]) * bf2f(w[k * 3 * D + c]);
    float t = rbf(acc);
    if (c < 2 * D) { float ph = t > 0.f ? rbf(t + 1.f) : rbf(expf(t)); t = rbf(ph + 1e-6f); }
    qkv[s * 3 * D + c] = f2bf(t);
}
__global__ void nv_kv(const u16* qkv, float* kv) {
    int h = blockIdx.x; int t = threadIdx.x; if (t >= 28 * 29) return;
    int d = t / 29, e = t % 29;
    float acc = 0.f;
    for (int s = 0; s < L; ++s) {
        float pk = bf2f(qkv[s * 3 * D + D + h * DH + d]);
        float v = e < 28 ? bf2f(qkv[s * 3 * D + 2 * D + h * DH + e]) : 1.f;
        acc += pk * v;
    }
    kv[(h * 28 + d) * 29 + e] = rbf(acc);
}
__global__ void nv_o(const u16* qkv, const float* kv, u16* o) {
    int c = blockIdx.x * 64 + (threadIdx.x & 63); int s = blockIdx.y * 4 + (threadIdx.x >> 6);
    int h = c / DH, e = c % DH;
    float num = 0.f, den = 0.f;
    for (int d = 0; d < DH; ++d) {
        float pq = bf2f(qkv[s * 3 * D + h * DH + d]);
        num += pq * kv[(h * 28 + d) * 29 + e];
        den += pq * kv[(h * 28 + d) * 29 + 28];
    }
    num = rbf(num); den = rbf(rbf(den) + 1e-6f);
    o[s * D + c] = f2bf(num / den);
}
__global__ void nv_out(const u16* o, const u16* wo, u16* out) {
    int c = blockIdx.x * 64 + (threadIdx.x & 63); int s = blockIdx.y * 4 + (threadIdx.x >> 6);
    float acc = 0.f;
    for (int k = 0; k < D; ++k) acc += bf2f(o[s * D + k]) * bf2f(wo[k * D + c]);
    out[s * 2 * D + D + c] = f2bf(acc);
}

extern "C" void kernel_launch(void* const* d_in, const int* in_sizes, int n_in,
                              void* d_out, int out_size, void* d_ws, size_t ws_size,
                              hipStream_t stream) {
    const u16* u = (const u16*)d_in[0];
    const u16* logA = (const u16*)d_in[1];
    const u16* Aim = (const u16*)d_in[2];
    const u16* Cre = (const u16*)d_in[3];
    const u16* Cim = (const u16*)d_in[4];
    const u16* logdt = (const u16*)d_in[5];
    const u16* Dskip = (const u16*)d_in[6];
    const u16* x = (const u16*)d_in[7];
    const u16* wqkv = (const u16*)d_in[8];
    const u16* wo = (const u16*)d_in[9];
    u16* out = (u16*)d_out;
    char* ws = (char*)d_ws;
    float* K = (float*)ws;
    u16* qkv = (u16*)(ws + (4 << 20));
    float* kv = (float*)(ws + (12 << 20));
    u16* o = (u16*)(ws + (13 << 20));
    nv_kgen<<<dim3(L / 256, D), 256, 0, stream>>>(logA, Aim, Cre, Cim, logdt, K);
    nv_conv<<<dim3(D / 64, L / 4), 256, 0, stream>>>(u, K, Dskip, out);
    nv_qkv<<<dim3(3 * D / 64, L / 4), 256, 0, stream>>>(x, wqkv, qkv);
    nv_kv<<<H, 832, 0, stream>>>(qkv, kv);
    nv_o<<<dim3(D / 64, L / 4), 256, 0, stream>>>(qkv, kv, o);
    nv_out<<<dim3(D / 64, L / 4), 256, 0, stream>>>(o, wo, out);
}
```

```cpp
#include <hip/hip_runtime.h>
#include <stdint.h>

typedef unsigned short u16;
typedef unsigned int u32;
typedef unsigned long long u64;
typedef __bf16 bf16x8 __attribute__((ext_vector_type(8)));
typedef __bf16 bf16x2 __attribute__((ext_vector_type(2)));
typedef short s16x4 __attribute__((ext_vector_type(4)));
typedef float f32x4 __attribute__((ext_vector_type(4)));
typedef float f32x2 __attribute__((ext_vector_type(2)));
typedef u32 u32x2 __attribute__((ext_vector_type(2)));
typedef u32 u32x4 __attribute__((ext_vector_type(4)));
#define LAS __attribute__((address_space(3)))
#define GAS __attribute__((address_space(1)))
#define DI __device__ __forceinline__
#define RLX_AGENT __ATOMIC_RELAXED, __HIP_MEMORY_SCOPE_AGENT
typedef __amdgpu_buffer_rsrc_t rsrc_t;

#define STAMP(i) do {} while (0)

constexpr u32 WS_EPA = 0;
constexpr u32 WS_EPS = 256;
constexpr u32 WS_FLA = 4096;
constexpr u32 WS_FLS = 8192;
constexpr u32 WS_KVP = 65536;
constexpr u32 WS_OX  = WS_KVP + (1u << 20);
constexpr u32 WS_UX  = WS_OX + (2u << 20);
constexpr u32 WS_YX  = WS_UX + (2u << 20);
constexpr u32 WS_END = WS_YX + (2u << 20);
constexpr u32 WS_DBG = 16u << 20;
constexpr int SPIN_CAP = 1 << 16;

struct Args {
    const u16 *u, *logA, *Aim, *Cre, *Cim, *logdt, *Dskip, *x, *wqkv, *wo;
    u16* out;
    char* ws;
};

DI f32x4 mfma(u32x4 a, u32x4 b, f32x4 c) {
    return __builtin_amdgcn_mfma_f32_16x16x32_bf16(__builtin_bit_cast(bf16x8, a), __builtin_bit_cast(bf16x8, b), c, 0, 0, 0);
}
DI u32 pk(float lo, float hi) { f32x2 v = {lo, hi}; return __builtin_bit_cast(u32, __builtin_convertvector(v, bf16x2)); }
DI float bflo(u32 v) { return __uint_as_float(v << 16); }
DI float bfhi(u32 v) { return __uint_as_float(v & 0xffff0000u); }
DI float rbf(float f) { return bflo(pk(f, f)); }
DI rsrc_t mkrsrc(const void* p, u32 bytes) { return __builtin_amdgcn_make_buffer_rsrc((void*)p, 0, bytes, 0x00020000); }
DI u32x4 bld(rsrc_t r, u32 voff, u32 soff) { return __builtin_amdgcn_raw_buffer_load_b128(r, voff, soff, 0); }
DI u32x4 bld_dev(rsrc_t r, u32 voff, u32 soff) { return __builtin_amdgcn_raw_buffer_load_b128(r, voff, soff, 16); }
DI void bst_dev(u32x4 v, rsrc_t r, u32 voff, u32 soff) { __builtin_amdgcn_raw_buffer_store_b128(v, r, voff, soff, 16); }
DI u32x4 lds128(LAS char* p) { return *(LAS u32x4*)p; }
DI void sts128(LAS char* p, u32x4 v) { *(LAS u32x4*)p = v; }
DI void sts64(LAS char* p, u32 lo, u32 hi) { u32x2 v = {lo, hi}; *(LAS u32x2*)p = v; }
DI u32x4 trfrag(LAS char* p0, LAS char* p1) {
    u32x2 a = __builtin_bit_cast(u32x2, __builtin_amdgcn_ds_read_tr16_b64_v4i16((LAS s16x4*)p0));
    u32x2 b = __builtin_bit_cast(u32x2, __builtin_amdgcn_ds_read_tr16_b64_v4i16((LAS s16x4*)p1));
    u32x4 r = {a.x, a.y, b.x, b.y};
    return r;
}
DI void cbar() { asm volatile("" ::: "memory"); }
DI void wg_barrier() { asm volatile("s_waitcnt lgkmcnt(0)\n\ts_barrier" ::: "memory"); }
DI void drain() { asm volatile("s_waitcnt vmcnt(0)" ::: "memory"); }
DI u32 umax4(u32x4 v) { return max(max(v.x, v.y), max(v.z, v.w)); }
DI u32x4 sane(u32x4 v) { u32x4 r = {min(v.x, 0xfffffffeu), min(v.y, 0xfffffffeu), min(v.z, 0xfffffffeu), min(v.w, 0xfffffffeu)}; return r; }
DI float phi(float t) {
    float e = __builtin_amdgcn_exp2f(t * 1.44269504089f);
    return fmaxf(t, 1e-6f) + __builtin_amdgcn_fmed3f(e, 0.f, 1.f);
}
DI void wait_flags(const char* p, bool active, u64 tag) {
    for (int sp = 0; sp < SPIN_CAP; ++sp) {
        u64 f = tag;
        if (active) f = __hip_atomic_load((GAS u64*)p, RLX_AGENT);
        if (__all(f == tag)) break;
        __builtin_amdgcn_s_sleep(1);
    }
    cbar();
}

constexpr int A_W = 0, A_X = 57344, A_KVB = 73728, A_E = 75776;

DI void attn_role(const Args& a, LAS char* sm) {
    const int tid = threadIdx.x, lane = tid & 63, wave = __builtin_amdgcn_readfirstlane(tid >> 6);
    const int b = blockIdx.x, xcd = b & 7, j = b >> 3;
    const int h = (xcd & 3) * 4 + (j & 3), rb = (xcd >> 2) * 8 + (j >> 2);
    const int c0 = (rb + 2 * (j & 3)) % 7;
    const int g = lane >> 4, r16 = lane & 15, q = (lane >> 2) & 3, p = lane & 3;
    const rsrc_t rx = mkrsrc(a.x, 2048 * 896), rw = mkrsrc(a.wqkv, 448 * 2688), rwo = mkrsrc(a.wo, 448 * 896), rws = mkrsrc(a.ws, WS_END);
    STAMP(0);
    __builtin_amdgcn_s_setprio(2);

    if (wave == 3) {
        u64 E = __hip_atomic_load((GAS u64*)(a.ws + WS_EPA), RLX_AGENT);
        const u32x4 ones = {~0u, ~0u, ~0u, ~0u};
#pragma unroll
        for (int i = 0; i < 4; ++i) bst_dev(ones, rws, lane * 16, WS_KVP + (h * 16 + rb) * 4096 + i * 1024);
#pragma unroll
        for (int i = 0; i < 8; ++i) bst_dev(ones, rws, lane * 16, WS_OX + ((rb * 8 + i) * 16 + h) * 1024);
        drain();
        if (lane == 0) {
            __hip_atomic_store((GAS u64*)(a.ws + WS_FLA + (h * 16 + rb) * 8), E + 1, RLX_AGENT);
            *(LAS u64*)(sm + A_E) = E + 1;
        }
    }
    const int wc = tid & 3, wseg = (tid >> 2) & 1, wrow = tid >> 3;
    const u32 wvoff = wrow * 2688 + (1 + wseg) * 896 + h * 56 + (wc == 3 ? 40 : wc * 16);
    const u32 xvoff = (rb * 128 + wave * 32 + (lane >> 3)) * 896 + (lane & 7) * 16;
    const u32 qvoff = (tid >> 2) * 2688 + h * 56 + (wc == 3 ? 40 : wc * 16);
    u32x4 wr[7][2], xr[7][4], qr[7];
#pragma unroll
    for (int i = 0; i < 7; ++i) {
        int kc = i + c0; kc = kc >= 7 ? kc - 7 : kc;
#pragma unroll
        for (int m = 0; m < 2; ++m) wr[i][m] = bld(rw, wvoff, (kc * 64 + m * 32) * 2688);
#pragma unroll
        for (int m = 0; m < 4; ++m) xr[i][m] = bld(rx, xvoff, kc * 128 + m * 8 * 896);
    }
    cbar();
#pragma unroll
    for (int i = 0; i < 4; ++i) {
        int idx = tid + 256 * i;
        if (idx < 896) {
            int k = idx >> 1, f = ((k >> 1) & 1) | ((k >> 2) & 2);
            sts64(sm + A_W + k * 128 + ((((idx & 1) * 2 + 1) ^ f) * 32) + 24, 0, 0);
        }
    }
    const int fw = ((wrow >> 1) & 1) | ((wrow >> 2) & 2);
    LAS char* wdst = sm + A_W + wrow * 128 + (((wseg * 2 + (wc >> 1)) ^ fw) * 32) + (wc == 3 ? 8 : (wc & 1) * 16);
    LAS char* xs = sm + A_X + wave * 4096;
    LAS char* xdst0 = xs + (lane >> 3) * 128 + (((lane & 7) ^ (lane >> 4)) * 16);
    LAS char* xdst1 = xs + (lane >> 3) * 128 + (((lane & 7) ^ (4 + (lane >> 4))) * 16);
    LAS char* xsrc0 = xs + r16 * 128 + ((g ^ (r16 >> 1)) * 16);
    LAS char* xsrc1 = xs + r16 * 128 + (((4 + g) ^ (r16 >> 1)) * 16);
    const int ft = (q >> 1) | ((g & 1) << 1);
    LAS char* wsrc[4];
#pragma unroll
    for (int ct = 0; ct < 4; ++ct) wsrc[ct] = sm + A_W + (8 * g + q) * 128 + ((ct ^ ft) * 32) + 8 * p;

    f32x4 acc[2][4];
#pragma unroll
    for (int st = 0; st < 2; ++st)
#pragma unroll
        for (int ct = 0; ct < 4; ++ct) acc[st][ct] = f32x4{0.f, 0.f, 0.f, 0.f};
    u32x4 xf[14][2];
#pragma unroll
    for (int i = 0; i < 7; ++i) {
#pragma unroll
        for (int m = 0; m < 2; ++m) {
            sts64(wdst + (i * 64 + m * 32) * 128, wr[i][m].x, wr[i][m].y);
            sts64(wdst + (i * 64 + m * 32) * 128 + 8, wr[i][m].z, wr[i][m].w);
        }
#pragma unroll
        for (int m = 0; m < 4; ++m) sts128(((m & 1) ? xdst1 : xdst0) + m * 1024, xr[i][m]);
        if (i == 2) {
#pragma unroll
            for (int ii = 0; ii < 7; ++ii) {
                int kc = ii + c0; kc = kc >= 7 ? kc - 7 : kc;
                qr[ii] = bld(rw, qvoff, kc * 64 * 2688);
            }
        }
        wg_barrier();
        if (i == 0) STAMP(1);
#pragma unroll
        for (int st = 0; st < 2; ++st) {
            xf[2 * i][st] = lds128(xsrc0 + st * 2048);
            xf[2 * i + 1][st] = lds128(xsrc1 + st * 2048);
        }
#pragma unroll
        for (int ks = 0; ks < 2; ++ks)
#pragma unroll
            for (int ct = 0; ct < 4; ++ct) {
                LAS char* s = wsrc[ct] + (2 * i + ks) * 4096;
                u32x4 wf = trfrag(s, s + 512);
#pragma unroll
                for (int st = 0; st < 2; ++st) acc[st][ct] = mfma(xf[2 * i + ks][st], wf, acc[st][ct]);
            }
        cbar();
    }
    STAMP(2);
    {
        u32x4 pkf[2], vf[2];
#pragma unroll
        for (int t = 0; t < 2; ++t) {
            pkf[t] = u32x4{pk(phi(acc[0][t].x), phi(acc[0][t].y)), pk(phi(acc[0][t].z), phi(acc[0][t].w)),
                           pk(phi(acc[1][t].x), phi(acc[1][t].y)), pk(phi(acc[1][t].z), phi(acc[1][t].w))};
            vf[t] = u32x4{pk(acc[0][2 + t].x, acc[0][2 + t].y), pk(acc[0][2 + t].z, acc[0][2 + t].w),
                          pk(acc[1][2 + t].x, acc[1][2 + t].y), pk(acc[1][2 + t].z, acc[1][2 + t].w)};
        }
        if (r16 == 12) vf[1] = u32x4{0x3f803f80u, 0x3f803f80u, 0x3f803f80u, 0x3f803f80u};
#pragma unroll
        for (int dt = 0; dt < 2; ++dt)
#pragma unroll
            for (int et = 0; et < 2; ++et) {
                f32x4 kv = mfma(pkf[dt], vf[et], f32x4{0.f, 0.f, 0.f, 0.f});
                sts128(xs + (dt * 2 + et) * 1024 + lane * 16, __builtin_bit_cast(u32x4, kv));
            }
    }
    wg_barrier();
    {
        f32x4 s = __builtin_bit_cast(f32x4, lds128(sm + A_X + tid * 16));
#pragma unroll
        for (int w = 1; w < 4; ++w) s += __builtin_bit_cast(f32x4, lds128(sm + A_X + w * 4096 + tid * 16));
        bst_dev(sane(__builtin_bit_cast(u32x4, s)), rws, tid * 16, WS_KVP + (h * 16 + rb) * 4096);
    }
    STAMP(3);
    {
        const int qrow = tid >> 2;
        LAS char* qdst = sm + A_W + qrow * 64 + ((((wc >> 1) ^ (qrow >> 3)) & 1) * 32) + (wc == 3 ? 8 : (wc & 1) * 16);
#pragma unroll
        for (int i = 0; i < 7; ++i) {
            sts64(qdst + i * 4096, qr[i].x, qr[i].y);
            sts64(qdst + i * 4096 + 8, qr[i].z, qr[i].w);
        }
    }
    const bool p3 = j < 28;
    const int r64 = xcd * 4 + j / 7, nb = j % 7;
    const u64 tag = *(LAS u64*)(sm + A_E);
    wait_flags(a.ws + WS_FLA + (lane < 16 ? h * 16 + lane : (lane & 15) * 16 + (r64 >> 1)) * 8, lane < 16 || (lane < 32 && p3), tag);
    wg_barrier();
    STAMP(4);
    u32x4 gk[16];
#pragma unroll
    for (int i = 0; i < 16; ++i) gk[i] = bld_dev(rws, tid * 16, WS_KVP + (h * 16 + ((rb + i) & 15)) * 4096);
    cbar();
    u32x4 pq[2];
    {
        f32x4 aq[2][2];
#pragma unroll
        for (int st = 0; st < 2; ++st)
#pragma unroll
            for (int dt = 0; dt < 2; ++dt) aq[st][dt] = f32x4{0.f, 0.f, 0.f, 0.f};
        LAS char* qsrc[2];
#pragma unroll
        for (int dt = 0; dt < 2; ++dt) qsrc[dt] = sm + A_W + (8 * g + q) * 64 + (((dt ^ g) & 1) * 32) + 8 * p;
#pragma unroll
        for (int ks = 0; ks < 14; ++ks)
#pragma unroll
            for (int dt = 0; dt < 2; ++dt) {
                LAS char* s = qsrc[dt] + ks * 2048;
                u32x4 wf = trfrag(s, s + 256);
#pragma unroll
                for (int st = 0; st < 2; ++st) aq[st][dt] = mfma(wf, xf[ks][st], aq[st][dt]);
            }
#pragma unroll
        for (int st = 0; st < 2; ++st)
            pq[st] = u32x4{pk(phi(aq[st][0].x), phi(aq[st][0].y)), pk(phi(aq[st][0].z), phi(aq[st][0].w)),
                           pk(phi(aq[st][1].x), phi(aq[st][1].y)), pk(phi(aq[st][1].z), phi(aq[st][1].w))};
    }
    cbar();
    STAMP(5);
    u32x4 wo[14];
    {
        const u32 ovoff = (tid >> 3) * 896 + nb * 128 + (tid & 7) * 16;
#pragma unroll
        for (int i = 0; i < 14; ++i) wo[i] = bld(rwo, ovoff, i * 32 * 896);
    }
    cbar();
    for (int sp = 0; sp < SPIN_CAP; ++sp) {
        u32 m = 0;
#pragma unroll
        for (int i = 0; i < 16; ++i) m = max(m, umax4(gk[i]));
        if (__all(m != 0xffffffffu)) break;
        __builtin_amdgcn_s_sleep(1);
        cbar();
#pragma unroll
        for (int i = 0; i < 16; ++i) gk[i] = bld_dev(rws, tid * 16, WS_KVP + (h * 16 + ((rb + i) & 15)) * 4096);
        cbar();
    }
    {
        f32x4 s[8];
#pragma unroll
        for (int i = 0; i < 8; ++i) s[i] = __builtin_bit_cast(f32x4, gk[i]) + __builtin_bit_cast(f32x4, gk[i + 8]);
#pragma unroll
        for (int i = 0; i < 4; ++i) s[i] += s[i + 4];
        f32x4 t = (s[0] + s[2]) + (s[1] + s[3]);
        u32 lo = pk(t.x, t.y), hi = pk(t.z, t.w);
        const int dt = wave >> 1, et = wave & 1;
        if (dt == 1 && g == 3) { lo = 0; hi = 0; }
        sts64(sm + A_KVB + (et * 64 + lane) * 16 + dt * 8, lo, hi);
    }
    wg_barrier();
    STAMP(6);
    {
        u32x4 kf0 = lds128(sm + A_KVB + lane * 16), kf1 = lds128(sm + A_KVB + 1024 + lane * 16);
        u32x4 zf = lds128(sm + A_KVB + 1024 + (12 + 16 * g) * 16);
#pragma unroll
        for (int st = 0; st < 2; ++st) {
            const f32x4 z4 = {0.f, 0.f, 0.f, 0.f};
            f32x4 n0 = mfma(kf0, pq[st], z4), n1 = mfma(kf1, pq[st], z4), dn = mfma(zf, pq[st], z4);
            float rd = __builtin_amdgcn_rcpf(dn.x + 1e-6f);
            u32x4 of = {pk(n0.x * rd, n0.y * rd), pk(n0.z * rd, n0.w * rd), pk(n1.x * rd, n1.y * rd), pk(n1.z * rd, n1.w * rd)};
            if (g == 3) { of.z = 0; of.w = 0; }
            bst_dev(sane(of), rws, lane * 16, WS_OX + ((rb * 8 + wave * 2 + st) * 16 + h) * 1024);
        }
    }
    STAMP(7);
    if (!p3) return;
    {
        const int orow = tid >> 3, oc = tid & 7;
        LAS char* odst = sm + A_W + orow * 128 + (((((2 * oc) & 3) ^ (orow >> 1)) & 3) * 32) + (oc >> 1) * 8;
        LAS char* odst1 = sm + A_W + orow * 128 + ((((((2 * oc) & 3) + 1) ^ (orow >> 1)) & 3) * 32) + (oc >> 1) * 8;
#pragma unroll
        for (int i = 0; i < 14; ++i) {
            sts64(odst + i * 4096, wo[i].x, wo[i].y);
            sts64(odst1 + i * 4096, wo[i].z, wo[i].w);
        }
    }
    u32x4 go[16];
    for (int sp = 0; sp < SPIN_CAP; ++sp) {
        cbar();
#pragma unroll
        for (int i = 0; i < 16; ++i) go[i] = bld_dev(rws, lane * 16, WS_OX + ((r64 * 4 + wave) * 16 + i) * 1024);
        cbar();
        if (sp == 0) wg_barrier();
        u32 m = 0;
#pragma unroll
        for (int i = 0; i < 16; ++i) m = max(m, umax4(go[i]));
        if (__all(m != 0xffffffffu)) break;
        __builtin_amdgcn_s_sleep(1);
    }
    STAMP(8);
    {
        LAS char *os0[4], *os1[4];
#pragma unroll
        for (int t = 0; t < 4; ++t) {
            os0[t] = sm + A_W + (4 * g + q) * 128 + (((t ^ (2 * g + (q >> 1))) & 3) * 32) + 8 * p;
            os1[t] = os0[t] + (g == 3 ? 0 : 2048);
        }
        f32x4 oa[4];
#pragma unroll
        for (int t = 0; t < 4; ++t) oa[t] = f32x4{0.f, 0.f, 0.f, 0.f};
#pragma unroll
        for (int hh = 0; hh < 16; ++hh)
#pragma unroll
            for (int t = 0; t < 4; ++t) {
                const int tt = t ^ ((hh & 1) * 2);
                u32x4 wf = trfrag(os0[tt] + hh * 28 * 128, os1[tt] + hh * 28 * 128);
                oa[t] = mfma(wf, go[hh], oa[t]);
            }
        u32x4 o0 = {pk(oa[0].x, oa[0].y), pk(oa[0].z, oa[0].w), pk(oa[1].x, oa[1].y), pk(oa[1].z, oa[1].w)};
        u32x4 o1 = {pk(oa[2].x, oa[2].y), pk(oa[2].z, oa[2].w), pk(oa[3].x, oa[3].y), pk(oa[3].z, oa[3].w)};
        u32x4* dst = (u32x4*)(a.out + (size_t)(r64 * 64 + wave * 16 + r16) * 896 + 448 + nb * 64 + 16 * g);
        dst[0] = o0; dst[1] = o1;
    }
    if (b == 0 && tid == 0) __hip_atomic_store((GAS u64*)(a.ws + WS_EPA), tag, RLX_AGENT);
    STAMP(9);
}

constexpr int S_TAB = 0;
constexpr int S_KC = 34816;
constexpr int S_P = 47104;
constexpr int S_UT = 52480;
constexpr int S_TR = 61696;
constexpr int S_E = 70912;

struct cf { float r, i; };
DI cf cmul(cf a, cf b) { return cf{a.r * b.r - a.i * b.i, a.r * b.i + a.i * b.r}; }
DI cf cexp(float lr, float rev, float pw) {
    float m = __builtin_amdgcn_exp2f(lr * pw), f = __builtin_amdgcn_fractf(rev * pw);
    return cf{m * __builtin_amdgcn_cosf(f), m * __builtin_amdgcn_sinf(f)};
}
DI int rne_div(int x, int s) {
    int y = x + 64;
    return ((y + (1 << (s - 1)) - 1 + ((y >> s) & 1)) >> s) - (64 >> s);
}

DI void ssm_role(const Args& a, LAS char* sm) {
    const int tid = threadIdx.x, lane = tid & 63, wave = __builtin_amdgcn_readfirstlane(tid >> 6);
    const int bb = blockIdx.x - 256, ct = bb & 7, j = bb >> 3;
    if (ct == 7) return;
    const int ch = wave >> 1, hf = wave & 1, d = ct * 64 + 2 * j + ch;
    const int g = lane >> 4, r16 = lane & 15;
    const rsrc_t rws = mkrsrc(a.ws, WS_END);
    STAMP(0);
    const int po = d * 128 + hf * 64 + lane;
    const u32 pA = ((const u32*)a.logA)[po], pI = ((const u32*)a.Aim)[po], pCr = ((const u32*)a.Cre)[po], pCi = ((const u32*)a.Cim)[po];
    const u32 ldt = a.logdt[d];
    const u32 dsk = ((const u32*)a.Dskip)[ct * 32 + j];
    u64 E = 0;
    if (wave < 2) {
        if (wave == 0) E = __hip_atomic_load((GAS u64*)(a.ws + WS_EPS + ct * 256), RLX_AGENT);
        const u32x4* src = (const u32x4*)(a.u + (size_t)(64 * j + 32 * wave + 4 * (lane >> 3)) * 448 + 64 * ct) + (lane & 7);
        u32x4 v[4];
#pragma unroll
        for (int i = 0; i < 4; ++i) v[i] = src[i * 56];
        LAS char* tw = sm + S_TR + (lane & 7) * 288 + (wave * 8 + (lane >> 3)) * 16;
        sts128(tw + 0 * 8 * 288, u32x4{v[0].x, v[1].x, v[2].x, v[3].x});
        sts128(tw + 1 * 8 * 288, u32x4{v[0].y, v[1].y, v[2].y, v[3].y});
        sts128(tw + 2 * 8 * 288, u32x4{v[0].z, v[1].z, v[2].z, v[3].z});
        sts128(tw + 3 * 8 * 288, u32x4{v[0].w, v[1].w, v[2].w, v[3].w});
        cbar();
#pragma unroll
        for (int i = 0; i < 4; ++i) {
            const int pr = 8 * i + (lane >> 3);
            u32x4 t = lds128(sm + S_TR + ((pr & 3) * 8 + (pr >> 2)) * 288 + (wave * 8 + (lane & 7)) * 16);
            bst_dev(t, rws, pr * 8192 + (lane & 7) * 16, WS_UX + ct * 32 * 8192 + j * 256 + wave * 128);
        }
    } else {
        const u32x4 ones = {~0u, ~0u, ~0u, ~0u};
#pragma unroll
        for (int i = 0; i < 4; ++i) bst_dev(ones, rws, lane * 16, WS_YX + (ct * 32 + j) * 8192 + (wave - 2) * 4096 + i * 1024);
        if (lane < 32) sts128(sm + S_UT + (wave - 2) * 4608 + lane * 16, u32x4{0, 0, 0, 0});
    }
    cbar();
    STAMP(1);
    LAS char* tab = sm + S_TAB + wave * 8704;
    LAS char* tw = tab + lane * 8;
    LAS char* tr = tab + r16 * 544 + g * 16;
    const float dt = rbf(__builtin_amdgcn_exp2f(bflo(ldt) * 1.44269504089f));
    float lr[2], rev[2]; cf G[2];
#pragma unroll
    for (int m = 0; m < 2; ++m) {
        float la = m ? bfhi(pA) : bflo(pA);
        lr[m] = -rbf(__builtin_amdgcn_exp2f(la * 1.44269504089f)) * dt * 1.44269504089f;
        rev[m] = (m ? bfhi(pI) : bflo(pI)) * dt * 0.15915494309f;
        G[m] = cf{(m ? bfhi(pCr) : bflo(pCr)) * dt * 2.f, (m ? bfhi(pCi) : bflo(pCi)) * dt * 2.f};
    }
#define EMIT(row, v) sts64(tw + (row) * 544, pk(v[0].r, v[0].i), pk(v[1].r, v[1].i))
#define CM(dst, x, y) do { dst[0] = cmul(x[0], y[0]); dst[1] = cmul(x[1], y[1]); } while (0)
    u32x4 ff[8];
    {
        cf e8[2], F[2];
        e8[0] = cexp(lr[0], -rev[0], 8.f); e8[1] = cexp(lr[1], -rev[1], 8.f);
        sts64(tw, 0x00003f80u, 0x00003f80u);
        F[0] = e8[0]; F[1] = e8[1];
        EMIT(1, F);
#pragma unroll
        for (int i = 2; i < 16; ++i) { CM(F, F, e8); EMIT(i, F); }
        cbar();
#pragma unroll
        for (int ks = 0; ks < 8; ++ks) ff[ks] = lds128(tr + ks * 64);
        cbar();
    }
    cf w1[2], Hh[2], T[2];
    w1[0] = cexp(lr[0], rev[0], 1.f); w1[1] = cexp(lr[1], rev[1], 1.f);
    Hh[0] = cexp(lr[0], rev[0], 128.f); Hh[1] = cexp(lr[1], rev[1], 128.f);
    LAS char* kc = sm + S_KC + wave * 3072;
    f32x4 ka;
#define KMM() do { cbar(); ka = f32x4{0.f, 0.f, 0.f, 0.f}; _Pragma("unroll") for (int ks = 0; ks < 8; ++ks) ka = mfma(lds128(tr + ks * 64), ff[ks], ka); cbar(); } while (0)
#pragma unroll
    for (int i = 0; i < 2; ++i) {
        EMIT(8 * i, G);
        T[0] = G[0]; T[1] = G[1];
#pragma unroll
        for (int r = 1; r < 8; ++r) { CM(T, T, w1); EMIT(8 * i + r, T); }
        CM(G, G, Hh);
    }
    KMM();
    sts128(kc + (128 * (g >> 1) + 4 * (g & 1) + 8 * r16) * 4, __builtin_bit_cast(u32x4, ka));
    drain();
    wg_barrier();
    if (tid == 0) {
        __hip_atomic_store((GAS u64*)(a.ws + WS_FLS + (ct * 32 + j) * 8), E + 1, RLX_AGENT);
        *(LAS u64*)(sm + S_E) = E + 1;
    }
    STAMP(2);
    CM(w1, w1, w1);
#pragma unroll
    for (int i = 0; i < 2; ++i) {
        EMIT(4 * i, G);
        T[0] = G[0]; T[1] = G[1];
#pragma unroll
        for (int r = 1; r < 4; ++r) { CM(T, T, w1); EMIT(4 * i + r, T); }
        CM(G, G, Hh);
    }
    CM(w1, w1, w1);
#pragma unroll
    for (int i = 0; i < 4; ++i) {
        EMIT(8 + 2 * i, G);
        CM(T, G, w1); EMIT(9 + 2 * i, T);
        CM(G, G, Hh);
    }
    KMM();
    {
        LAS char* k0 = kc + (g < 2 ? 256 + 64 * g + 4 * r16 : 384 + 64 * (g - 2) + 2 * r16) * 4;
        u32x4 kb = __builtin_bit_cast(u32x4, ka);
        sts64(k0, kb.x, kb.y);
        sts64(k0 + (g < 2 ? 8 : 128), kb.z, kb.w);
    }
#pragma unroll
    for (int i = 0; i < 9; ++i) { EMIT(i, G); if (i < 8) CM(G, G, Hh); }
    KMM();
    {
        LAS float* k0 = (LAS float*)(kc + (512 + 64 * g + r16) * 4);
        k0[0] = ka.x; k0[16] = ka.y; k0[32] = ka.z; k0[48] = ka.w;
    }
    wg_barrier();
    STAMP(3);
    const u64 tag = *(LAS u64*)(sm + S_E);
    wait_flags(a.ws + WS_FLS + (ct * 32 + (lane & 31)) * 8, lane < 32, tag);
    u32x4 uu[2];
#pragma unroll
    for (int i = 0; i < 2; ++i) uu[i] = bld_dev(rws, tid * 16, WS_UX + (ct * 32 + j) * 8192 + i * 4096);
    cbar();
    if (j == 0 && tid == 0) __hip_atomic_store((GAS u64*)(a.ws + WS_EPS + ct * 256), tag, RLX_AGENT);
    {
        LAS float* k0 = (LAS float*)(sm + S_KC + ch * 6144);
        LAS u32* P = (LAS u32*)(sm + S_P + ch * 2688);
#pragma unroll
        for (int it = 0; it < 6; ++it) {
            int e = (tid & 127) + 128 * it, i = e - 16;
            if (e < 672) {
                float k_lo = i + 1 >= 0 ? k0[max(i + 1, 0)] + k0[768 + max(i + 1, 0)] : 0.f;
                float k_hi = i >= 0 ? k0[max(i, 0)] + k0[768 + max(i, 0)] : 0.f;
                P[e] = pk(k_lo, k_hi);
            }
        }
    }
    const int w = 16 + r16 - 8 * g;
    u32 sel1[2], sel2[4], sel3[4], msk[4];
    const int b1 = rne_div(w - 7, 1), b2 = rne_div(w - 7, 2), b3 = rne_div(w - 7, 3);
    {
        auto hsel = [](int dl) -> u32 { int by = ((dl ^ 1) * 2); return (u32)(by | ((by + 1) << 8)); };
#pragma unroll
        for (int e = 0; e < 4; ++e) {
            int x0 = w - 2 * e, x1 = x0 - 1;
            sel3[e] = hsel(rne_div(x0, 3) - b3) | (hsel(rne_div(x1, 3) - b3) << 16);
            sel2[e] = hsel(rne_div(x0, 2) - b2) | (hsel(rne_div(x1, 2) - b2) << 16);
            if (e >= 2) sel1[e - 2] = hsel(rne_div(x0, 1) - b1) | (hsel(rne_div(x1, 1) - b1) << 16);
            msk[e] = (x0 < 0 ? 0xffffu : 0u) | (x1 < 0 ? 0xffff0000u : 0u);
        }
    }
#pragma unroll
    for (int i = 0; i < 2; ++i) {
        LAS char* ud = sm + S_UT + 512 + (i * 256 + tid) * 8;
        sts64(ud, __builtin_amdgcn_perm(uu[i].y, uu[i].x, 0x05040100u), __builtin_amdgcn_perm(uu[i].w, uu[i].z, 0x05040100u));
        sts64(ud + 4608, __builtin_amdgcn_perm(uu[i].y, uu[i].x, 0x07060302u), __builtin_amdgcn_perm(uu[i].w, uu[i].z, 0x07060302u));
    }
    wg_barrier();
    STAMP(4);
    f32x4 y[8];
#pragma unroll
    for (int i = 0; i < 8; ++i) y[i] = f32x4{0.f, 0.f, 0.f, 0.f};
    {
        LAS u32* P = (LAS u32*)(sm + S_P + ch * 2688) + 16;
        LAS char* ub = sm + S_UT + ch * 4608 + 480 + r16 * 32 + g * 16;
        auto f0 = [&](int m) { LAS u32* s = P + 32 * m + w - 7; return u32x4{s[6], s[4], s[2], s[0]}; };
        auto f1 = [&](int m) { LAS u32* s = P + 128 + 16 * m + b1; u32 d0 = s[0], d1 = s[2], d2 = s[4];
            return u32x4{__builtin_amdgcn_perm(d2, d1, sel1[0]), __builtin_amdgcn_perm(d2, d1, sel1[1]), __builtin_amdgcn_perm(d1, d0, sel1[0]), __builtin_amdgcn_perm(d1, d0, sel1[1])}; };
        auto f2 = [&](int m) { LAS u32* s = P + 256 + 8 * m + b2; u32 d0 = s[0], d1 = s[2];
            return u32x4{__builtin_amdgcn_perm(d1, d0, sel2[0]), __builtin_amdgcn_perm(d1, d0, sel2[1]), __builtin_amdgcn_perm(d1, d0, sel2[2]), __builtin_amdgcn_perm(d1, d0, sel2[3])}; };
        auto f3 = [&](int m) { LAS u32* s = P + 384 + 4 * m + b3; u32 d0 = s[0];
            return u32x4{__builtin_amdgcn_perm(d0, d0, sel3[0]), __builtin_amdgcn_perm(d0, d0, sel3[1]), __builtin_amdgcn_perm(d0, d0, sel3[2]), __builtin_amdgcn_perm(d0, d0, sel3[3])}; };
        auto mix = [&](u32x4 lo, u32x4 hi) { return u32x4{(lo.x & msk[0]) | (hi.x & ~msk[0]), (lo.y & msk[1]) | (hi.y & ~msk[1]), (lo.z & msk[2]) | (hi.z & ~msk[2]), (lo.w & msk[3]) | (hi.w & ~msk[3])}; };
#pragma unroll
        for (int it = 0; it < 4; ++it) {
            const int rho = 4 * hf + it;
            u32x4 T[8], U[8];
            T[0] = f0(rho); T[1] = f1(rho + 8); T[2] = f2(rho + 16); T[3] = f2(rho + 24);
            T[4] = f3(rho + 32); T[5] = f3(rho + 40); T[6] = f3(rho + 48); T[7] = f3(rho + 56);
            if (it == 0 && hf == 0) { T[1] = mix(f0(8), T[1]); T[2] = mix(f1(16), T[2]); T[4] = mix(f2(32), T[4]); }
#pragma unroll
            for (int bq = 0; bq < 8; ++bq) U[bq] = lds128(ub + 512 * bq - 64 * rho);
#pragma unroll
            for (int aa = 0; aa < 8; ++aa)
#pragma unroll
                for (int bq = 0; bq + aa < 8; ++bq) y[aa + bq] = mfma(T[aa], U[bq], y[aa + bq]);
        }
    }
    STAMP(5);
    {
        LAS char* yd = sm + S_TAB + wave * 8192 + (g * 16 + ((r16 + 4 * g) & 15)) * 16;
#pragma unroll
        for (int i = 0; i < 8; ++i) sts128(yd + i * 1024, __builtin_bit_cast(u32x4, y[i]));
    }
    wg_barrier();
    {
        const float d0 = bflo(dsk), d1 = bfhi(dsk);
#pragma unroll
        for (int i = 0; i < 2; ++i) {
            const int gg = wave + 4 * i;
            LAS char* ys = sm + S_TAB + gg * 1024 + ((lane & 3) * 16 + (((lane >> 2) + 4 * (lane & 3)) & 15)) * 16;
            f32x4 y0 = __builtin_bit_cast(f32x4, lds128(ys)) + __builtin_bit_cast(f32x4, lds128(ys + 8192));
            f32x4 y1 = __builtin_bit_cast(f32x4, lds128(ys + 16384)) + __builtin_bit_cast(f32x4, lds128(ys + 24576));
            u32x4 o = {pk(y0.x + bflo(uu[i].x) * d0, y1.x + bfhi(uu[i].x) * d1), pk(y0.y + bflo(uu[i].y) * d0, y1.y + bfhi(uu[i].y) * d1),
                       pk(y0.z + bflo(uu[i].z) * d0, y1.z + bfhi(uu[i].z) * d1), pk(y0.w + bflo(uu[i].w) * d0, y1.w + bfhi(uu[i].w) * d1)};
            bst_dev(sane(o), rws, tid * 16, WS_YX + (ct * 32 + j) * 8192 + i * 4096);
        }
    }
    STAMP(6);
    if (wave >= 2) return;
    {
        u32x4 v[4];
        for (int sp = 0; sp < SPIN_CAP; ++sp) {
            cbar();
#pragma unroll
            for (int i = 0; i < 4; ++i) v[i] = bld_dev(rws, (8 * i + (lane >> 3)) * 8192 + (lane & 7) * 16, WS_YX + ct * 32 * 8192 + j * 256 + wave * 128);
            cbar();
            u32 m = max(max(umax4(v[0]), umax4(v[1])), max(umax4(v[2]), umax4(v[3])));
            if (__all(m != 0xffffffffu)) break;
            __builtin_amdgcn_s_sleep(2);
        }
        STAMP(7);
#pragma unroll
        for (int i = 0; i < 4; ++i) {
            const int pr = 8 * i + (lane >> 3);
            sts128(sm + S_TR + ((pr & 3) * 8 + (pr >> 2)) * 288 + (wave * 8 + (lane & 7)) * 16, v[i]);
        }
        cbar();
        LAS char* tw2 = sm + S_TR + (lane & 7) * 288 + (wave * 8 + (lane >> 3)) * 16;
        u32x4 t0 = lds128(tw2), t1 = lds128(tw2 + 8 * 288), t2 = lds128(tw2 + 16 * 288), t3 = lds128(tw2 + 24 * 288);
        u32x4* dst = (u32x4*)(a.out + (size_t)(64 * j + 32 * wave + 4 * (lane >> 3)) * 896 + 64 * ct) + (lane & 7);
        dst[0] = u32x4{t0.x, t1.x, t2.x, t3.x};
        dst[112] = u32x4{t0.y, t1.y, t2.y, t3.y};
        dst[224] = u32x4{t0.z, t1.z, t2.z, t3.z};
        dst[336] = u32x4{t0.w, t1.w, t2.w, t3.w};
    }
    STAMP(8);
}

__global__ __launch_bounds__(256, 2) void fused_kernel(Args a) {
    __shared__ __attribute__((aligned(16))) char smem[75840];
    LAS char* sm = (LAS char*)smem;
    if (blockIdx.x < 256) attn_role(a, sm);
    else ssm_role(a, sm);
}

extern "C" void kernel_launch(void* const* d_in, const int* in_sizes, int n_in,
                              void* d_out, int out_size, void* d_ws, size_t ws_size,
                              hipStream_t stream) {
    Args a;
    a.u = (const u16*)d_in[0]; a.logA = (const u16*)d_in[1]; a.Aim = (const u16*)d_in[2];
    a.Cre = (const u16*)d_in[3]; a.Cim = (const u16*)d_in[4]; a.logdt = (const u16*)d_in[5];
    a.Dskip = (const u16*)d_in[6]; a.x = (const u16*)d_in[7]; a.wqkv = (const u16*)d_in[8];
    a.wo = (const u16*)d_in[9]; a.out = (u16*)d_out; a.ws = (char*)d_ws;
    fused_kernel<<<512, 256, 0, stream>>>(a);
}
```

```cpp
#include <hip/hip_runtime.h>
#include <stdint.h>

typedef unsigned short u16;
typedef unsigned int u32;
typedef unsigned long long u64;
typedef __bf16 bf16x8 __attribute__((ext_vector_type(8)));
typedef __bf16 bf16x2 __attribute__((ext_vector_type(2)));
typedef short s16x4 __attribute__((ext_vector_type(4)));
typedef float f32x4 __attribute__((ext_vector_type(4)));
typedef float f32x2 __attribute__((ext_vector_type(2)));
typedef u32 u32x2 __attribute__((ext_vector_type(2)));
typedef u32 u32x4 __attribute__((ext_vector_type(4)));
#define LAS __attribute__((address_space(3)))
#define GAS __attribute__((address_space(1)))
#define DI __device__ __forceinline__
#define RLX_AGENT __ATOMIC_RELAXED, __HIP_MEMORY_SCOPE_AGENT
#define SB() __builtin_amdgcn_sched_barrier(0)
#define WAITV(n) asm volatile("s_waitcnt vmcnt(" #n ")" ::: "memory")
#define KEEP(x) asm volatile("" :: "v"(x))
typedef __amdgpu_buffer_rsrc_t rsrc_t;
#define EXP 0
#define PA0 2
#define PA1 2
#define PS0 0
#define PS1 0
#define SSM_FIRST 1

#define STAMP(i) do {} while (0)
#define STAMP_DECL do {} while (0)
#define STAMP_FLUSH(slot) do {} while (0)

constexpr u32 WS_EPA = 0;
constexpr u32 WS_EPS = 256;
constexpr u32 WS_UC  = 65536;
constexpr u32 WS_YC  = WS_UC + 32768;
constexpr u32 WS_KVP = 131072;
constexpr u32 WS_OX  = WS_KVP + (1u << 20);
constexpr u32 WS_UX  = WS_OX + (2u << 20);
constexpr u32 WS_YX  = WS_UX + (2u << 20);
constexpr u32 WS_END = WS_YX + (2u << 20);
constexpr u32 WS_DBG = 16u << 20;
constexpr int SPIN_CAP = 1 << 16;

struct Args {
    const u16 *u, *logA, *Aim, *Cre, *Cim, *logdt, *Dskip, *x, *wqkv, *wo;
    u16* out;
    char* ws;
};

DI f32x4 mfma(u32x4 a, u32x4 b, f32x4 c) {
    return __builtin_amdgcn_mfma_f32_16x16x32_bf16(__builtin_bit_cast(bf16x8, a), __builtin_bit_cast(bf16x8, b), c, 0, 0, 0);
}
DI u32 pk2(f32x2 v) { return __builtin_bit_cast(u32, __builtin_convertvector(v, bf16x2)); }
DI u32 pk(float lo, float hi) { f32x2 v = {lo, hi}; return pk2(v); }
DI float bflo(u32 v) { return __uint_as_float(v << 16); }
DI float bfhi(u32 v) { return __uint_as_float(v & 0xffff0000u); }
DI float rbf(float f) { return bflo(pk(f, f)); }
DI rsrc_t mkrsrc(const void* p, u32 bytes) { return __builtin_amdgcn_make_buffer_rsrc((void*)p, 0, bytes, 0x00020000); }
DI u32x4 bld_dev(rsrc_t r, u32 voff, u32 soff) { return __builtin_amdgcn_raw_buffer_load_b128(r, voff, soff, 16); }
DI u32 bld32_dev(rsrc_t r, u32 voff, u32 soff) { return __builtin_amdgcn_raw_buffer_load_b32(r, voff, soff, 16); }
DI void bst_dev(u32x4 v, rsrc_t r, u32 voff) { __builtin_amdgcn_raw_buffer_store_b128(v, r, voff, 0, 16); }
DI void bst32_dev(u32 v, rsrc_t r, u32 voff) { __builtin_amdgcn_raw_buffer_store_b32(v, r, voff, 0, 16); }
DI u32x4 mkdesc(const void* p, u32 bytes) { u64 v = (u64)p; return u32x4{(u32)v, (u32)(v >> 32) & 0xffffu, bytes, 0x00020000u}; }
DI void dma(u32x4 desc, LAS char* dst, u32 voff, u32 soff) {
    asm volatile("s_mov_b32 m0, %0\n\ts_nop 0\n\tbuffer_load_dwordx4 %1, %2, %3 offen lds"
                 :: "s"((u32)(uintptr_t)dst), "v"(voff), "s"(desc), "s"(soff) : "memory");
}
DI u32x4 lds128(LAS char* p) { return *(LAS u32x4*)p; }
DI void sts128(LAS char* p, u32x4 v) { *(LAS u32x4*)p = v; }
DI void sts64(LAS char* p, u32 lo, u32 hi) { u32x2 v = {lo, hi}; *(LAS u32x2*)p = v; }
DI u32x4 trfrag(LAS char* p0, LAS char* p1) {
    u32x2 a = __builtin_bit_cast(u32x2, __builtin_amdgcn_ds_read_tr16_b64_v4i16((LAS s16x4*)p0));
    u32x2 b = __builtin_bit_cast(u32x2, __builtin_amdgcn_ds_read_tr16_b64_v4i16((LAS s16x4*)p1));
    u32x4 r = {a.x, a.y, b.x, b.y};
    return r;
}
DI void cbar() { asm volatile("" ::: "memory"); }
DI void wg_barrier() { asm volatile("s_waitcnt lgkmcnt(0)\n\ts_barrier" ::: "memory"); }
DI f32x4 asf(u32x4 v) { return __builtin_bit_cast(f32x4, v); }
DI u32x4 asu(f32x4 v) { return __builtin_bit_cast(u32x4, v); }
DI u32 sum4(u32x4 v) { return v.x + v.y + v.z + v.w; }
DI u32 rowsum(u32 s) {
    s += __builtin_amdgcn_update_dpp(0, s, 0x128, 0xf, 0xf, false);
    s += __builtin_amdgcn_update_dpp(0, s, 0x124, 0xf, 0xf, false);
    s += __builtin_amdgcn_update_dpp(0, s, 0x122, 0xf, 0xf, false);
    s += __builtin_amdgcn_update_dpp(0, s, 0x121, 0xf, 0xf, false);
    return s;
}
DI u32 colsum(u32 s) {
    auto r = __builtin_amdgcn_permlane32_swap(s, s, false, false);
    s = r[0] + r[1];
    auto t = __builtin_amdgcn_permlane16_swap(s, s, false, false);
    return t[0] + t[1];
}
DI float phi(float t) {
    float e = __builtin_amdgcn_exp2f(t * 1.44269504089f);
    return fmaxf(t, 1e-6f) + __builtin_amdgcn_fmed3f(e, 0.f, 1.f);
}
DI u32x4 phi8(f32x4 a, f32x4 b) { return u32x4{pk(phi(a.x), phi(a.y)), pk(phi(a.z), phi(a.w)), pk(phi(b.x), phi(b.y)), pk(phi(b.z), phi(b.w))}; }
DI u32x4 pk8(f32x4 a, f32x4 b) { return u32x4{pk(a.x, a.y), pk(a.z, a.w), pk(b.x, b.y), pk(b.z, b.w)}; }
DI u32 ldepoch(const char* p) {
    u32 z = 0; asm volatile("" : "+v"(z));
    return __hip_atomic_load((GAS u32*)(p + z), RLX_AGENT);
}
template <int N, class OK, class LD>
DI void settle(OK ok, LD ld) {
#pragma unroll 1
    for (int sp = 0; sp < SPIN_CAP; ++sp) {
        bool again = false, bad[N];
#pragma unroll
        for (int i = 0; i < N; ++i) { bad[i] = !__all(ok(i)); again |= bad[i]; }
        if (!again) break;
        cbar();
#pragma unroll
        for (int i = 0; i < N; ++i) if (bad[i]) ld(i);
        cbar();
    }
}

constexpr int A_XR = 32768, A_WQ = A_XR + 16384, A_KVB = 77824;

DI void attn_role(const Args& a, LAS char* sm, const int b) {
    STAMP_DECL;
    const int tid = threadIdx.x, lane = tid & 63, wave = __builtin_amdgcn_readfirstlane(tid >> 6);
    const int xcd = b & 7, j = b >> 3;
    const int h = (xcd & 3) * 4 + (j & 3), rb = (xcd >> 2) * 8 + (j >> 2);
    const int c0 = (rb + 2 * (j & 3)) % 7;
    const int g = lane >> 4, r16 = lane & 15, q = (lane >> 2) & 3, p = lane & 3;
    const u32x4 rx = mkdesc(a.x, 2048 * 896), rw = mkdesc(a.wqkv, 448 * 2688), rwo = mkdesc(a.wo, 448 * 896);
    const rsrc_t rws = mkrsrc(a.ws, WS_END);
    const u32x4 rnull = mkdesc(a.x, 0);
    STAMP(0);
    __builtin_amdgcn_s_setprio(PA0);
    u32 tag = ldepoch(a.ws + WS_EPA);
    cbar();
    u32 wv[2], xv[2];
#pragma unroll
    for (int m = 0; m < 2; ++m) {
        const int ch = EXP == 3 ? (lane & 7) : (lane & 7) ^ ((((lane >> 4) & 1) | (m << 1)) << 1);
        wv[m] = (16 * wave + 8 * m + (lane >> 3)) * 2688 + (1 + (ch >> 2)) * 896 + h * 56 + (ch & 3) * 16;
        xv[m] = (rb * 128 + wave * 32 + 8 * m + (lane >> 3)) * 896 + ((EXP == 3 ? (lane & 7) : ((lane & 7) ^ (4 * m + (lane >> 4)))) * 16);
    }
    auto kchunk = [&](int i) { int kc = i + c0; return kc >= 7 ? kc - 7 : kc; };
    auto ldW = [&](int i) {
        const int kc = kchunk(i);
#pragma unroll
        for (int m = 0; m < 2; ++m) dma(EXP == 1 ? rnull : rw, sm + (i % 3) * 8192 + wave * 2048 + m * 1024, wv[m], kc * 64 * 2688);
    };
    auto ldX1 = [&](int i, int m) {
        dma(EXP == 2 ? rnull : rx, sm + A_XR + (i % 3) * 16384 + wave * 4096 + m * 1024, xv[m & 1], kchunk(i) * 128 + (m >> 1) * 16 * 896);
    };
    auto ldX = [&](int i) {
#pragma unroll
        for (int m = 0; m < 4; ++m) ldX1(i, m);
    };
    const u32 qv = (lane >> 2) * 2688 + h * 56 + (((lane & 3) ^ ((lane >> 4) & 2)) * 16);
    auto ldQ1 = [&](int part, int n) {
        int kc = 4 * part + wave + c0; kc = kc >= 7 ? kc - 7 : kc;
        dma(EXP == 1 || EXP == 4 ? rnull : rw, sm + A_WQ + part * 16384 + wave * 4096 + n * 1024, qv, (kc * 64 + n * 16) * 2688);
    };
#pragma unroll
    for (int i = 0; i < 3; ++i) { ldW(i); ldX(i); }
    cbar();

    LAS char* xsrc0 = sm + A_XR + wave * 4096 + r16 * 128 + ((g ^ (r16 >> 1)) * 16);
    LAS char* xsrc1 = sm + A_XR + wave * 4096 + r16 * 128 + (((4 + g) ^ (r16 >> 1)) * 16);
    const int ft = (q >> 1) | ((g & 1) << 1);
    LAS char* wsrc[4];
#pragma unroll
    for (int ct = 0; ct < 4; ++ct) wsrc[ct] = sm + (8 * g + q) * 128 + ((ct ^ ft) * 32) + 8 * p;

    const bool p3 = j < 28;
    const int r64 = p3 ? xcd * 4 + j / 7 : 0, nb = j % 7;
    f32x4 acc[2][4];
#pragma unroll
    for (int st = 0; st < 2; ++st)
#pragma unroll
        for (int ct = 0; ct < 4; ++ct) acc[st][ct] = f32x4{0.f, 0.f, 0.f, 0.f};
    u32x4 xf[14][2];
#pragma unroll
    for (int i = 0; i < 7; ++i) {
        if (i == 0) WAITV(12); else if (i < 6) WAITV(10); else WAITV(4);
        wg_barrier();
        if (i == 0) STAMP(1);
        u32x4 wf[2][4];
#pragma unroll
        for (int st = 0; st < 2; ++st) {
            xf[2 * i][st] = lds128(xsrc0 + (i % 3) * 16384 + st * 2048);
            xf[2 * i + 1][st] = lds128(xsrc1 + (i % 3) * 16384 + st * 2048);
        }
#pragma unroll
        for (int ct = 0; ct < 4; ++ct) { LAS char* s = wsrc[ct] + (i % 3) * 8192; wf[0][ct] = trfrag(s, s + 512); }
#pragma unroll
        for (int ct = 0; ct < 4; ++ct) { LAS char* s = wsrc[ct] + (i % 3) * 8192 + 4096; wf[1][ct] = trfrag(s, s + 512); }
        SB();
        if (i >= 1 && i + 2 < 7) ldW(i + 2);
        asm volatile("s_waitcnt lgkmcnt(14)" ::: "memory");
#pragma unroll
        for (int m = 0; m < 4; ++m) {
            if (i + 3 < 7) ldX1(i + 3, m);
            if (i == 4) ldQ1(0, m);
            if (i == 5 && wave < 3) ldQ1(1, m);
            SB();
#pragma unroll
            for (int ct = 2 * (m & 1); ct < 2 * (m & 1) + 2; ++ct)
#pragma unroll
                for (int st = 0; st < 2; ++st) acc[st][ct] = mfma(xf[2 * i + (m >> 1)][st], wf[m >> 1][ct], acc[st][ct]);
            SB();
        }
    }
    STAMP(2);
    __builtin_amdgcn_s_setprio(PA1);
    {
        u32x4 pkf[2], vf[2];
#pragma unroll
        for (int t = 0; t < 2; ++t) { pkf[t] = phi8(acc[0][t], acc[1][t]); vf[t] = pk8(acc[0][2 + t], acc[1][2 + t]); }
        if (r16 == 12) vf[1] = u32x4{0x3f803f80u, 0x3f803f80u, 0x3f803f80u, 0x3f803f80u};
#pragma unroll
        for (int dt = 0; dt < 2; ++dt)
#pragma unroll
            for (int et = 0; et < 2; ++et)
                sts128(sm + A_XR + wave * 4096 + (dt * 2 + et) * 1024 + lane * 16, asu(mfma(pkf[dt], vf[et], f32x4{0.f, 0.f, 0.f, 0.f})));
    }
    WAITV(0);
    asm volatile("" : "+v"(tag));
    tag += 1;
    wg_barrier();
    const u32x4 tb = {tag & 255u, ((tag >> 8) + tag) & 255u, ((tag >> 16) + tag) & 255u, ((tag >> 24) + tag) & 255u};
    {
        f32x4 s0 = asf(lds128(sm + A_XR + tid * 16)), s1 = asf(lds128(sm + A_XR + 4096 + tid * 16));
        f32x4 s2 = asf(lds128(sm + A_XR + 8192 + tid * 16)), s3 = asf(lds128(sm + A_XR + 12288 + tid * 16));
        bst_dev((asu((s0 + s1) + (s2 + s3)) & 0xffffff00u) | tb, rws, WS_KVP + (h * 16 + rb) * 4096 + tid * 16);
    }
    cbar();
    STAMP(3);
    const u32 ov = (lane >> 3) * 896 + nb * 128 + (((lane & 7) ^ ((lane >> 4) << 1)) * 16);
    auto ldO = [&](int n0, int n1) {
#pragma unroll
        for (int n = n0; n < n1; ++n) dma(rwo, sm + (n * 4 + wave) * 1024, ov, (n * 4 + wave) * 8 * 896);
    };
    u32x4 gk[16];
    auto ldk = [&](int i) { gk[i] = bld_dev(rws, tid * 16, WS_KVP + (h * 16 + ((rb + i) & 15)) * 4096); };
    u32x4 pq[2];
    {
        f32x4 aq[2][2];
#pragma unroll
        for (int st = 0; st < 2; ++st)
#pragma unroll
            for (int dt = 0; dt < 2; ++dt) aq[st][dt] = f32x4{0.f, 0.f, 0.f, 0.f};
        LAS char* qsrc[2];
#pragma unroll
        for (int dt = 0; dt < 2; ++dt) qsrc[dt] = sm + A_WQ + (8 * g + q) * 64 + (((dt ^ g) & 1) * 32) + 8 * p;
        u32x4 wq[2][2][2];
        auto rdq = [&](int i) {
#pragma unroll
            for (int ks = 0; ks < 2; ++ks)
#pragma unroll
                for (int dt = 0; dt < 2; ++dt) { LAS char* s = qsrc[dt] + (2 * i + ks) * 2048; wq[i & 1][ks][dt] = trfrag(s, s + 256); }
        };
        rdq(0);
#pragma unroll
        for (int i = 0; i < 7; ++i) {
            if (i + 1 < 7) rdq(i + 1);
            SB();
#pragma unroll
            for (int ks = 0; ks < 2; ++ks)
#pragma unroll
                for (int dt = 0; dt < 2; ++dt)
#pragma unroll
                    for (int st = 0; st < 2; ++st) aq[st][dt] = mfma(wq[i & 1][ks][dt], xf[2 * i + ks][st], aq[st][dt]);
            SB();
            if (p3) ldO(i == 0 ? 0 : i + 1, i + 2);
            SB();
        }
        STAMP(4);
#pragma unroll
        for (int k = 0; k < 16; ++k) ldk(k);
        cbar();
        SB();
#pragma unroll
        for (int st = 0; st < 2; ++st) pq[st] = phi8(aq[st][0], aq[st][1]);
    }
    SB();
    {
        u32x4 m = gk[0] ^ tb;
#pragma unroll
        for (int i = 1; i < 16; ++i) m |= gk[i] ^ tb;
        if (!__all(((m.x | m.y | m.z | m.w) & 255u) == 0))
            settle<16>([&](int i) { u32x4 t = gk[i] ^ tb; return ((t.x | t.y | t.z | t.w) & 255u) == 0; }, ldk);
    }
    STAMP(5);
    {
        f32x4 s[8];
#pragma unroll
        for (int i = 0; i < 8; ++i) s[i] = asf(gk[i] ^ tb) + asf(gk[i + 8] ^ tb);
#pragma unroll
        for (int i = 0; i < 4; ++i) s[i] += s[i + 4];
        f32x4 t = (s[0] + s[2]) + (s[1] + s[3]);
        u32 lo = pk(t.x, t.y), hi = pk(t.z, t.w);
        const int dt = wave >> 1, et = wave & 1;
        if (dt == 1 && g == 3) { lo = 0; hi = 0; }
        sts64(sm + A_KVB + (et * 64 + lane) * 16 + dt * 8, lo, hi);
    }
    wg_barrier();
    STAMP(6);
    {
        u32x4 kf0 = lds128(sm + A_KVB + lane * 16), kf1 = lds128(sm + A_KVB + 1024 + lane * 16);
        u32x4 zf = lds128(sm + A_KVB + 1024 + (12 + 16 * g) * 16);
#pragma unroll
        for (int st = 0; st < 2; ++st) {
            const f32x4 z4 = {0.f, 0.f, 0.f, 0.f};
            f32x4 dn = mfma(zf, pq[st], z4), n0 = mfma(kf0, pq[st], z4), n1 = mfma(kf1, pq[st], z4);
            float rd = __builtin_amdgcn_rcpf(dn.x + 1e-6f);
            u32x4 of = pk8(n0 * rd, n1 * rd);
            if (g == 3) { of.z = 0; of.w = 0; }
            const u32 cs = tag - colsum(sum4(of));
            if (g == 3) of.w = cs;
            bst_dev(of, rws, WS_OX + ((rb * 8 + wave * 2 + st) * 16 + h) * 1024 + lane * 16);
        }
    }
    cbar();
    STAMP(7);
    if (p3) {
        ldO(8, 14);
        WAITV(0);
        wg_barrier();
        u32x4 go[16];
        auto ldo = [&](int i) { go[i] = bld_dev(rws, lane * 16, WS_OX + ((r64 * 4 + wave) * 16 + i) * 1024); };
#pragma unroll
        for (int i = 0; i < 16; ++i) ldo(i);
        cbar();
        LAS char *os0[4], *os1[4];
#pragma unroll
        for (int t = 0; t < 4; ++t) {
            os0[t] = sm + (4 * g + q) * 128 + (((t ^ (2 * g + (q >> 1))) & 3) * 32) + 8 * p;
            os1[t] = os0[t] + (g == 3 ? 0 : 2048);
        }
        constexpr int PRE = 6;
        u32x4 wf[16][4];
        auto rdo = [&](int hh) {
#pragma unroll
            for (int t = 0; t < 4; ++t) { const int tt = t ^ ((hh & 1) * 2); wf[hh][t] = trfrag(os0[tt] + hh * 28 * 128, os1[tt] + hh * 28 * 128); }
        };
#pragma unroll
        for (int hh = 0; hh < PRE; ++hh) rdo(hh);
        SB();
        {
            u32 s = 0;
#pragma unroll
            for (int i = 0; i < 16; ++i) s += sum4(go[i]);
            if (!__all(colsum(s) == tag * 16u))
                settle<16>([&](int i) { return colsum(sum4(go[i])) == tag; }, ldo);
        }
        STAMP(8);
        const u32 wmask = g == 3 ? 0u : ~0u;
        f32x4 oa[4];
#pragma unroll
        for (int t = 0; t < 4; ++t) oa[t] = f32x4{0.f, 0.f, 0.f, 0.f};
#pragma unroll
        for (int hh = 0; hh < 16; ++hh) go[hh].w &= wmask;
#pragma unroll
        for (int hh = 0; hh < 16; ++hh) {
#pragma unroll
            for (int t = 0; t < 4; ++t) oa[t] = mfma(wf[hh][t], go[hh], oa[t]);
            SB();
            if (hh + PRE < 16) rdo(hh + PRE);
            SB();
        }
        u32 d[4][2];
#pragma unroll
        for (int t = 0; t < 4; ++t) { d[t][0] = pk(oa[t].x, oa[t].y); d[t][1] = pk(oa[t].z, oa[t].w); }
#pragma unroll
        for (int c = 0; c < 2; ++c) {
#pragma unroll
            for (int t = 0; t < 2; ++t) { auto r = __builtin_amdgcn_permlane32_swap(d[t][c], d[t + 2][c], false, false); d[t][c] = r[0]; d[t + 2][c] = r[1]; }
#pragma unroll
            for (int t = 0; t < 4; t += 2) { auto r = __builtin_amdgcn_permlane16_swap(d[t][c], d[t + 1][c], false, false); d[t][c] = r[0]; d[t + 1][c] = r[1]; }
        }
        u32x4* dst = (u32x4*)(a.out + (size_t)(r64 * 64 + wave * 16 + r16) * 896 + 448 + nb * 64 + 16 * g);
        dst[0] = u32x4{d[0][0], d[0][1], d[1][0], d[1][1]};
        dst[1] = u32x4{d[2][0], d[2][1], d[3][0], d[3][1]};
        if (b == 0 && tid == 0) __hip_atomic_store((GAS u32*)(a.ws + WS_EPA), tag, RLX_AGENT);
        STAMP(9);
    }
    STAMP_FLUSH(b);
}

constexpr int S_TAB = 0;
constexpr int S_KC = 34816;
constexpr int S_P = 47104;
constexpr int S_UT = 52480;
constexpr int S_TR = 61696;
constexpr int S_PF = 70912;
constexpr u32 CODE_BYTES = 24576;

struct c2 { f32x2 r, i; };
DI c2 cmul(c2 a, c2 b) { return c2{a.r * b.r - a.i * b.i, a.r * b.i + a.i * b.r}; }
DI c2 cexp2(f32x2 lr, f32x2 rev, float pw) {
    f32x2 e = lr * pw, f = rev * pw;
    f32x2 m = {__builtin_amdgcn_exp2f(e.x), __builtin_amdgcn_exp2f(e.y)};
    f.x = __builtin_amdgcn_fractf(f.x); f.y = __builtin_amdgcn_fractf(f.y);
    f32x2 c = {__builtin_amdgcn_cosf(f.x), __builtin_amdgcn_cosf(f.y)}, s = {__builtin_amdgcn_sinf(f.x), __builtin_amdgcn_sinf(f.y)};
    return c2{m * c, m * s};
}
DI f32x2 unpk(u32 v) { return f32x2{bflo(v), bfhi(v)}; }
DI int rne_div(int x, int s) {
    int y = x + 64;
    return ((y + (1 << (s - 1)) - 1 + ((y >> s) & 1)) >> s) - (64 >> s);
}

DI void ssm_role(const Args& a, LAS char* sm, const int bb, const char* code) {
    STAMP_DECL;
    const int tid = threadIdx.x, lane = tid & 63, wave = __builtin_amdgcn_readfirstlane(tid >> 6);
    const int ct = bb & 7, j = bb >> 3;
    const int ch = wave >> 1, hf = wave & 1, d = ct * 64 + 2 * j + ch;
    const int g = lane >> 4, r16 = lane & 15;
    STAMP(0);
    if (false) {
        const u32x4 rc = mkdesc(code, CODE_BYTES);
        asm volatile("s_mov_b32 m0, %0\n\ts_nop 0\n\tbuffer_load_dword %1, %2, 0 offen lds" :: "s"((u32)(uintptr_t)(sm + S_PF + wave * 256)), "v"(tid * 128), "s"(rc) : "memory");
    }
    if (ct == 7) return;
    __builtin_amdgcn_s_setprio(PS0);
    const rsrc_t rws = mkrsrc(a.ws, WS_END);
    const int trow = 256 * (j >> 2) + 16 * (tid >> 4) + 4 * (j & 3) + 2 * ((tid >> 3) & 1);
    const u32x4* usrc = (const u32x4*)(a.u + (size_t)trow * 448 + 64 * ct) + (tid & 7);
    const u32x4 ua = usrc[0], ub = usrc[56];
    u32 tag = ldepoch(a.ws + WS_EPS + ct * 256);
    const int po = d * 128 + hf * 64 + lane;
    const u32 ldt = a.logdt[d];
    const u32 pA = ((const u32*)a.logA)[po], pI = ((const u32*)a.Aim)[po], pCr = ((const u32*)a.Cre)[po], pCi = ((const u32*)a.Cim)[po];
    const u32 dsk = ((const u32*)a.Dskip)[ct * 32 + j];
    cbar();
    if (tid < 64) sts128(sm + S_UT + (tid >> 5) * 4608 + (tid & 31) * 16, u32x4{0, 0, 0, 0});
    const u32 pofs = (ct * 32 + j) * 8192 + tid * 16;
    const u32 cofs = (ct * 32 + (tid >> 4)) * 8192 + j * 256 + (tid & 15) * 16;
    const u32 pcs = (ct * 32 + j) * 128 + (tid >> 4) * 8 + (tid & 1) * 4;
    const u32 ccs = (ct * 32 + (tid >> 4)) * 128 + (j & 15) * 8 + (j >> 4) * 4;
    auto publish = [&](u32x4 v0, u32x4 v1, u32 xbase, u32 cbase) {
        bst_dev(v0, rws, xbase + pofs);
        bst_dev(v1, rws, xbase + pofs + 4096);
        const u32 s0 = rowsum(sum4(v0)), s1 = rowsum(sum4(v1));
        if (r16 < 2) bst32_dev(tag - (r16 ? s1 : s0), rws, cbase + pcs);
    };
    const int w = 16 + r16 - 8 * g;
    u32 sel1[2], sel2[4], sel3[4], msk[4];
    const int b1 = rne_div(w - 7, 1), b2 = rne_div(w - 7, 2), b3 = rne_div(w - 7, 3);
    {
        auto hsel = [](int dl) -> u32 { int by = ((dl ^ 1) * 2); return (u32)(by | ((by + 1) << 8)); };
#pragma unroll
        for (int e = 0; e < 4; ++e) {
            int x0 = w - 2 * e, x1 = x0 - 1;
            sel3[e] = hsel(rne_div(x0, 3) - b3) | (hsel(rne_div(x1, 3) - b3) << 16);
            sel2[e] = hsel(rne_div(x0, 2) - b2) | (hsel(rne_div(x1, 2) - b2) << 16);
            if (e >= 2) sel1[e - 2] = hsel(rne_div(x0, 1) - b1) | (hsel(rne_div(x1, 1) - b1) << 16);
            msk[e] = hf ? 0u : (x0 < 0 ? 0xffffu : 0u) | (x1 < 0 ? 0xffff0000u : 0u);
        }
        asm volatile("" : "+v"(sel1[0]), "+v"(sel1[1]), "+v"(sel2[0]), "+v"(sel2[1]), "+v"(sel2[2]), "+v"(sel2[3]), "+v"(sel3[0]), "+v"(sel3[1]), "+v"(sel3[2]), "+v"(sel3[3]));
        asm volatile("" : "+v"(msk[0]), "+v"(msk[1]), "+v"(msk[2]), "+v"(msk[3]));
    }
    WAITV(0);
    asm volatile("" : "+v"(tag));
    tag += 1;
    LAS char* trw = sm + S_TR + (tid & 7) * 288 + (tid >> 3) * 8;
    LAS char* trr = sm + S_TR + (((tid >> 4) & 3) * 8 + (tid >> 6)) * 288 + (tid & 15) * 16;
    sts64(trw, ua.x, ub.x); sts64(trw + 8 * 288, ua.y, ub.y); sts64(trw + 16 * 288, ua.z, ub.z); sts64(trw + 24 * 288, ua.w, ub.w);
    wg_barrier();
    publish(lds128(trr), lds128(trr + 4 * 288), WS_UX, WS_UC);
    cbar();
    STAMP(1);
    LAS char* tab = sm + S_TAB + wave * 8704;
    LAS char* tw = tab + lane * 8;
    LAS char* tr = tab + r16 * 544 + g * 16;
    const float L2E = 1.44269504089f;
    const float dt = rbf(__builtin_amdgcn_exp2f(bflo(ldt) * L2E));
    f32x2 lr = unpk(pA) * L2E;
    lr = unpk(pk(__builtin_amdgcn_exp2f(lr.x), __builtin_amdgcn_exp2f(lr.y))) * dt * -L2E;
    const f32x2 rev = unpk(pI) * dt * 0.15915494309f;
    c2 G = {unpk(pCr) * (dt * 2.f), unpk(pCi) * (dt * 2.f)};
    u32x2 em[16];
#define EMIT(row, v) do { em[row] = u32x2{pk2(v.r), pk2(v.i)}; if ((row) & 1) { if ((row) > 1) { KEEP(em[(row) - 3]); KEEP(em[(row) - 2]); } \
        *(LAS u32x2*)(tw + ((row) - 1) * 544) = em[(row) - 1]; *(LAS u32x2*)(tw + (row) * 544) = em[row]; } } while (0)
    u32x4 ff[8], gf[8];
    {
        c2 e8 = cexp2(lr, -rev, 8.f), F = e8;
        em[0] = u32x2{0x3f803f80u, 0u};
        EMIT(1, F);
#pragma unroll
        for (int i = 2; i < 16; ++i) { F = cmul(F, e8); EMIT(i, F); }
        cbar();
#pragma unroll
        for (int ks = 0; ks < 8; ++ks) ff[ks] = lds128(tr + ks * 64);
        cbar();
    }
    c2 w1 = cexp2(lr, rev, 1.f), Hh = cexp2(lr, rev, 128.f), T;
    LAS char* kc = sm + S_KC + wave * 3072;
    f32x4 ka;
#define KRD() do { cbar(); _Pragma("unroll") for (int ks = 0; ks < 8; ++ks) gf[ks] = lds128(tr + ks * 64); cbar(); } while (0)
#define KMM() do { ka = f32x4{0.f, 0.f, 0.f, 0.f}; _Pragma("unroll") for (int ks = 0; ks < 8; ++ks) ka = mfma(gf[ks], ff[ks], ka); } while (0)
#pragma unroll
    for (int i = 0; i < 2; ++i) {
        EMIT(8 * i, G);
        T = G;
#pragma unroll
        for (int r = 1; r < 8; ++r) { T = cmul(T, w1); EMIT(8 * i + r, T); }
        G = cmul(G, Hh);
    }
    KRD();
    w1 = cmul(w1, w1);
#pragma unroll
    for (int i = 0; i < 2; ++i) {
        EMIT(4 * i, G);
        T = G;
#pragma unroll
        for (int r = 1; r < 4; ++r) { T = cmul(T, w1); EMIT(4 * i + r, T); }
        G = cmul(G, Hh);
        if (i == 0) { SB(); KMM(); SB(); }
    }
    sts128(kc + (128 * (g >> 1) + 4 * (g & 1) + 8 * r16) * 4, asu(ka));
    w1 = cmul(w1, w1);
#pragma unroll
    for (int i = 0; i < 4; ++i) {
        EMIT(8 + 2 * i, G);
        T = cmul(G, w1); EMIT(9 + 2 * i, T);
        G = cmul(G, Hh);
    }
    KRD();
    STAMP(2);
    u32x4 uu[2]; u32 uc[2];
    auto ldu = [&](int i) { uu[i] = bld_dev(rws, cofs, WS_UX + i * 16 * 8192); uc[i] = bld32_dev(rws, ccs, WS_UC + i * 16 * 128); };
    ldu(0); ldu(1);
    cbar();
#pragma unroll
    for (int i = 0; i < 8; ++i) {
        EMIT(i, G);
        G = cmul(G, Hh);
        if (i == 3) { SB(); KMM(); SB(); }
    }
    sts64(tw + 8 * 544, pk2(G.r), pk2(G.i));
    {
        LAS char* k0 = kc + (g < 2 ? 256 + 64 * g + 4 * r16 : 384 + 64 * (g - 2) + 2 * r16) * 4;
        u32x4 kb = asu(ka);
        sts64(k0, kb.x, kb.y);
        sts64(k0 + (g < 2 ? 8 : 128), kb.z, kb.w);
    }
    KRD();
    KMM();
    {
        LAS float* k0 = (LAS float*)(kc + (512 + 64 * g + r16) * 4);
        k0[0] = ka.x; k0[16] = ka.y; k0[32] = ka.z; k0[48] = ka.w;
    }
    wg_barrier();
    STAMP(3);
    {
        LAS char* k0 = sm + S_KC + ch * 6144;
        LAS char* R = sm + S_P + ch * 2688;
        const int t = tid & 127;
#pragma unroll
        for (int it = 0; it < 2; ++it) {
            const int e = t + 128 * it;
            if (e < 164) {
                u32x4 ka0 = lds128(k0 + e * 16), kb0 = lds128(k0 + 3072 + e * 16);
                float kn = *(LAS float*)(k0 + e * 16 + 16) + *(LAS float*)(k0 + 3072 + e * 16 + 16);
                float k_0 = asf(ka0).x + asf(kb0).x, k_1 = asf(ka0).y + asf(kb0).y, k_2 = asf(ka0).z + asf(kb0).z, k_3 = asf(ka0).w + asf(kb0).w;
                asm volatile("" : "+v"(k_0), "+v"(k_1), "+v"(k_2), "+v"(k_3));
                sts128(R + (163 - e) * 16, u32x4{pk(kn, k_3), pk(k_3, k_2), pk(k_2, k_1), pk(k_1, k_0)});
                if (e == 0) sts128(R + 164 * 16, u32x4{pk(k_0, 0.f), 0, 0, 0});
            } else if (e < 167) sts128(R + (e + 1) * 16, u32x4{0, 0, 0, 0});
        }
    }
    if (!__all(rowsum(sum4(uu[0]) + sum4(uu[1])) + uc[0] + uc[1] == 2u * tag))
        settle<2>([&](int i) { return rowsum(sum4(uu[i])) + uc[i] == tag; }, ldu);
#pragma unroll
    for (int i = 0; i < 2; ++i) {
        LAS char* ud = sm + S_UT + 512 + (4 * i + wave) * 512 + r16 * 32 + g * 8;
        sts64(ud, __builtin_amdgcn_perm(uu[i].y, uu[i].x, 0x05040100u), __builtin_amdgcn_perm(uu[i].w, uu[i].z, 0x05040100u));
        sts64(ud + 4608, __builtin_amdgcn_perm(uu[i].y, uu[i].x, 0x07060302u), __builtin_amdgcn_perm(uu[i].w, uu[i].z, 0x07060302u));
    }
    wg_barrier();
    if (j == 0 && tid == 0) __hip_atomic_store((GAS u32*)(a.ws + WS_EPS + ct * 256), tag, RLX_AGENT);
    STAMP(4);
    __builtin_amdgcn_s_setprio(PS1);
    f32x4 y[8];
#pragma unroll
    for (int i = 0; i < 8; ++i) y[i] = f32x4{0.f, 0.f, 0.f, 0.f};
    {
        LAS u32* R = (LAS u32*)(sm + S_P + ch * 2688) + 655;
        LAS u32 *P0 = R - (128 * hf + w - 7) - 262, *P1 = R - (256 + 64 * hf + b1) - 132, *P2 = R - (384 + 32 * hf + b2) - 130, *P3 = R - (512 + 16 * hf + b3) - 108;
#define Q0(i) P0[262 - (i)]
#define Q1(i) P1[132 - (i)]
#define Q2(i) P2[130 - (i)]
#define Q3(i) P3[108 - (i)]
        LAS char* ubase = sm + S_UT + ch * 4608 + 480 + r16 * 32 + g * 16 - 256 * hf - 192;
#define PERM __builtin_amdgcn_perm
        auto f1 = [&](u32 d0, u32 d1, u32 d2) { return u32x4{PERM(d2, d1, sel1[0]), PERM(d2, d1, sel1[1]), PERM(d1, d0, sel1[0]), PERM(d1, d0, sel1[1])}; };
        auto f2 = [&](u32 d0, u32 d1) { return u32x4{PERM(d1, d0, sel2[0]), PERM(d1, d0, sel2[1]), PERM(d1, d0, sel2[2]), PERM(d1, d0, sel2[3])}; };
        auto f3 = [&](u32 d0) { return u32x4{PERM(d0, d0, sel3[0]), PERM(d0, d0, sel3[1]), PERM(d0, d0, sel3[2]), PERM(d0, d0, sel3[3])}; };
        auto mix = [&](u32x4 lo, u32x4 hi) { return u32x4{(lo.x & msk[0]) | (hi.x & ~msk[0]), (lo.y & msk[1]) | (hi.y & ~msk[1]), (lo.z & msk[2]) | (hi.z & ~msk[2]), (lo.w & msk[3]) | (hi.w & ~msk[3])}; };
        u32x4 T[2][8], U[2][8];
        auto prep = [&](int it) {
            u32x4* t = T[it & 1];
            t[0] = u32x4{Q0(32 * it + 6), Q0(32 * it + 4), Q0(32 * it + 2), Q0(32 * it)};
#pragma unroll
            for (int bq = 0; bq < 8; ++bq) U[it & 1][bq] = lds128(ubase + 512 * bq + 192 - 64 * it);
            t[1] = f1(Q1(16 * it), Q1(16 * it + 2), Q1(16 * it + 4));
            t[2] = f2(Q2(8 * it), Q2(8 * it + 2)); t[3] = f2(Q2(8 * it + 64), Q2(8 * it + 66));
            t[4] = f3(Q3(4 * it)); t[5] = f3(Q3(4 * it + 32)); t[6] = f3(Q3(4 * it + 64)); t[7] = f3(Q3(4 * it + 96));
            if (it == 0) {
                t[1] = mix(u32x4{Q0(262), Q0(260), Q0(258), Q0(256)}, t[1]);
                t[2] = mix(f1(Q1(128), Q1(130), Q1(132)), t[2]);
                t[4] = mix(f2(Q2(128), Q2(130)), t[4]);
            }
        };
        prep(3);
#pragma unroll
        for (int it = 3; it >= 0; --it) {
            if (it < 3) SB();
            if (it > 0) prep(it - 1);
#pragma unroll
            for (int aa = 0; aa < 8; ++aa)
#pragma unroll
                for (int bq = 0; bq + aa < 8; ++bq) y[aa + bq] = mfma(T[it & 1][aa], U[it & 1][bq], y[aa + bq]);
            if (it > 0 && it < 3) {
                __builtin_amdgcn_sched_group_barrier(0x100, 22, 0);
                __builtin_amdgcn_sched_group_barrier(0x008, 10, 0);
#pragma unroll
                for (int k = 0; k < 26; ++k) { __builtin_amdgcn_sched_group_barrier(0x002, 2, 0); __builtin_amdgcn_sched_group_barrier(0x008, 1, 0); }
            } else if (it == 3) {
                __builtin_amdgcn_sched_group_barrier(0x100, 32, 0);
#pragma unroll
                for (int k = 0; k < 36; ++k) { __builtin_amdgcn_sched_group_barrier(0x008, 1, 0); __builtin_amdgcn_sched_group_barrier(0x002, 2, 0); }
            }
        }
    }
    SB();
    STAMP(5);
    {
        LAS char* yd = sm + S_TAB + wave * 8192 + lane * 16;
#pragma unroll
        for (int i = 0; i < 8; ++i) sts128(yd + i * 1024, asu(y[i]));
    }
    wg_barrier();
    {
        const float d0 = bflo(dsk), d1 = bfhi(dsk);
        u32x4 o[2];
#pragma unroll
        for (int i = 0; i < 2; ++i) {
            LAS char* ys = sm + S_TAB + (wave + 4 * i) * 1024 + lane * 16;
            f32x4 y0 = asf(lds128(ys)) + asf(lds128(ys + 8192));
            f32x4 y1 = asf(lds128(ys + 16384)) + asf(lds128(ys + 24576));
            o[i] = u32x4{pk(y0.x + bflo(uu[i].x) * d0, y1.x + bfhi(uu[i].x) * d1), pk(y0.y + bflo(uu[i].y) * d0, y1.y + bfhi(uu[i].y) * d1),
                         pk(y0.z + bflo(uu[i].z) * d0, y1.z + bfhi(uu[i].z) * d1), pk(y0.w + bflo(uu[i].w) * d0, y1.w + bfhi(uu[i].w) * d1)};
        }
        publish(o[0], o[1], WS_YX, WS_YC);
    }
    WAITV(0);
    STAMP(6);
    {
        u32x4 v[2]; u32 vc[2];
        auto ldy = [&](int i) { v[i] = bld_dev(rws, cofs, WS_YX + i * 16 * 8192); vc[i] = bld32_dev(rws, ccs, WS_YC + i * 16 * 128); };
        ldy(0); ldy(1);
        cbar();
        settle<2>([&](int i) { return rowsum(sum4(v[i])) + vc[i] == tag; }, ldy);
        STAMP(7);
        sts128(trr, v[0]); sts128(trr + 4 * 288, v[1]);
        wg_barrier();
        u32x2 t0 = *(LAS u32x2*)trw, t1 = *(LAS u32x2*)(trw + 8 * 288), t2 = *(LAS u32x2*)(trw + 16 * 288), t3 = *(LAS u32x2*)(trw + 24 * 288);
        u32x4* dst = (u32x4*)(a.out + (size_t)trow * 896 + 64 * ct) + (tid & 7);
        dst[0] = u32x4{t0.x, t1.x, t2.x, t3.x};
        dst[112] = u32x4{t0.y, t1.y, t2.y, t3.y};
    }
    STAMP(8);
    STAMP_FLUSH(256 + bb);
}

#if defined(__HIP_DEVICE_COMPILE__)
#define NOPK __attribute__((target("no-packed-fp32-ops")))
#else
#define NOPK
#endif
__global__ NOPK __launch_bounds__(256, 2) void fused_kernel(Args a) {
    __shared__ __attribute__((aligned(16))) char smem[81920];
    LAS char* sm = (LAS char*)smem;
    const char* code = (const char*)(__builtin_amdgcn_s_getpc() & ~(u64)255);
    const bool is_attn = SSM_FIRST ? blockIdx.x >= 256 : blockIdx.x < 256;
    const int b = blockIdx.x & 255;
    if (is_attn) attn_role(a, sm, b);
    else ssm_role(a, sm, b, code);
}

extern "C" void kernel_launch(void* const* d_in, const int* in_sizes, int n_in,
                              void* d_out, int out_size, void* d_ws, size_t ws_size,
                              hipStream_t stream) {
    Args a;
    a.u = (const u16*)d_in[0]; a.logA = (const u16*)d_in[1]; a.Aim = (const u16*)d_in[2];
    a.Cre = (const u16*)d_in[3]; a.Cim = (const u16*)d_in[4]; a.logdt = (const u16*)d_in[5];
    a.Dskip = (const u16*)d_in[6]; a.x = (const u16*)d_in[7]; a.wqkv = (const u16*)d_in[8];
    a.wo = (const u16*)d_in[9]; a.out = (u16*)d_out; a.ws = (char*)d_ws;
    fused_kernel<<<512, 256, 0, stream>>>(a);
}
```

```cpp
#include <hip/hip_runtime.h>
#include <stdint.h>

typedef unsigned short u16;
typedef unsigned int u32;
typedef unsigned long long u64;
typedef __bf16 bf16x8 __attribute__((ext_vector_type(8)));
typedef __bf16 bf16x2 __attribute__((ext_vector_type(2)));
typedef short s16x4 __attribute__((ext_vector_type(4)));
typedef float f32x4 __attribute__((ext_vector_type(4)));
typedef float f32x2 __attribute__((ext_vector_type(2)));
typedef u32 u32x2 __attribute__((ext_vector_type(2)));
typedef u32 u32x4 __attribute__((ext_vector_type(4)));
#define LAS __attribute__((address_space(3)))
#define GAS __attribute__((address_space(1)))
#define DI __device__ __forceinline__
#define RLX_AGENT __ATOMIC_RELAXED, __HIP_MEMORY_SCOPE_AGENT
#define SB() __builtin_amdgcn_sched_barrier(0)
#define WAITV(n) asm volatile("s_waitcnt vmcnt(" #n ")" ::: "memory")
#define KEEP(x) asm volatile("" :: "v"(x))
typedef __amdgpu_buffer_rsrc_t rsrc_t;
#define EXP 0
#define PA0 2
#define PA1 2
#define PS0 0
#define PS1 0
#define SSM_FIRST 1

#define STAMP(i) do {} while (0)
#define STAMP_DECL do {} while (0)
#define STAMP_FLUSH(slot) do {} while (0)

constexpr u32 WS_EPA = 0;
constexpr u32 WS_EPS = 256;
constexpr u32 WS_UC  = 65536;
constexpr u32 WS_YC  = WS_UC + 32768;
constexpr u32 WS_KVP = 131072;
constexpr u32 WS_OX  = WS_KVP + (1u << 20);
constexpr u32 WS_UX  = WS_OX + (2u << 20);
constexpr u32 WS_YX  = WS_UX + (2u << 20);
constexpr u32 WS_END = WS_YX + (2u << 20);
constexpr u32 WS_DBG = 16u << 20;
constexpr int SPIN_CAP = 1 << 16;

struct Args {
    const u16 *u, *logA, *Aim, *Cre, *Cim, *logdt, *Dskip, *x, *wqkv, *wo;
    u16* out;
    char* ws;
};

DI f32x4 mfma(u32x4 a, u32x4 b, f32x4 c) {
    return __builtin_amdgcn_mfma_f32_16x16x32_bf16(__builtin_bit_cast(bf16x8, a), __builtin_bit_cast(bf16x8, b), c, 0, 0, 0);
}
DI u32 pk2(f32x2 v) { return __builtin_bit_cast(u32, __builtin_convertvector(v, bf16x2)); }
DI u32 pk(float lo, float hi) { f32x2 v = {lo, hi}; return pk2(v); }
DI float bflo(u32 v) { return __uint_as_float(v << 16); }
DI float bfhi(u32 v) { return __uint_as_float(v & 0xffff0000u); }
DI float rbf(float f) { return bflo(pk(f, f)); }
DI rsrc_t mkrsrc(const void* p, u32 bytes) { return __builtin_amdgcn_make_buffer_rsrc((void*)p, 0, bytes, 0x00020000); }
DI u32x4 bld_dev(rsrc_t r, u32 voff, u32 soff) { return __builtin_amdgcn_raw_buffer_load_b128(r, voff, soff, 16); }
DI u32 bld32_dev(rsrc_t r, u32 voff, u32 soff) { return __builtin_amdgcn_raw_buffer_load_b32(r, voff, soff, 16); }
DI void bst_dev(u32x4 v, rsrc_t r, u32 voff) { __builtin_amdgcn_raw_buffer_store_b128(v, r, voff, 0, 16); }
DI void bst32_dev(u32 v, rsrc_t r, u32 voff) { __builtin_amdgcn_raw_buffer_store_b32(v, r, voff, 0, 16); }
DI u32x4 mkdesc(const void* p, u32 bytes) { u64 v = (u64)p; return u32x4{(u32)v, (u32)(v >> 32) & 0xffffu, bytes, 0x00020000u}; }
DI void dma(u32x4 desc, LAS char* dst, u32 voff, u32 soff) {
    asm volatile("s_mov_b32 m0, %0\n\ts_nop 0\n\tbuffer_load_dwordx4 %1, %2, %3 offen lds"
                 :: "s"((u32)(uintptr_t)dst), "v"(voff), "s"(desc), "s"(soff) : "memory");
}
DI u32x4 lds128(LAS char* p) { return *(LAS u32x4*)p; }
DI void sts128(LAS char* p, u32x4 v) { *(LAS u32x4*)p = v; }
DI void sts64(LAS char* p, u32 lo, u32 hi) { u32x2 v = {lo, hi}; *(LAS u32x2*)p = v; }
DI u32x4 trfrag(LAS char* p0, LAS char* p1) {
    u32x2 a = __builtin_bit_cast(u32x2, __builtin_amdgcn_ds_read_tr16_b64_v4i16((LAS s16x4*)p0));
    u32x2 b = __builtin_bit_cast(u32x2, __builtin_amdgcn_ds_read_tr16_b64_v4i16((LAS s16x4*)p1));
    u32x4 r = {a.x, a.y, b.x, b.y};
    return r;
}
DI void cbar() { asm volatile("" ::: "memory"); }
DI void wg_barrier() { asm volatile("s_waitcnt lgkmcnt(0)\n\ts_barrier" ::: "memory"); }
DI f32x4 asf(u32x4 v) { return __builtin_bit_cast(f32x4, v); }
DI u32x4 asu(f32x4 v) { return __builtin_bit_cast(u32x4, v); }
DI u32 sum4(u32x4 v) { return v.x + v.y + v.z + v.w; }
DI u32 rowsum(u32 s) {
    s += __builtin_amdgcn_update_dpp(0, s, 0x128, 0xf, 0xf, false);
    s += __builtin_amdgcn_update_dpp(0, s, 0x124, 0xf, 0xf, false);
    s += __builtin_amdgcn_update_dpp(0, s, 0x122, 0xf, 0xf, false);
    s += __builtin_amdgcn_update_dpp(0, s, 0x121, 0xf, 0xf, false);
    return s;
}
DI u32 colsum(u32 s) {
    auto r = __builtin_amdgcn_permlane32_swap(s, s, false, false);
    s = r[0] + r[1];
    auto t = __builtin_amdgcn_permlane16_swap(s, s, false, false);
    return t[0] + t[1];
}
DI float phi(float t) {
    float e = __builtin_amdgcn_exp2f(t * 1.44269504089f);
    return fmaxf(t, 1e-6f) + __builtin_amdgcn_fmed3f(e, 0.f, 1.f);
}
DI u32x4 phi8(f32x4 a, f32x4 b) { return u32x4{pk(phi(a.x), phi(a.y)), pk(phi(a.z), phi(a.w)), pk(phi(b.x), phi(b.y)), pk(phi(b.z), phi(b.w))}; }
DI u32x4 pk8(f32x4 a, f32x4 b) { return u32x4{pk(a.x, a.y), pk(a.z, a.w), pk(b.x, b.y), pk(b.z, b.w)}; }
DI u32 ldepoch(const char* p) {
    u32 z = 0; asm volatile("" : "+v"(z));
    return __hip_atomic_load((GAS u32*)(p + z), RLX_AGENT);
}
template <int N, class OK, class LD>
DI void settle(OK ok, LD ld) {
#pragma unroll 1
    for (int sp = 0; sp < SPIN_CAP; ++sp) {
        bool again = false, bad[N];
#pragma unroll
        for (int i = 0; i < N; ++i) { bad[i] = !__all(ok(i)); again |= bad[i]; }
        if (!again) break;
        cbar();
#pragma unroll
        for (int i = 0; i < N; ++i) if (bad[i]) ld(i);
        cbar();
    }
}

constexpr int A_XR = 32768, A_WQ = A_XR + 16384, A_KVB = 77824;

DI void attn_role(const Args& a, LAS char* sm, const int b) {
    STAMP_DECL;
    const int tid = threadIdx.x, lane = tid & 63, wave = __builtin_amdgcn_readfirstlane(tid >> 6);
    const int xcd = b & 7, j = b >> 3;
    const int h = (xcd & 3) * 4 + (j & 3), rb = (xcd >> 2) * 8 + (j >> 2);
    const int c0 = (rb + 2 * (j & 3)) % 7;
    const int g = lane >> 4, r16 = lane & 15, q = (lane >> 2) & 3, p = lane & 3;
    const u32x4 rx = mkdesc(a.x, 2048 * 896), rw = mkdesc(a.wqkv, 448 * 2688), rwo = mkdesc(a.wo, 448 * 896);
    const rsrc_t rws = mkrsrc(a.ws, WS_END);
    const u32x4 rnull = mkdesc(a.x, 0);
    STAMP(0);
    __builtin_amdgcn_s_setprio(PA0);
    u32 tag = ldepoch(a.ws + WS_EPA);
    cbar();
    u32 wv[2], xv[2];
#pragma unroll
    for (int m = 0; m < 2; ++m) {
        const int ch = EXP == 3 ? (lane & 7) : (lane & 7) ^ ((((lane >> 4) & 1) | (m << 1)) << 1);
        wv[m] = (16 * wave + 8 * m + (lane >> 3)) * 2688 + (1 + (ch >> 2)) * 896 + h * 56 + (ch & 3) * 16;
        xv[m] = (rb * 128 + wave * 32 + 8 * m + (lane >> 3)) * 896 + ((EXP == 3 ? (lane & 7) : ((lane & 7) ^ (4 * m + (lane >> 4)))) * 16);
    }
    auto kchunk = [&](int i) { int kc = i + c0; return kc >= 7 ? kc - 7 : kc; };
    auto ldW = [&](int i) {
        const int kc = kchunk(i);
#pragma unroll
        for (int m = 0; m < 2; ++m) dma(EXP == 1 ? rnull : rw, sm + (i % 3) * 8192 + wave * 2048 + m * 1024, wv[m], kc * 64 * 2688);
    };
    auto ldX1 = [&](int i, int m) {
        dma(EXP == 2 ? rnull : rx, sm + A_XR + (i % 3) * 16384 + wave * 4096 + m * 1024, xv[m & 1], kchunk(i) * 128 + (m >> 1) * 16 * 896);
    };
    auto ldX = [&](int i) {
#pragma unroll
        for (int m = 0; m < 4; ++m) ldX1(i, m);
    };
    const u32 qv = (lane >> 2) * 2688 + h * 56 + (((lane & 3) ^ ((lane >> 4) & 2)) * 16);
    auto ldQ1 = [&](int part, int n) {
        int kc = 4 * part + wave + c0; kc = kc >= 7 ? kc - 7 : kc;
        dma(EXP == 1 || EXP == 4 ? rnull : rw, sm + A_WQ + part * 16384 + wave * 4096 + n * 1024, qv, (kc * 64 + n * 16) * 2688);
    };
#pragma unroll
    for (int i = 0; i < 3; ++i) { ldW(i); ldX(i); }
    cbar();

    LAS char* xsrc0 = sm + A_XR + wave * 4096 + r16 * 128 + ((g ^ (r16 >> 1)) * 16);
    LAS char* xsrc1 = sm + A_XR + wave * 4096 + r16 * 128 + (((4 + g) ^ (r16 >> 1)) * 16);
    const int ft = (q >> 1) | ((g & 1) << 1);
    LAS char* wsrc[4];
#pragma unroll
    for (int ct = 0; ct < 4; ++ct) wsrc[ct] = sm + (8 * g + q) * 128 + ((ct ^ ft) * 32) + 8 * p;

    const bool p3 = j < 28;
    const int r64 = p3 ? xcd * 4 + j / 7 : 0, nb = j % 7;
    f32x4 acc[2][4];
#pragma unroll
    for (int st = 0; st < 2; ++st)
#pragma unroll
        for (int ct = 0; ct < 4; ++ct) acc[st][ct] = f32x4{0.f, 0.f, 0.f, 0.f};
    u32x4 xf[14][2];
#pragma unroll
    for (int i = 0; i < 7; ++i) {
        if (i == 0) WAITV(12); else if (i < 6) WAITV(10); else WAITV(4);
        wg_barrier();
        if (i == 0) STAMP(1);
        u32x4 wf[2][4];
#pragma unroll
        for (int st = 0; st < 2; ++st) {
            xf[2 * i][st] = lds128(xsrc0 + (i % 3) * 16384 + st * 2048);
            xf[2 * i + 1][st] = lds128(xsrc1 + (i % 3) * 16384 + st * 2048);
        }
#pragma unroll
        for (int ct = 0; ct < 4; ++ct) { LAS char* s = wsrc[ct] + (i % 3) * 8192; wf[0][ct] = trfrag(s, s + 512); }
#pragma unroll
        for (int ct = 0; ct < 4; ++ct) { LAS char* s = wsrc[ct] + (i % 3) * 8192 + 4096; wf[1][ct] = trfrag(s, s + 512); }
        SB();
        if (i >= 1 && i + 2 < 7) ldW(i + 2);
        asm volatile("s_waitcnt lgkmcnt(14)" ::: "memory");
#pragma unroll
        for (int m = 0; m < 4; ++m) {
            if (i + 3 < 7) ldX1(i + 3, m);
            if (i == 4) ldQ1(0, m);
            if (i == 5 && wave < 3) ldQ1(1, m);
            SB();
#pragma unroll
            for (int ct = 2 * (m & 1); ct < 2 * (m & 1) + 2; ++ct)
#pragma unroll
                for (int st = 0; st < 2; ++st) acc[st][ct] = mfma(xf[2 * i + (m >> 1)][st], wf[m >> 1][ct], acc[st][ct]);
            SB();
        }
    }
    STAMP(2);
    __builtin_amdgcn_s_setprio(PA1);
    {
        u32x4 pkf[2], vf[2];
#pragma unroll
        for (int t = 0; t < 2; ++t) { pkf[t] = phi8(acc[0][t], acc[1][t]); vf[t] = pk8(acc[0][2 + t], acc[1][2 + t]); }
        if (r16 == 12) vf[1] = u32x4{0x3f803f80u, 0x3f803f80u, 0x3f803f80u, 0x3f803f80u};
#pragma unroll
        for (int dt = 0; dt < 2; ++dt)
#pragma unroll
            for (int et = 0; et < 2; ++et)
                sts128(sm + A_XR + wave * 4096 + (dt * 2 + et) * 1024 + lane * 16, asu(mfma(pkf[dt], vf[et], f32x4{0.f, 0.f, 0.f, 0.f})));
    }
    WAITV(0);
    asm volatile("" : "+v"(tag));
    tag += 1;
    wg_barrier();
    const u32x4 tb = {tag & 255u, ((tag >> 8) + tag) & 255u, ((tag >> 16) + tag) & 255u, ((tag >> 24) + tag) & 255u};
    {
        f32x4 s0 = asf(lds128(sm + A_XR + tid * 16)), s1 = asf(lds128(sm + A_XR + 4096 + tid * 16));
        f32x4 s2 = asf(lds128(sm + A_XR + 8192 + tid * 16)), s3 = asf(lds128(sm + A_XR + 12288 + tid * 16));
        bst_dev((asu((s0 + s1) + (s2 + s3)) & 0xffffff00u) | tb, rws, WS_KVP + (h * 16 + rb) * 4096 + tid * 16);
    }
    cbar();
    STAMP(3);
    const u32 ov = (lane >> 3) * 896 + nb * 128 + (((lane & 7) ^ ((lane >> 4) << 1)) * 16);
    auto ldO = [&](int n0, int n1) {
#pragma unroll
        for (int n = n0; n < n1; ++n) dma(rwo, sm + (n * 4 + wave) * 1024, ov, (n * 4 + wave) * 8 * 896);
    };
    u32x4 gk[16];
    auto ldk = [&](int i) { gk[i] = bld_dev(rws, tid * 16, WS_KVP + (h * 16 + ((rb + i) & 15)) * 4096); };
    u32x4 pq[2];
    {
        f32x4 aq[2][2];
#pragma unroll
        for (int st = 0; st < 2; ++st)
#pragma unroll
            for (int dt = 0; dt < 2; ++dt) aq[st][dt] = f32x4{0.f, 0.f, 0.f, 0.f};
        LAS char* qsrc[2];
#pragma unroll
        for (int dt = 0; dt < 2; ++dt) qsrc[dt] = sm + A_WQ + (8 * g + q) * 64 + (((dt ^ g) & 1) * 32) + 8 * p;
        u32x4 wq[2][2][2];
        auto rdq = [&](int i) {
#pragma unroll
            for (int ks = 0; ks < 2; ++ks)
#pragma unroll
                for (int dt = 0; dt < 2; ++dt) { LAS char* s = qsrc[dt] + (2 * i + ks) * 2048; wq[i & 1][ks][dt] = trfrag(s, s + 256); }
        };
        rdq(0);
#pragma unroll
        for (int i = 0; i < 7; ++i) {
            if (i + 1 < 7) rdq(i + 1);
            SB();
#pragma unroll
            for (int ks = 0; ks < 2; ++ks)
#pragma unroll
                for (int dt = 0; dt < 2; ++dt)
#pragma unroll
                    for (int st = 0; st < 2; ++st) aq[st][dt] = mfma(wq[i & 1][ks][dt], xf[2 * i + ks][st], aq[st][dt]);
            SB();
            if (p3) ldO(i == 0 ? 0 : i + 1, i + 2);
            SB();
        }
        STAMP(4);
#pragma unroll
        for (int k = 0; k < 16; ++k) ldk(k);
        cbar();
        SB();
#pragma unroll
        for (int st = 0; st < 2; ++st) pq[st] = phi8(aq[st][0], aq[st][1]);
    }
    SB();
    {
        u32x4 m = gk[0] ^ tb;
#pragma unroll
        for (int i = 1; i < 16; ++i) m |= gk[i] ^ tb;
        if (!__all(((m.x | m.y | m.z | m.w) & 255u) == 0))
            settle<16>([&](int i) { u32x4 t = gk[i] ^ tb; return ((t.x | t.y | t.z | t.w) & 255u) == 0; }, ldk);
    }
    STAMP(5);
    {
        f32x4 s[8];
#pragma unroll
        for (int i = 0; i < 8; ++i) s[i] = asf(gk[i] ^ tb) + asf(gk[i + 8] ^ tb);
#pragma unroll
        for (int i = 0; i < 4; ++i) s[i] += s[i + 4];
        f32x4 t = (s[0] + s[2]) + (s[1] + s[3]);
        u32 lo = pk(t.x, t.y), hi = pk(t.z, t.w);
        const int dt = wave >> 1, et = wave & 1;
        if (dt == 1 && g == 3) { lo = 0; hi = 0; }
        sts64(sm + A_KVB + (et * 64 + lane) * 16 + dt * 8, lo, hi);
    }
    wg_barrier();
    STAMP(6);
    {
        u32x4 kf0 = lds128(sm + A_KVB + lane * 16), kf1 = lds128(sm + A_KVB + 1024 + lane * 16);
        u32x4 zf = lds128(sm + A_KVB + 1024 + (12 + 16 * g) * 16);
#pragma unroll
        for (int st = 0; st < 2; ++st) {
            const f32x4 z4 = {0.f, 0.f, 0.f, 0.f};
            f32x4 dn = mfma(zf, pq[st], z4), n0 = mfma(kf0, pq[st], z4), n1 = mfma(kf1, pq[st], z4);
            float rd = __builtin_amdgcn_rcpf(dn.x + 1e-6f);
            u32x4 of = pk8(n0 * rd, n1 * rd);
            if (g == 3) { of.z = 0; of.w = 0; }
            const u32 cs = tag - colsum(sum4(of));
            if (g == 3) of.w = cs;
            bst_dev(of, rws, WS_OX + ((rb * 8 + wave * 2 + st) * 16 + h) * 1024 + lane * 16);
        }
    }
    cbar();
    STAMP(7);
    if (p3) {
        ldO(8, 14);
        WAITV(0);
        wg_barrier();
        u32x4 go[16];
        auto ldo = [&](int i) { go[i] = bld_dev(rws, lane * 16, WS_OX + ((r64 * 4 + wave) * 16 + i) * 1024); };
#pragma unroll
        for (int i = 0; i < 16; ++i) ldo(i);
        cbar();
        LAS char *os0[4], *os1[4];
#pragma unroll
        for (int t = 0; t < 4; ++t) {
            os0[t] = sm + (4 * g + q) * 128 + (((t ^ (2 * g + (q >> 1))) & 3) * 32) + 8 * p;
            os1[t] = os0[t] + (g == 3 ? 0 : 2048);
        }
        constexpr int PRE = 6;
        u32x4 wf[16][4];
        auto rdo = [&](int hh) {
#pragma unroll
            for (int t = 0; t < 4; ++t) { const int tt = t ^ ((hh & 1) * 2); wf[hh][t] = trfrag(os0[tt] + hh * 28 * 128, os1[tt] + hh * 28 * 128); }
        };
#pragma unroll
        for (int hh = 0; hh < PRE; ++hh) rdo(hh);
        SB();
        {
            u32 s = 0;
#pragma unroll
            for (int i = 0; i < 16; ++i) s += sum4(go[i]);
            if (!__all(colsum(s) == tag * 16u))
                settle<16>([&](int i) { return colsum(sum4(go[i])) == tag; }, ldo);
        }
        STAMP(8);
        const u32 wmask = g == 3 ? 0u : ~0u;
        f32x4 oa[4];
#pragma unroll
        for (int t = 0; t < 4; ++t) oa[t] = f32x4{0.f, 0.f, 0.f, 0.f};
#pragma unroll
        for (int hh = 0; hh < 16; ++hh) go[hh].w &= wmask;
#pragma unroll
        for (int hh = 0; hh < 16; ++hh) {
#pragma unroll
            for (int t = 0; t < 4; ++t) oa[t] = mfma(wf[hh][t], go[hh], oa[t]);
            SB();
            if (hh + PRE < 16) rdo(hh + PRE);
            SB();
        }
        u32 d[4][2];
#pragma unroll
        for (int t = 0; t < 4; ++t) { d[t][0] = pk(oa[t].x, oa[t].y); d[t][1] = pk(oa[t].z, oa[t].w); }
#pragma unroll
        for (int c = 0; c < 2; ++c) {
#pragma unroll
            for (int t = 0; t < 2; ++t) { auto r = __builtin_amdgcn_permlane32_swap(d[t][c], d[t + 2][c], false, false); d[t][c] = r[0]; d[t + 2][c] = r[1]; }
#pragma unroll
            for (int t = 0; t < 4; t += 2) { auto r = __builtin_amdgcn_permlane16_swap(d[t][c], d[t + 1][c], false, false); d[t][c] = r[0]; d[t + 1][c] = r[1]; }
        }
        u32x4* dst = (u32x4*)(a.out + (size_t)(r64 * 64 + wave * 16 + r16) * 896 + 448 + nb * 64 + 16 * g);
        { const rsrc_t ro = mkrsrc(a.out, 2048 * 1792); u32 oo = (u32)((char*)dst - (char*)a.out); bst_dev(u32x4{d[0][0], d[0][1], d[1][0], d[1][1]}, ro, oo); bst_dev(u32x4{d[2][0], d[2][1], d[3][0], d[3][1]}, ro, oo + 16); }
        if (b == 0 && tid == 0) __hip_atomic_store((GAS u32*)(a.ws + WS_EPA), tag, RLX_AGENT);
        STAMP(9);
    }
    STAMP_FLUSH(b);
}

constexpr int S_TAB = 0;
constexpr int S_KC = 34816;
constexpr int S_P = 47104;
constexpr int S_UT = 52480;
constexpr int S_TR = 61696;
constexpr int S_PF = 70912;
constexpr u32 CODE_BYTES = 24576;

struct c2 { f32x2 r, i; };
DI c2 cmul(c2 a, c2 b) { return c2{a.r * b.r - a.i * b.i, a.r * b.i + a.i * b.r}; }
DI c2 cexp2(f32x2 lr, f32x2 rev, float pw) {
    f32x2 e = lr * pw, f = rev * pw;
    f32x2 m = {__builtin_amdgcn_exp2f(e.x), __builtin_amdgcn_exp2f(e.y)};
    f.x = __builtin_amdgcn_fractf(f.x); f.y = __builtin_amdgcn_fractf(f.y);
    f32x2 c = {__builtin_amdgcn_cosf(f.x), __builtin_amdgcn_cosf(f.y)}, s = {__builtin_amdgcn_sinf(f.x), __builtin_amdgcn_sinf(f.y)};
    return c2{m * c, m * s};
}
DI f32x2 unpk(u32 v) { return f32x2{bflo(v), bfhi(v)}; }
DI int rne_div(int x, int s) {
    int y = x + 64;
    return ((y + (1 << (s - 1)) - 1 + ((y >> s) & 1)) >> s) - (64 >> s);
}

DI void ssm_role(const Args& a, LAS char* sm, const int bb, const char* code) {
    STAMP_DECL;
    const int tid = threadIdx.x, lane = tid & 63, wave = __builtin_amdgcn_readfirstlane(tid >> 6);
    const int ct = bb & 7, j = bb >> 3;
    const int ch = wave >> 1, hf = wave & 1, d = ct * 64 + 2 * j + ch;
    const int g = lane >> 4, r16 = lane & 15;
    STAMP(0);
    if (false) {
        const u32x4 rc = mkdesc(code, CODE_BYTES);
        asm volatile("s_mov_b32 m0, %0\n\ts_nop 0\n\tbuffer_load_dword %1, %2, 0 offen lds" :: "s"((u32)(uintptr_t)(sm + S_PF + wave * 256)), "v"(tid * 128), "s"(rc) : "memory");
    }
    if (ct == 7) return;
    __builtin_amdgcn_s_setprio(PS0);
    const rsrc_t rws = mkrsrc(a.ws, WS_END);
    const int trow = 256 * (j >> 2) + 16 * (tid >> 4) + 4 * (j & 3) + 2 * ((tid >> 3) & 1);
    const u32x4* usrc = (const u32x4*)(a.u + (size_t)trow * 448 + 64 * ct) + (tid & 7);
    const u32x4 ua = usrc[0], ub = usrc[56];
    u32 tag = ldepoch(a.ws + WS_EPS + ct * 256);
    const int po = d * 128 + hf * 64 + lane;
    const u32 ldt = a.logdt[d];
    const u32 pA = ((const u32*)a.logA)[po], pI = ((const u32*)a.Aim)[po], pCr = ((const u32*)a.Cre)[po], pCi = ((const u32*)a.Cim)[po];
    const u32 dsk = ((const u32*)a.Dskip)[ct * 32 + j];
    cbar();
    if (tid < 64) sts128(sm + S_UT + (tid >> 5) * 4608 + (tid & 31) * 16, u32x4{0, 0, 0, 0});
    const u32 pofs = (ct * 32 + j) * 8192 + tid * 16;
    const u32 cofs = (ct * 32 + (tid >> 4)) * 8192 + j * 256 + (tid & 15) * 16;
    const u32 pcs = (ct * 32 + j) * 128 + (tid >> 4) * 8 + (tid & 1) * 4;
    const u32 ccs = (ct * 32 + (tid >> 4)) * 128 + (j & 15) * 8 + (j >> 4) * 4;
    auto publish = [&](u32x4 v0, u32x4 v1, u32 xbase, u32 cbase) {
        bst_dev(v0, rws, xbase + pofs);
        bst_dev(v1, rws, xbase + pofs + 4096);
        const u32 s0 = rowsum(sum4(v0)), s1 = rowsum(sum4(v1));
        if (r16 < 2) bst32_dev(tag - (r16 ? s1 : s0), rws, cbase + pcs);
    };
    const int w = 16 + r16 - 8 * g;
    u32 sel1[2], sel2[4], sel3[4], msk[4];
    const int b1 = rne_div(w - 7, 1), b2 = rne_div(w - 7, 2), b3 = rne_div(w - 7, 3);
    {
        auto hsel = [](int dl) -> u32 { int by = ((dl ^ 1) * 2); return (u32)(by | ((by + 1) << 8)); };
#pragma unroll
        for (int e = 0; e < 4; ++e) {
            int x0 = w - 2 * e, x1 = x0 - 1;
            sel3[e] = hsel(rne_div(x0, 3) - b3) | (hsel(rne_div(x1, 3) - b3) << 16);
            sel2[e] = hsel(rne_div(x0, 2) - b2) | (hsel(rne_div(x1, 2) - b2) << 16);
            if (e >= 2) sel1[e - 2] = hsel(rne_div(x0, 1) - b1) | (hsel(rne_div(x1, 1) - b1) << 16);
            msk[e] = hf ? 0u : (x0 < 0 ? 0xffffu : 0u) | (x1 < 0 ? 0xffff0000u : 0u);
        }
        asm volatile("" : "+v"(sel1[0]), "+v"(sel1[1]), "+v"(sel2[0]), "+v"(sel2[1]), "+v"(sel2[2]), "+v"(sel2[3]), "+v"(sel3[0]), "+v"(sel3[1]), "+v"(sel3[2]), "+v"(sel3[3]));
        asm volatile("" : "+v"(msk[0]), "+v"(msk[1]), "+v"(msk[2]), "+v"(msk[3]));
    }
    WAITV(0);
    asm volatile("" : "+v"(tag));
    tag += 1;
    LAS char* trw = sm + S_TR + (tid & 7) * 288 + (tid >> 3) * 8;
    LAS char* trr = sm + S_TR + (((tid >> 4) & 3) * 8 + (tid >> 6)) * 288 + (tid & 15) * 16;
    sts64(trw, ua.x, ub.x); sts64(trw + 8 * 288, ua.y, ub.y); sts64(trw + 16 * 288, ua.z, ub.z); sts64(trw + 24 * 288, ua.w, ub.w);
    wg_barrier();
    publish(lds128(trr), lds128(trr + 4 * 288), WS_UX, WS_UC);
    cbar();
    STAMP(1);
    LAS char* tab = sm + S_TAB + wave * 8704;
    LAS char* tw = tab + lane * 8;
    LAS char* tr = tab + r16 * 544 + g * 16;
    const float L2E = 1.44269504089f;
    const float dt = rbf(__builtin_amdgcn_exp2f(bflo(ldt) * L2E));
    f32x2 lr = unpk(pA) * L2E;
    lr = unpk(pk(__builtin_amdgcn_exp2f(lr.x), __builtin_amdgcn_exp2f(lr.y))) * dt * -L2E;
    const f32x2 rev = unpk(pI) * dt * 0.15915494309f;
    c2 G = {unpk(pCr) * (dt * 2.f), unpk(pCi) * (dt * 2.f)};
    u32x2 em[16];
#define EMIT(row, v) do { em[row] = u32x2{pk2(v.r), pk2(v.i)}; if ((row) & 1) { if ((row) > 1) { KEEP(em[(row) - 3]); KEEP(em[(row) - 2]); } \
        *(LAS u32x2*)(tw + ((row) - 1) * 544) = em[(row) - 1]; *(LAS u32x2*)(tw + (row) * 544) = em[row]; } } while (0)
    u32x4 ff[8], gf[8];
    {
        c2 e8 = cexp2(lr, -rev, 8.f), F = e8;
        em[0] = u32x2{0x3f803f80u, 0u};
        EMIT(1, F);
#pragma unroll
        for (int i = 2; i < 16; ++i) { F = cmul(F, e8); EMIT(i, F); }
        cbar();
#pragma unroll
        for (int ks = 0; ks < 8; ++ks) ff[ks] = lds128(tr + ks * 64);
        cbar();
    }
    c2 w1 = cexp2(lr, rev, 1.f), Hh = cexp2(lr, rev, 128.f), T;
    LAS char* kc = sm + S_KC + wave * 3072;
    f32x4 ka;
#define KRD() do { cbar(); _Pragma("unroll") for (int ks = 0; ks < 8; ++ks) gf[ks] = lds128(tr + ks * 64); cbar(); } while (0)
#define KMM() do { ka = f32x4{0.f, 0.f, 0.f, 0.f}; _Pragma("unroll") for (int ks = 0; ks < 8; ++ks) ka = mfma(gf[ks], ff[ks], ka); } while (0)
#pragma unroll
    for (int i = 0; i < 2; ++i) {
        EMIT(8 * i, G);
        T = G;
#pragma unroll
        for (int r = 1; r < 8; ++r) { T = cmul(T, w1); EMIT(8 * i + r, T); }
        G = cmul(G, Hh);
    }
    KRD();
    w1 = cmul(w1, w1);
#pragma unroll
    for (int i = 0; i < 2; ++i) {
        EMIT(4 * i, G);
        T = G;
#pragma unroll
        for (int r = 1; r < 4; ++r) { T = cmul(T, w1); EMIT(4 * i + r, T); }
        G = cmul(G, Hh);
        if (i == 0) { SB(); KMM(); SB(); }
    }
    sts128(kc + (128 * (g >> 1) + 4 * (g & 1) + 8 * r16) * 4, asu(ka));
    w1 = cmul(w1, w1);
#pragma unroll
    for (int i = 0; i < 4; ++i) {
        EMIT(8 + 2 * i, G);
        T = cmul(G, w1); EMIT(9 + 2 * i, T);
        G = cmul(G, Hh);
    }
    KRD();
    STAMP(2);
    u32x4 uu[2]; u32 uc[2];
    auto ldu = [&](int i) { uu[i] = bld_dev(rws, cofs, WS_UX + i * 16 * 8192); uc[i] = bld32_dev(rws, ccs, WS_UC + i * 16 * 128); };
    ldu(0); ldu(1);
    cbar();
#pragma unroll
    for (int i = 0; i < 8; ++i) {
        EMIT(i, G);
        G = cmul(G, Hh);
        if (i == 3) { SB(); KMM(); SB(); }
    }
    sts64(tw + 8 * 544, pk2(G.r), pk2(G.i));
    {
        LAS char* k0 = kc + (g < 2 ? 256 + 64 * g + 4 * r16 : 384 + 64 * (g - 2) + 2 * r16) * 4;
        u32x4 kb = asu(ka);
        sts64(k0, kb.x, kb.y);
        sts64(k0 + (g < 2 ? 8 : 128), kb.z, kb.w);
    }
    KRD();
    KMM();
    {
        LAS float* k0 = (LAS float*)(kc + (512 + 64 * g + r16) * 4);
        k0[0] = ka.x; k0[16] = ka.y; k0[32] = ka.z; k0[48] = ka.w;
    }
    wg_barrier();
    STAMP(3);
    {
        LAS char* k0 = sm + S_KC + ch * 6144;
        LAS char* R = sm + S_P + ch * 2688;
        const int t = tid & 127;
#pragma unroll
        for (int it = 0; it < 2; ++it) {
            const int e = t + 128 * it;
            if (e < 164) {
                u32x4 ka0 = lds128(k0 + e * 16), kb0 = lds128(k0 + 3072 + e * 16);
                float kn = *(LAS float*)(k0 + e * 16 + 16) + *(LAS float*)(k0 + 3072 + e * 16 + 16);
                float k_0 = asf(ka0).x + asf(kb0).x, k_1 = asf(ka0).y + asf(kb0).y, k_2 = asf(ka0).z + asf(kb0).z, k_3 = asf(ka0).w + asf(kb0).w;
                asm volatile("" : "+v"(k_0), "+v"(k_1), "+v"(k_2), "+v"(k_3));
                sts128(R + (163 - e) * 16, u32x4{pk(kn, k_3), pk(k_3, k_2), pk(k_2, k_1), pk(k_1, k_0)});
                if (e == 0) sts128(R + 164 * 16, u32x4{pk(k_0, 0.f), 0, 0, 0});
            } else if (e < 167) sts128(R + (e + 1) * 16, u32x4{0, 0, 0, 0});
        }
    }
    if (!__all(rowsum(sum4(uu[0]) + sum4(uu[1])) + uc[0] + uc[1] == 2u * tag))
        settle<2>([&](int i) { return rowsum(sum4(uu[i])) + uc[i] == tag; }, ldu);
#pragma unroll
    for (int i = 0; i < 2; ++i) {
        LAS char* ud = sm + S_UT + 512 + (4 * i + wave) * 512 + r16 * 32 + g * 8;
        sts64(ud, __builtin_amdgcn_perm(uu[i].y, uu[i].x, 0x05040100u), __builtin_amdgcn_perm(uu[i].w, uu[i].z, 0x05040100u));
        sts64(ud + 4608, __builtin_amdgcn_perm(uu[i].y, uu[i].x, 0x07060302u), __builtin_amdgcn_perm(uu[i].w, uu[i].z, 0x07060302u));
    }
    wg_barrier();
    if (j == 0 && tid == 0) __hip_atomic_store((GAS u32*)(a.ws + WS_EPS + ct * 256), tag, RLX_AGENT);
    STAMP(4);
    __builtin_amdgcn_s_setprio(PS1);
    f32x4 y[8];
#pragma unroll
    for (int i = 0; i < 8; ++i) y[i] = f32x4{0.f, 0.f, 0.f, 0.f};
    {
        LAS u32* R = (LAS u32*)(sm + S_P + ch * 2688) + 655;
        LAS u32 *P0 = R - (128 * hf + w - 7) - 262, *P1 = R - (256 + 64 * hf + b1) - 132, *P2 = R - (384 + 32 * hf + b2) - 130, *P3 = R - (512 + 16 * hf + b3) - 108;
#define Q0(i) P0[262 - (i)]
#define Q1(i) P1[132 - (i)]
#define Q2(i) P2[130 - (i)]
#define Q3(i) P3[108 - (i)]
        LAS char* ubase = sm + S_UT + ch * 4608 + 480 + r16 * 32 + g * 16 - 256 * hf - 192;
#define PERM __builtin_amdgcn_perm
        auto f1 = [&](u32 d0, u32 d1, u32 d2) { return u32x4{PERM(d2, d1, sel1[0]), PERM(d2, d1, sel1[1]), PERM(d1, d0, sel1[0]), PERM(d1, d0, sel1[1])}; };
        auto f2 = [&](u32 d0, u32 d1) { return u32x4{PERM(d1, d0, sel2[0]), PERM(d1, d0, sel2[1]), PERM(d1, d0, sel2[2]), PERM(d1, d0, sel2[3])}; };
        auto f3 = [&](u32 d0) { return u32x4{PERM(d0, d0, sel3[0]), PERM(d0, d0, sel3[1]), PERM(d0, d0, sel3[2]), PERM(d0, d0, sel3[3])}; };
        auto mix = [&](u32x4 lo, u32x4 hi) { return u32x4{(lo.x & msk[0]) | (hi.x & ~msk[0]), (lo.y & msk[1]) | (hi.y & ~msk[1]), (lo.z & msk[2]) | (hi.z & ~msk[2]), (lo.w & msk[3]) | (hi.w & ~msk[3])}; };
        u32x4 T[2][8], U[2][8];
        auto prep = [&](int it) {
            u32x4* t = T[it & 1];
            t[0] = u32x4{Q0(32 * it + 6), Q0(32 * it + 4), Q0(32 * it + 2), Q0(32 * it)};
#pragma unroll
            for (int bq = 0; bq < 8; ++bq) U[it & 1][bq] = lds128(ubase + 512 * bq + 192 - 64 * it);
            t[1] = f1(Q1(16 * it), Q1(16 * it + 2), Q1(16 * it + 4));
            t[2] = f2(Q2(8 * it), Q2(8 * it + 2)); t[3] = f2(Q2(8 * it + 64), Q2(8 * it + 66));
            t[4] = f3(Q3(4 * it)); t[5] = f3(Q3(4 * it + 32)); t[6] = f3(Q3(4 * it + 64)); t[7] = f3(Q3(4 * it + 96));
            if (it == 0) {
                t[1] = mix(u32x4{Q0(262), Q0(260), Q0(258), Q0(256)}, t[1]);
                t[2] = mix(f1(Q1(128), Q1(130), Q1(132)), t[2]);
                t[4] = mix(f2(Q2(128), Q2(130)), t[4]);
            }
        };
        prep(3);
#pragma unroll
        for (int it = 3; it >= 0; --it) {
            if (it < 3) SB();
            if (it > 0) prep(it - 1);
#pragma unroll
            for (int aa = 0; aa < 8; ++aa)
#pragma unroll
                for (int bq = 0; bq + aa < 8; ++bq) y[aa + bq] = mfma(T[it & 1][aa], U[it & 1][bq], y[aa + bq]);
            if (it > 0 && it < 3) {
                __builtin_amdgcn_sched_group_barrier(0x100, 22, 0);
                __builtin_amdgcn_sched_group_barrier(0x008, 10, 0);
#pragma unroll
                for (int k = 0; k < 26; ++k) { __builtin_amdgcn_sched_group_barrier(0x002, 2, 0); __builtin_amdgcn_sched_group_barrier(0x008, 1, 0); }
            } else if (it == 3) {
                __builtin_amdgcn_sched_group_barrier(0x100, 32, 0);
#pragma unroll
                for (int k = 0; k < 36; ++k) { __builtin_amdgcn_sched_group_barrier(0x008, 1, 0); __builtin_amdgcn_sched_group_barrier(0x002, 2, 0); }
            }
        }
    }
    SB();
    STAMP(5);
    {
        LAS char* yd = sm + S_TAB + wave * 8192 + lane * 16;
#pragma unroll
        for (int i = 0; i < 8; ++i) sts128(yd + i * 1024, asu(y[i]));
    }
    wg_barrier();
    {
        const float d0 = bflo(dsk), d1 = bfhi(dsk);
        u32x4 o[2];
#pragma unroll
        for (int i = 0; i < 2; ++i) {
            LAS char* ys = sm + S_TAB + (wave + 4 * i) * 1024 + lane * 16;
            f32x4 y0 = asf(lds128(ys)) + asf(lds128(ys + 8192));
            f32x4 y1 = asf(lds128(ys + 16384)) + asf(lds128(ys + 24576));
            o[i] = u32x4{pk(y0.x + bflo(uu[i].x) * d0, y1.x + bfhi(uu[i].x) * d1), pk(y0.y + bflo(uu[i].y) * d0, y1.y + bfhi(uu[i].y) * d1),
                         pk(y0.z + bflo(uu[i].z) * d0, y1.z + bfhi(uu[i].z) * d1), pk(y0.w + bflo(uu[i].w) * d0, y1.w + bfhi(uu[i].w) * d1)};
        }
        publish(o[0], o[1], WS_YX, WS_YC);
    }
    WAITV(0);
    STAMP(6);
    {
        u32x4 v[2]; u32 vc[2];
        auto ldy = [&](int i) { v[i] = bld_dev(rws, cofs, WS_YX + i * 16 * 8192); vc[i] = bld32_dev(rws, ccs, WS_YC + i * 16 * 128); };
        ldy(0); ldy(1);
        cbar();
        settle<2>([&](int i) { return rowsum(sum4(v[i])) + vc[i] == tag; }, ldy);
        STAMP(7);
        sts128(trr, v[0]); sts128(trr + 4 * 288, v[1]);
        wg_barrier();
        u32x2 t0 = *(LAS u32x2*)trw, t1 = *(LAS u32x2*)(trw + 8 * 288), t2 = *(LAS u32x2*)(trw + 16 * 288), t3 = *(LAS u32x2*)(trw + 24 * 288);
        u32x4* dst = (u32x4*)(a.out + (size_t)trow * 896 + 64 * ct) + (tid & 7);
        { const rsrc_t ro = mkrsrc(a.out, 2048 * 1792); u32 oo = (u32)((char*)dst - (char*)a.out); bst_dev(u32x4{t0.x, t1.x, t2.x, t3.x}, ro, oo); bst_dev(u32x4{t0.y, t1.y, t2.y, t3.y}, ro, oo + 1792); }
    }
    STAMP(8);
    STAMP_FLUSH(256 + bb);
}

#if defined(__HIP_DEVICE_COMPILE__)
#define NOPK __attribute__((target("no-packed-fp32-ops")))
#else
#define NOPK
#endif
__global__ NOPK __launch_bounds__(256, 2) void fused_kernel(Args a) {
    __shared__ __attribute__((aligned(16))) char smem[81920];
    u64 te_; asm volatile("s_memrealtime %0\n\ts_waitcnt lgkmcnt(0)" : "=s"(te_) :: "memory");
    LAS char* sm = (LAS char*)smem;
    const char* code = (const char*)(__builtin_amdgcn_s_getpc() & ~(u64)255);
    const bool is_attn = SSM_FIRST ? blockIdx.x >= 256 : blockIdx.x < 256;
    const int b = blockIdx.x & 255;
    if (is_attn) attn_role(a, sm, b);
    else ssm_role(a, sm, b, code);
    if (false) { while (__builtin_amdgcn_s_memrealtime() - te_ < 4000) {} }
}

extern "C" void kernel_launch(void* const* d_in, const int* in_sizes, int n_in,
                              void* d_out, int out_size, void* d_ws, size_t ws_size,
                              hipStream_t stream) {
    Args a;
    a.u = (const u16*)d_in[0]; a.logA = (const u16*)d_in[1]; a.Aim = (const u16*)d_in[2];
    a.Cre = (const u16*)d_in[3]; a.Cim = (const u16*)d_in[4]; a.logdt = (const u16*)d_in[5];
    a.Dskip = (const u16*)d_in[6]; a.x = (const u16*)d_in[7]; a.wqkv = (const u16*)d_in[8];
    a.wo = (const u16*)d_in[9]; a.out = (u16*)d_out; a.ws = (char*)d_ws;
    fused_kernel<<<512, 256, 0, stream>>>(a);
}
```

```cpp
#include <hip/hip_runtime.h>
#include <stdint.h>

typedef unsigned short u16;
typedef unsigned int u32;
typedef unsigned long long u64;
typedef __bf16 bf16x8 __attribute__((ext_vector_type(8)));
typedef __bf16 bf16x2 __attribute__((ext_vector_type(2)));
typedef short s16x4 __attribute__((ext_vector_type(4)));
typedef float f32x4 __attribute__((ext_vector_type(4)));
typedef float f32x2 __attribute__((ext_vector_type(2)));
typedef float f32x16 __attribute__((ext_vector_type(16)));
typedef u32 u32x2 __attribute__((ext_vector_type(2)));
typedef u32 u32x4 __attribute__((ext_vector_type(4)));
#define LAS __attribute__((address_space(3)))
#define GAS __attribute__((address_space(1)))
#define DI __device__ __forceinline__
#define RLX_AGENT __ATOMIC_RELAXED, __HIP_MEMORY_SCOPE_AGENT
#define SB() __builtin_amdgcn_sched_barrier(0)
#define WAITV(n) asm volatile("s_waitcnt vmcnt(" #n ")" ::: "memory")
#define KEEP(x) asm volatile("" :: "v"(x))
typedef __amdgpu_buffer_rsrc_t rsrc_t;
#define EXP 0
#define PA0 1
#define PA1 2
#define PS0 1
#define PS1 0
#define POLL_SLEEP 2
#define ABL 0
#define GK0 14
#define PRO 10
#define QS0 44
#define PSV 3
#define NO1 4
#define PRE 10
#define SSM_FIRST 0

#define STAMP(i) do {} while (0)
#define STAMP_DECL do {} while (0)
#define STAMP_FLUSH(slot) do {} while (0)

constexpr u32 WS_EPA = 0;
constexpr u32 WS_EPS = 256;
constexpr u32 WS_UC  = 65536;
constexpr u32 WS_YC  = WS_UC + 131072;
constexpr u32 WS_KVP = 524288;
constexpr u32 WS_OX = WS_KVP + (1u << 20);
constexpr u32 WS_UX  = WS_OX + (2u << 20);
constexpr u32 WS_YX  = WS_UX + (2u << 20);
constexpr u32 WS_END = WS_YX + (2u << 20);
constexpr u32 WS_DBG = 16u << 20;
constexpr int SPIN_CAP = 1 << 16;

struct Args {
    const u16 *u, *logA, *Aim, *Cre, *Cim, *logdt, *Dskip, *x, *wqkv, *wo;
    u16* out;
    char* ws;
};

DI f32x4 mfma(u32x4 a, u32x4 b, f32x4 c) {
    return __builtin_amdgcn_mfma_f32_16x16x32_bf16(__builtin_bit_cast(bf16x8, a), __builtin_bit_cast(bf16x8, b), c, 0, 0, 0);
}
DI u32 pk2(f32x2 v) { return __builtin_bit_cast(u32, __builtin_convertvector(v, bf16x2)); }
DI u32 pk(float lo, float hi) { f32x2 v = {lo, hi}; return pk2(v); }
DI float bflo(u32 v) { return __uint_as_float(v << 16); }
DI float bfhi(u32 v) { return __uint_as_float(v & 0xffff0000u); }
DI float rbf(float f) { return bflo(pk(f, f)); }
DI rsrc_t mkrsrc(const void* p, u32 bytes) { return __builtin_amdgcn_make_buffer_rsrc((void*)p, 0, bytes, 0x00020000); }
DI u32x4 bld_dev(rsrc_t r, u32 voff, u32 soff) { return __builtin_amdgcn_raw_buffer_load_b128(r, voff, soff, 16); }
DI u32 bld32_dev(rsrc_t r, u32 voff, u32 soff) { return __builtin_amdgcn_raw_buffer_load_b32(r, voff, soff, 16); }
DI void bst_dev(u32x4 v, rsrc_t r, u32 voff) { __builtin_amdgcn_raw_buffer_store_b128(v, r, voff, 0, 16); }
DI void bst32_dev(u32 v, rsrc_t r, u32 voff) { __builtin_amdgcn_raw_buffer_store_b32(v, r, voff, 0, 16); }
DI u32x4 mkdesc(const void* p, u32 bytes) { u64 v = (u64)p; return u32x4{(u32)v, (u32)(v >> 32) & 0xffffu, bytes, 0x00020000u}; }
DI void setm0(LAS char* dst) { asm volatile("s_mov_b32 m0, %0\n\ts_nop 0" :: "s"((u32)(uintptr_t)dst) : "memory"); }
template <int OFF>
DI void dma(u32x4 desc, u32 voff, u32 soff) {
    asm volatile("buffer_load_dwordx4 %0, %1, %2 offen offset:%3 lds" :: "v"(voff), "s"(desc), "s"(soff), "i"(OFF) : "memory");
}
DI u32x4 lds128(LAS char* p) { return *(LAS u32x4*)p; }
DI void sts128(LAS char* p, u32x4 v) { *(LAS u32x4*)p = v; }
DI void sts64(LAS char* p, u32 lo, u32 hi) { u32x2 v = {lo, hi}; *(LAS u32x2*)p = v; }
DI u32x4 trfrag(LAS char* p0, LAS char* p1) {
    u32x2 a = __builtin_bit_cast(u32x2, __builtin_amdgcn_ds_read_tr16_b64_v4i16((LAS s16x4*)p0));
    u32x2 b = __builtin_bit_cast(u32x2, __builtin_amdgcn_ds_read_tr16_b64_v4i16((LAS s16x4*)p1));
    u32x4 r = {a.x, a.y, b.x, b.y};
    return r;
}
DI void cbar() { asm volatile("" ::: "memory"); }
DI void wg_barrier() { asm volatile("s_waitcnt lgkmcnt(0)\n\ts_barrier" ::: "memory"); }
DI f32x4 asf(u32x4 v) { return __builtin_bit_cast(f32x4, v); }
DI u32x4 asu(f32x4 v) { return __builtin_bit_cast(u32x4, v); }
DI u32 sum4(u32x4 v) { return v.x + v.y + v.z + v.w; }
DI u32 rowsum(u32 s) {
    s += __builtin_amdgcn_update_dpp(0, s, 0x128, 0xf, 0xf, false);
    s += __builtin_amdgcn_update_dpp(0, s, 0x124, 0xf, 0xf, false);
    s += __builtin_amdgcn_update_dpp(0, s, 0x122, 0xf, 0xf, false);
    s += __builtin_amdgcn_update_dpp(0, s, 0x121, 0xf, 0xf, false);
    return s;
}
DI u32 colsum(u32 s) {
    auto r = __builtin_amdgcn_permlane32_swap(s, s, false, false);
    s = r[0] + r[1];
    auto t = __builtin_amdgcn_permlane16_swap(s, s, false, false);
    return t[0] + t[1];
}
DI float phi(float t) {
    float e = __builtin_amdgcn_exp2f(t * 1.44269504089f);
    return __builtin_amdgcn_fmed3f(t, 1e-6f, __builtin_inff()) + __builtin_amdgcn_fmed3f(e, 0.f, 1.f);
}
DI u32x4 phi8(const f32x16& a, int s) { return u32x4{pk(phi(a[8 * s]), phi(a[8 * s + 1])), pk(phi(a[8 * s + 2]), phi(a[8 * s + 3])), pk(phi(a[8 * s + 4]), phi(a[8 * s + 5])), pk(phi(a[8 * s + 6]), phi(a[8 * s + 7]))}; }
DI u32x4 pk8(const f32x16& a, int s) { return u32x4{pk(a[8 * s], a[8 * s + 1]), pk(a[8 * s + 2], a[8 * s + 3]), pk(a[8 * s + 4], a[8 * s + 5]), pk(a[8 * s + 6], a[8 * s + 7])}; }
DI u32x4 quad(const f32x16& a, int r) { return asu(f32x4{a[4 * r], a[4 * r + 1], a[4 * r + 2], a[4 * r + 3]}); }
DI f32x16 zero16() { f32x16 z; for (int i = 0; i < 16; ++i) z[i] = 0.f; return z; }
DI f32x16 mfma32(u32x4 a, u32x4 b, f32x16 c) {
    return __builtin_amdgcn_mfma_f32_32x32x16_bf16(__builtin_bit_cast(bf16x8, a), __builtin_bit_cast(bf16x8, b), c, 0, 0, 0);
}
DI u32 halfsum(u32 s) { auto r = __builtin_amdgcn_permlane32_swap(s, s, false, false); return r[0] + r[1]; }
DI float colsumf(float f) {
    auto r = __builtin_amdgcn_permlane32_swap(__float_as_uint(f), __float_as_uint(f), false, false);
    f = __uint_as_float(r[0]) + __uint_as_float(r[1]);
    auto t = __builtin_amdgcn_permlane16_swap(__float_as_uint(f), __float_as_uint(f), false, false);
    return __uint_as_float(t[0]) + __uint_as_float(t[1]);
}
DI u32 ldepoch(const char* p) {
    u32 v;
    asm volatile("global_load_dword %0, %1, %2 sc1" : "=v"(v) : "v"(0u), "s"(p) : "memory");
    return v;
}
template <int BIT> DI void reld(u32x4& v, u32x4 desc, u32 voff, u32 soff, u32 mask) {
    asm volatile("s_bitcmp1_b32 %4, %5\n\ts_cbranch_scc0 1f\n\tbuffer_load_dwordx4 %0, %1, %2, %3 offen sc1\n1:"
                 : "+v"(v) : "v"(voff), "s"(desc), "s"(soff), "s"(mask), "i"(BIT) : "scc", "memory");
}
template <int BIT> DI void reld32(u32& v, u32x4 desc, u32 voff, u32 soff, u32 mask) {
    asm volatile("s_bitcmp1_b32 %4, %5\n\ts_cbranch_scc0 1f\n\tbuffer_load_dword %0, %1, %2, %3 offen sc1\n1:"
                 : "+v"(v) : "v"(voff), "s"(desc), "s"(soff), "s"(mask), "i"(BIT) : "scc", "memory");
}
template <class T> DI void pin(T& v) { asm volatile("" : "+v"(v) :: "memory"); }
template <int I> struct IC { static constexpr int v = I; };
template <int N, int I = 0, class F> DI void sfor(F f) { if constexpr (I < N) { f(IC<I>{}); sfor<N, I + 1>(f); } }
template <int N, class OK, class LD, class PIN>
DI void settle(u32 bad, OK ok, LD ld, PIN pn) {
#pragma unroll 1
    for (int sp = 0; bad != 0 && sp < SPIN_CAP; ++sp) {
        __builtin_amdgcn_s_sleep(POLL_SLEEP);
        sfor<N>([&](auto i) { ld(i, bad); });
        WAITV(0);
        sfor<N>([&](auto i) { pn(i.v); });
        sfor<N>([&](auto i) { if ((bad >> i.v & 1) && __all(ok(i.v))) bad &= ~(1u << i.v); });
    }
}
template <int N, class OK> DI u32 badmask(OK ok) { u32 bad = 0; sfor<N>([&](auto i) { if (!__all(ok(i.v))) bad |= 1u << i.v; }); return bad; }

constexpr int A_RED = 32768, A_WQ = 49152, A_KVB = 77824;

template <int OFF>
DI void ldx(u32x4& v, u32x4 desc, u32 voff, u32 soff) {
    asm volatile("buffer_load_dwordx4 %0, %1, %2, %3 offen offset:%4" : "=v"(v) : "v"(voff), "s"(desc), "s"(soff), "i"(OFF) : "memory");
}
DI void ldd(u32x4& v, u32x4 desc, u32 voff, u32 soff) {
    asm volatile("buffer_load_dwordx4 %0, %1, %2, %3 offen sc1" : "=v"(v) : "v"(voff), "s"(desc), "s"(soff) : "memory");
}
template <int N> DI void waitx(u32x4* x) { asm volatile("s_waitcnt vmcnt(%4)" : "+v"(x[0]), "+v"(x[1]), "+v"(x[2]), "+v"(x[3]) : "i"(N) : "memory"); }
#define QSEL(PERMS, o, v, k, mask) asm volatile("s_mov_b64 vcc, %12\n\t" \
    "v_cndmask_b32_dpp %0, %4, %8, vcc " PERMS "\n\tv_cndmask_b32_dpp %1, %5, %9, vcc " PERMS "\n\t" \
    "v_cndmask_b32_dpp %2, %6, %10, vcc " PERMS "\n\tv_cndmask_b32_dpp %3, %7, %11, vcc " PERMS \
    : "=&v"(o[0]), "=&v"(o[1]), "=&v"(o[2]), "=&v"(o[3]) \
    : "v"(v[0]), "v"(v[1]), "v"(v[2]), "v"(v[3]), "v"(k[0]), "v"(k[1]), "v"(k[2]), "v"(k[3]), "s"(mask) : "vcc")
DI void qstep(int k, const u32x4* x, u32 (&t)[4][4], u32x4* o) {
#define QP1 "quad_perm:[1,0,3,2] row_mask:0xf bank_mask:0xf"
#define QP2 "quad_perm:[2,3,0,1] row_mask:0xf bank_mask:0xf"
    u32 r[4];
    if (k == 0) QSEL(QP1, t[0], x[1], x[0], 0x5555555555555555ull);
    if (k == 1) QSEL(QP1, t[2], x[3], x[2], 0x5555555555555555ull);
    if (k == 2) QSEL(QP1, t[1], x[0], x[1], 0xaaaaaaaaaaaaaaaaull);
    if (k == 3) QSEL(QP1, t[3], x[2], x[3], 0xaaaaaaaaaaaaaaaaull);
    if (k == 4) QSEL(QP2, r, t[2], t[0], 0x3333333333333333ull);
    if (k == 5) QSEL(QP2, r, t[3], t[1], 0x3333333333333333ull);
    if (k == 6) QSEL(QP2, r, t[0], t[2], 0xccccccccccccccccull);
    if (k == 7) QSEL(QP2, r, t[1], t[3], 0xccccccccccccccccull);
    if (k >= 4) o[k - 4] = u32x4{r[0], r[1], r[2], r[3]};
}

template <int N> DI void waitv() { asm volatile("s_waitcnt vmcnt(%0)" :: "i"(N) : "memory"); }
constexpr int issued_before(int gs) {
    int n = PRO + 4 + gs;
    if (n <= 42) return n;
    int q = gs - QS0;
    return 42 + (q < 0 ? 0 : q > 7 ? 7 : q);
}

DI void attn_role(const Args& a, LAS char* sm, const int b) {
    STAMP_DECL;
    const int tid = threadIdx.x, lane = tid & 63, wave = __builtin_amdgcn_readfirstlane(tid >> 6);
    const int xcd = b & 7, j = b >> 3;
    const int h = (xcd & 3) * 4 + (j & 3), rb = (xcd >> 2) * 8 + (j >> 2);
    const int c0 = (rb + 2 * (j & 3)) % 7;
    const int r32 = lane & 31, hh = lane >> 5, blk = (lane >> 4) & 1, q = (lane >> 2) & 3, p = lane & 3;
    const u32x4 rx = mkdesc(a.x, 2048 * 896), rw = mkdesc(a.wqkv, 448 * 2688), rwo = mkdesc(a.wo, 448 * 896);
    const rsrc_t rws = mkrsrc(a.ws, WS_END);
    const u32x4 dws = mkdesc(a.ws, WS_END);
    STAMP(0);
    __builtin_amdgcn_s_setprio(PA0);
    u32 tag = ldepoch(a.ws + WS_EPA);
    cbar();
    const int wch = (lane & 7) ^ (blk << 2);
    const u32 wv = (16 * wave + (lane >> 3)) * 2688 + (1 + (wch >> 2)) * 896 + h * 56 + (wch & 3) * 16;
    const u32 xv = (rb * 128 + wave * 32 + (lane & 28)) * 896 + hh * 64 + p * 16;
    auto kchunk = [&](int i) { int kc = i + c0; return kc >= 7 ? kc - 7 : kc; };
    u32x4 xr[7][4], xf[28];
    auto ldX1 = [&](int i, int m) {
        if (m == 0) ldx<0>(xr[i][0], rx, xv, kchunk(i) * 128);
        if (m == 1) ldx<896>(xr[i][1], rx, xv, kchunk(i) * 128);
        if (m == 2) ldx<1792>(xr[i][2], rx, xv, kchunk(i) * 128);
        if (m == 3) ldx<2688>(xr[i][3], rx, xv, kchunk(i) * 128);
    };
    const u32 qv = (16 * wave + (lane >> 2)) * 2688 + h * 56 + p * 16;
    auto ldQ1 = [&](int n) { setm0(sm + A_WQ + n * 4096 + wave * 1024); dma<0>(rw, qv, kchunk(n) * 64 * 2688); };
    auto ldn = [&](int n) {
        if (n >= 49) return;
        if (n >= 42) { ldQ1(n - 42); return; }
        const int i = n / 6, m = n % 6;
        if (m < 4) ldX1(i, m);
        if (m == 4) { setm0(sm + (i % 6) * 8192 + wave * 2048); dma<0>(rw, wv, kchunk(i) * 64 * 2688); }
        if (m == 5) dma<1024>(rw, wv, kchunk(i) * 64 * 2688 + 8 * 2688 - 1024);
    };
#pragma unroll
    for (int n = 0; n < PRO; ++n) ldn(n);
    cbar();

    LAS char* wsrc[2];
#pragma unroll
    for (int sg = 0; sg < 2; ++sg) wsrc[sg] = sm + (32 * hh + q) * 128 + ((sg ^ (q >> 1)) * 64) + blk * 32 + 8 * p;

    const bool p3 = j < 28;
    const int r64 = p3 ? xcd * 4 + j / 7 : 0, nb = j % 7;
    f32x16 acc[2] = {zero16(), zero16()};
    u32 qt[4][4];
    u32x4 wf[7][4][2];
    auto skk = [](int i, int sl) { return i < 6 ? sl >> 1 : sl & 3; };
    auto ssg = [](int i, int sl) { return i < 6 ? sl & 1 : sl >> 2; };
    auto rdw = [&](int i, int sl) { LAS char* s = wsrc[ssg(i, sl)] + (i % 6) * 8192 + skk(i, sl) * 1024; wf[i][skk(i, sl)][ssg(i, sl)] = trfrag(s, s + 512); };
    waitx<PRO - 4>(xr[0]);
    sfor<8>([&](auto k) { qstep(k.v, xr[0], qt, xf); if (k.v & 1) ldn(PRO + (k.v >> 1)); });
    waitv<PRO + 4 - 6>();
    wg_barrier();
    STAMP(1);
    rdw(0, 0); rdw(0, 1); rdw(0, 2); rdw(0, 3);
    u32 pkd[8];
    sfor<56>([&](auto gs) {
        constexpr int i = gs.v >> 3, sl = gs.v & 7;
        constexpr int issued = issued_before(gs.v);
        if (sl == 0 && i < 6) waitx<(issued - (6 * i + 10) > 0 ? issued - (6 * i + 10) : 0)>(xr[i < 6 ? i + 1 : 6]);
        if (sl == 4 && i < 6) { waitv<(issued - (6 * i + 12) > 0 ? issued - (6 * i + 12) : 0)>(); if (i == 5) wg_barrier(); else asm volatile("s_barrier" ::: "memory"); }
        static_assert(!(sl == 0 && i < 6) || issued >= 6 * i + 10, "x not issued yet");
        static_assert(!(sl == 4 && i < 6) || issued >= 6 * i + 12, "W not issued yet");
        SB();
        acc[ssg(i, sl)] = mfma32(xf[4 * i + skk(i, sl)], wf[i][skk(i, sl)][ssg(i, sl)], acc[ssg(i, sl)]);
        SB();
        if (sl < 4) rdw(i, sl + 4);
        else if (i < 6) rdw(i < 6 ? i + 1 : 6, sl - 4);
        if (i < 6) qstep(sl, xr[i < 6 ? i + 1 : 6], qt, xf + 4 * i + 4);
        if constexpr (i == 6 && sl >= 5) { pkd[sl - 5] = pk(phi(acc[0][2 * (sl - 5)]), phi(acc[0][2 * (sl - 5) + 1])); pin(pkd[sl - 5]); }
#pragma unroll
        for (int n = issued; n < issued_before(gs.v + 1); ++n) ldn(n);
        SB();
    });
    STAMP(2);
    __builtin_amdgcn_s_setprio(PA1);
    {
        const u32 vm = r32 == 28 ? 0u : ~0u, vo = r32 == 28 ? 0x3f803f80u : 0u;
        pkd[3] = pk(phi(acc[0][6]), phi(acc[0][7]));
        f32x16 kv = mfma32(u32x4{pkd[0], pkd[1], pkd[2], pkd[3]}, (pk8(acc[1], 0) & vm) | vo, zero16());
        SB();
        kv = mfma32(phi8(acc[0], 1), (pk8(acc[1], 1) & vm) | vo, kv);
#pragma unroll
        for (int rq = 0; rq < 4; ++rq) sts128(sm + A_RED + wave * 4096 + rq * 1024 + lane * 16, quad(kv, rq));
    }
    asm volatile("s_waitcnt vmcnt(7)" : "+v"(tag) :: "memory");
    tag += 1;
    wg_barrier();
    const u32x4 tb = {tag & 255u, ((tag >> 8) + tag) & 255u, ((tag >> 16) + tag) & 255u, ((tag >> 24) + tag) & 255u};
    {
        LAS char* rs = sm + A_RED + tid * 16;
        f32x4 s0 = asf(lds128(rs)), s1 = asf(lds128(rs + 4096)), s2 = asf(lds128(rs + 8192)), s3 = asf(lds128(rs + 12288));
        bst_dev((asu((s0 + s1) + (s2 + s3)) & 0xffffff00u) | tb, rws, WS_KVP + (h * 16 + rb) * 4096 + tid * 16);
    }
    STAMP(3);
    WAITV(1);
    wg_barrier();
    STAMP(4);
    const u32 ov = (8 * wave + (lane >> 3)) * 896 + nb * 128 + (((lane & 7) ^ ((lane >> 4) << 1)) * 16);
    auto ldO1 = [&](int n) { setm0(sm + n * 4096 + wave * 1024); dma<0>(rwo, ov, n * 32 * 896); };
    u32x4 gk[16];
    const u32 kbase = WS_KVP + (h * 16 + rb) * 4096;
    auto ldk = [&](int i) { ldd(gk[i], dws, tid * 16, kbase ^ (i * 4096)); };
    f32x16 aq = zero16();
    {
        LAS char* qsrc = sm + A_WQ + (32 * hh + q) * 64 + blk * 32 + 8 * p;
        u32x4 wq[28];
        auto rdq = [&](int k) { LAS char* s = qsrc + (k >> 2) * 4096 + (k & 3) * 512; wq[k] = trfrag(s, s + 256); };
#pragma unroll
        for (int k = 0; k < 4; ++k) rdq(k);
        SB();
#pragma unroll
        for (int k = 0; k < 28; ++k) {
            aq = mfma32(wq[k], xf[k], aq);
            SB();
            if (k + 4 < 28) rdq(k + 4);
            if (k < NO1) ldO1(k);
            if (k >= GK0) ldk(k - GK0);
            SB();
        }
#pragma unroll
        for (int k = 28 - GK0; k < 16; ++k) ldk(k);
    }
    STAMP(5);
    u32 pqd[8];
#pragma unroll
    for (int k = 0; k < 8; ++k) {
        pqd[k] = pk(phi(aq[2 * k]), phi(aq[2 * k + 1]));
        pin(pqd[k]);
        if (NO1 + 2 * k < 12) ldO1(NO1 + 2 * k);
        if (NO1 + 2 * k + 1 < 12) ldO1(NO1 + 2 * k + 1);
    }
    f32x4 ks[8];
    {
        u32x4 m = {0, 0, 0, 0};
        sfor<8>([&](auto i) {
            asm volatile("s_waitcnt vmcnt(%2)" : "+v"(gk[2 * i.v]), "+v"(gk[2 * i.v + 1]) : "i"(12 - NO1 + 14 - 2 * i.v) : "memory");
            const u32x4 t0 = gk[2 * i.v] ^ tb, t1 = gk[2 * i.v + 1] ^ tb;
            m |= t0 | t1;
            ks[i.v] = asf(t0) + asf(t1);
        });
        if (!__all(((m.x | m.y | m.z | m.w) & 255u) == 0)) {
            auto ok = [&](int i) { u32x4 t = gk[i] ^ tb; return ((t.x | t.y | t.z | t.w) & 255u) == 0; };
            settle<16>(badmask<16>(ok), ok,
                       [&](auto i, u32 bm) { reld<i.v>(gk[i.v], dws, tid * 16, kbase ^ (i.v * 4096), bm); }, [&](int i) { pin(gk[i]); });
#pragma unroll
            for (int i = 0; i < 8; ++i) ks[i] = asf(gk[2 * i] ^ tb) + asf(gk[2 * i + 1] ^ tb);
        }
    }
    STAMP(6);
    {
        f32x4 t = ((ks[0] + ks[1]) + (ks[2] + ks[3])) + ((ks[4] + ks[5]) + (ks[6] + ks[7]));
        const u32 km = (wave == 3 && hh) ? 0u : ~0u;
        sts64(sm + A_KVB + ((wave >> 1) * 64 + lane) * 16 + (wave & 1) * 8, pk(t.x, t.y) & km, pk(t.z, t.w) & km);
    }
    WAITV(0);
    wg_barrier();
    STAMP(7);
    const u32 hm = hh ? 0u : ~0u;
    {
        u32x4 kf0 = lds128(sm + A_KVB + lane * 16), kf1 = lds128(sm + A_KVB + 1024 + lane * 16);
        f32x16 oa = mfma32(kf1, u32x4{pqd[4], pqd[5], pqd[6], pqd[7]}, mfma32(kf0, u32x4{pqd[0], pqd[1], pqd[2], pqd[3]}, zero16()));
        u32 dn = __float_as_uint(oa[12]);
        auto r = __builtin_amdgcn_permlane32_swap(dn, dn, false, false);
        oa *= __builtin_amdgcn_rcpf(__uint_as_float(r[1]) + 1e-6f);
        u32x4 f0 = pk8(oa, 0);
        const u32 o = WS_OX + ((rb * 8 + wave * 2 + blk) * 16 + h) * 1024 + hh * 256 + (lane & 15) * 16;
        bst_dev(f0, rws, o);
        SB();
        u32x4 f1 = pk8(oa, 1);
        f1.z &= hm; f1.w &= hm;
        f1.w |= (tag - halfsum(sum4(f0) + sum4(f1))) & ~hm;
        bst_dev(f1, rws, o + 512);
    }
    cbar();
    STAMP(8);
    if (p3) {
        const int g = lane >> 4, r16 = lane & 15;
        LAS char *os0[4], *os1[4];
#pragma unroll
        for (int t = 0; t < 4; ++t) {
            os0[t] = sm + (16 * hh + 4 * blk + q) * 128 + (((t ^ (2 * blk + (q >> 1))) & 3) * 32) + 8 * p;
            os1[t] = os0[t] + (g == 3 ? 0 : 1024);
        }
        u32x4 wf[16][4];
        auto rdo = [&](int hd, int t) { const int tt = t ^ ((hd & 1) * 2); wf[hd][t] = trfrag(os0[tt] + hd * 28 * 128, os1[tt] + hd * 28 * 128); };
        ldO1(12); ldO1(13);
#pragma unroll
        for (int k = 0; k < 4 * PRE; ++k) rdo(k >> 2, k & 3);
        SB();
        WAITV(0);
        u32x4 go[16];
        auto ldo = [&](int i) { go[i] = bld_dev(rws, lane * 16, WS_OX + ((r64 * 4 + wave) * 16 + i) * 1024); };
#pragma unroll
        for (int i = 0; i < 16; ++i) ldo(i);
        wg_barrier();
        STAMP(9);
        {
            u32 bad = 0;
#pragma unroll
            for (int i = 0; i < 16; i += 4) {
                auto r0 = __builtin_amdgcn_permlane32_swap(sum4(go[i]), sum4(go[i + 2]), false, false);
                auto r1 = __builtin_amdgcn_permlane32_swap(sum4(go[i + 1]), sum4(go[i + 3]), false, false);
                auto r2 = __builtin_amdgcn_permlane16_swap(r0[0] + r0[1], r1[0] + r1[1], false, false);
                const u64 ne = __ballot(r2[0] + r2[1] != tag);
                if (ne) bad |= (((ne & 0xffffull) != 0) | ((ne & 0xffff0000ull) != 0) << 1 | ((ne & 0xffff00000000ull) != 0) << 2 | ((ne >> 48) != 0) << 3) << i;
            }
            settle<16>(bad, [&](int i) { return colsum(sum4(go[i])) == tag; },
                       [&](auto i, u32 bm) { reld<i.v>(go[i.v], dws, lane * 16, WS_OX + ((r64 * 4 + wave) * 16 + i.v) * 1024, bm); }, [&](int i) { pin(go[i]); });
        }
        STAMP(10);
        const u32 wmask = g == 3 ? 0u : ~0u;
        f32x4 oa[4];
        go[0].w &= wmask;
        SB();
#pragma unroll
        for (int k = 0; k < 64; ++k) {
            const int hd = k >> 2, t = k & 3;
            oa[t] = mfma(wf[hd][t], go[hd], hd ? oa[t] : f32x4{0.f, 0.f, 0.f, 0.f});
            SB();
            if (hd + PRE < 16) rdo(PRE + hd, t);
            if (t == 0 && hd < 15) go[hd + 1].w &= wmask;
            SB();
        }
        u32 d[4][2];
#pragma unroll
        for (int t = 0; t < 4; ++t) { d[t][0] = pk(oa[t].x, oa[t].y); d[t][1] = pk(oa[t].z, oa[t].w); }
#pragma unroll
        for (int c = 0; c < 2; ++c) {
#pragma unroll
            for (int t = 0; t < 2; ++t) { auto r = __builtin_amdgcn_permlane32_swap(d[t][c], d[t + 2][c], false, false); d[t][c] = r[0]; d[t + 2][c] = r[1]; }
#pragma unroll
            for (int t = 0; t < 4; t += 2) { auto r = __builtin_amdgcn_permlane16_swap(d[t][c], d[t + 1][c], false, false); d[t][c] = r[0]; d[t + 1][c] = r[1]; }
        }
        u32x4* dst = (u32x4*)(a.out + (size_t)(r64 * 64 + wave * 16 + r16) * 896 + 448 + nb * 64 + 16 * g);
        dst[0] = u32x4{d[0][0], d[0][1], d[1][0], d[1][1]};
        dst[1] = u32x4{d[2][0], d[2][1], d[3][0], d[3][1]};
        if (b == 0 && tid == 0) __hip_atomic_store((GAS u32*)(a.ws + WS_EPA), tag, RLX_AGENT);
        STAMP(11);
    }
    STAMP_FLUSH(b);
}

constexpr int S_TAB = 0;
constexpr int S_KC = 34816;
constexpr int S_P = 47104;
constexpr int S_UT = 52480;
constexpr int S_TR = 61696;

typedef _Float16 h2 __attribute__((ext_vector_type(2)));
typedef _Float16 h8 __attribute__((ext_vector_type(8)));
struct c2 { f32x2 r, i; };
struct hc { h2 r, i; };
DI c2 csq(c2 a) { return c2{a.r * a.r - a.i * a.i, 2.f * a.r * a.i}; }
DI hc hmul(hc a, hc b) { return hc{a.r * b.r - a.i * b.i, a.r * b.i + a.i * b.r}; }
DI hc toh(c2 a) { return hc{__builtin_convertvector(a.r, h2), __builtin_convertvector(a.i, h2)}; }
DI hc tohc(c2 a) { return hc{__builtin_convertvector(a.r, h2), __builtin_convertvector(-a.i, h2)}; }
DI c2 cexp2(f32x2 lr, f32x2 rev, float pw) {
    f32x2 e = lr * pw, f = rev * pw;
    f32x2 m = {__builtin_amdgcn_exp2f(e.x), __builtin_amdgcn_exp2f(e.y)};
    f.x = __builtin_amdgcn_fractf(f.x); f.y = __builtin_amdgcn_fractf(f.y);
    f32x2 c = {__builtin_amdgcn_cosf(f.x), __builtin_amdgcn_cosf(f.y)}, s = {__builtin_amdgcn_sinf(f.x), __builtin_amdgcn_sinf(f.y)};
    return c2{m * c, m * s};
}
DI f32x2 unpk(u32 v) { return f32x2{bflo(v), bfhi(v)}; }
DI f32x4 mfmah(u32x4 a, u32x4 b, f32x4 c) {
    return __builtin_amdgcn_mfma_f32_16x16x32_f16(__builtin_bit_cast(h8, a), __builtin_bit_cast(h8, b), c, 0, 0, 0);
}
DI u32 wavemax(u32 s) {
    s = max(s, (u32)__builtin_amdgcn_update_dpp(0, s, 0x128, 0xf, 0xf, false));
    s = max(s, (u32)__builtin_amdgcn_update_dpp(0, s, 0x124, 0xf, 0xf, false));
    s = max(s, (u32)__builtin_amdgcn_update_dpp(0, s, 0x122, 0xf, 0xf, false));
    s = max(s, (u32)__builtin_amdgcn_update_dpp(0, s, 0x121, 0xf, 0xf, false));
    auto r = __builtin_amdgcn_permlane32_swap(s, s, false, false);
    s = max(r[0], r[1]);
    auto t = __builtin_amdgcn_permlane16_swap(s, s, false, false);
    return max(t[0], t[1]);
}

DI void ssm_role(const Args& a, LAS char* sm, const int bb) {
    STAMP_DECL;
    const int tid = threadIdx.x, lane = tid & 63, wave = __builtin_amdgcn_readfirstlane(tid >> 6);
    const int ct = bb & 7, j = bb >> 3;
    const int ch = wave >> 1, hf = wave & 1, d = ct * 64 + 2 * j + ch;
    const int g = lane >> 4, r16 = lane & 15;
    STAMP(0);
    if (ct == 7) return;
    __builtin_amdgcn_s_setprio(PSV);
    const int trow = 256 * (j >> 2) + 16 * (tid >> 4) + 4 * (j & 3) + 2 * ((tid >> 3) & 1);
    const u32 po = (d * 128 + hf * 64 + lane) * 4, uo = trow * 896 + 128 * ct + (tid & 7) * 16;
    const u32 ldt = a.logdt[d];
    const u32 pA = *(const u32*)((const char*)a.logA + po), pI = *(const u32*)((const char*)a.Aim + po);
    const u32x4 ua = *(const u32x4*)((const char*)a.u + uo), ub = *(const u32x4*)((const char*)a.u + uo + 896);
    const u32 pCr = *(const u32*)((const char*)a.Cre + po), pCi = *(const u32*)((const char*)a.Cim + po);
    const u32 dsk = ((const u32*)a.Dskip)[ct * 32 + j];
    cbar();
    u32 tag = ldepoch(a.ws + WS_EPS + ct * 256);
    const rsrc_t rws = mkrsrc(a.ws, WS_END);
    const u32x4 dws = mkdesc(a.ws, WS_END);
    *(LAS u32*)(sm + S_UT + (tid >> 7) * 4608 + (tid & 127) * 4) = 0;
    const u32 pofs = (ct * 32 + j) * 8192 + tid * 16;
    const u32 cofs = (ct * 32 + (tid >> 4)) * 8192 + j * 256 + (tid & 15) * 16;
    const u32 pcs = r16 < 2 ? ((ct * 32 + j) * 4 + wave) * 128 + g * 8 + (tid & 1) * 4 : 0x80000000u;
    const u32 ccs = ((ct * 32 + (tid >> 4)) * 4 + ((j >> 2) & 3)) * 128 + (j & 3) * 8 + (j >> 4) * 4;
    auto pubcs = [&](u32x4 v0, u32x4 v1, u32 cbase) {
        const u32 s0 = rowsum(sum4(v0)), s1 = rowsum(sum4(v1));
        bst32_dev(tag - (r16 ? s1 : s0), rws, cbase + pcs);
    };
    const int w = 16 + r16 - 8 * g;
    u32 sel[4][4], msk[4], pb[4];
    {
        u32 y[4];
#pragma unroll
        for (int e = 0; e < 4; ++e) {
            y[e] = (u32)(w + 64 - 2 * e) * 0x10001u - 0x10000u;
            msk[e] = hf ? 0u : (((y[e] >> 6) & 0x10001u) ^ 0x10001u) * 0xffffu;
        }
        pb[0] = w - 7;
#pragma unroll
        for (int s = 1; s < 4; ++s) {
            u32 r[4];
#pragma unroll
            for (int e = (s == 1 ? 2 : 0); e < 4; ++e)
                r[e] = ((y[e] + ((1u << (s - 1)) - 1u) * 0x10001u + ((y[e] >> s) & 0x10001u)) >> s) & 0x00ff00ffu;
            const u32 base = r[3] >> 16;
            pb[s] = base - (64 >> s);
#pragma unroll
            for (int e = (s == 1 ? 2 : 0); e < 4; ++e) sel[s][e] = (((r[e] - base * 0x10001u) ^ 0x10001u) & 0xfffffu) * 0x202u + 0x01000100u;
        }
        asm volatile("" : "+v"(sel[1][2]), "+v"(sel[1][3]), "+v"(sel[2][0]), "+v"(sel[2][1]), "+v"(sel[2][2]), "+v"(sel[2][3]), "+v"(sel[3][0]), "+v"(sel[3][1]), "+v"(sel[3][2]), "+v"(sel[3][3]));
        asm volatile("" : "+v"(msk[0]), "+v"(msk[1]), "+v"(msk[2]), "+v"(msk[3]));
    }
    LAS u32* const R = (LAS u32*)(sm + S_P + ch * 2688) + 655;
    LAS u32 *P0 = R - (128 * hf + (int)pb[0]) - 102, *P1 = R - (256 + 64 * hf + (int)pb[1]) - 132, *P2 = R - (384 + 32 * hf + (int)pb[2]) - 130, *P3 = R - (512 + 16 * hf + (int)pb[3]) - 108;
    LAS char* ubase = sm + S_UT + ch * 4608 + 480 + r16 * 32 + g * 16 - 256 * hf - 192;
    asm volatile("" : "+v"(P0), "+v"(P1), "+v"(P2), "+v"(P3), "+v"(ubase));
    LAS char* trw = sm + S_TR + (tid & 7) * 288 + (tid >> 3) * 8;
    LAS char* trr = sm + S_TR + (((tid >> 4) & 3) * 8 + (tid >> 6)) * 288 + (tid & 15) * 16;
    LAS char* tab = sm + S_TAB + wave * 8704;
    LAS char* tw = tab + lane * 8;
    LAS char* tr = tab + r16 * 544 + g * 16;
    LAS char* kc = sm + S_KC + wave * 3072;
    SB();
#define EMIT2(row, v0, v1) do { SB(); sts64(tw + (row) * 544, __builtin_bit_cast(u32, (v0).r), __builtin_bit_cast(u32, (v0).i)); \
                                sts64(tw + ((row) + 1) * 544, __builtin_bit_cast(u32, (v1).r), __builtin_bit_cast(u32, (v1).i)); SB(); } while (0)
#define KRD() do { SB(); _Pragma("unroll") for (int ks = 0; ks < 8; ++ks) gf[ks] = lds128(tr + ks * 64); cbar(); SB(); } while (0)
#define KM2(k) do { ka = mfmah(gf[2 * (k)], ff[2 * (k)], (k) ? ka : f32x4{0.f, 0.f, 0.f, 0.f}); ka = mfmah(gf[2 * (k) + 1], ff[2 * (k) + 1], ka); } while (0)
    const float L2E = 1.44269504089f;
    const float dt = rbf(__builtin_amdgcn_exp2f(bflo(ldt) * L2E));
    f32x2 lr = unpk(pA) * L2E;
    lr = unpk(pk(__builtin_amdgcn_exp2f(lr.x), __builtin_amdgcn_exp2f(lr.y))) * dt * -L2E;
    const f32x2 rev = unpk(pI) * dt * 0.15915494309f;
    SB();
    sts64(trw, ua.x, ub.x); sts64(trw + 8 * 288, ua.y, ub.y); sts64(trw + 16 * 288, ua.z, ub.z); sts64(trw + 24 * 288, ua.w, ub.w);
    SB();
    hc W[8], F[16], G[17], H, t0, t1;
    u32x4 ff[8], gf[8];
    f32x4 ka;
    c2 e = cexp2(lr, -rev, 8.f);
    F[1] = toh(e); e = csq(e); F[2] = toh(e);
    asm volatile("s_waitcnt vmcnt(0)" : "+v"(tag) :: "memory");
    tag += 1;
    wg_barrier();
    const u32x4 up0 = lds128(trr), up1 = lds128(trr + 4 * 288);
    F[0] = hc{h2{1.f16, 1.f16}, h2{0.f16, 0.f16}};
    EMIT2(0, F[0], F[1]);
    e = csq(e); F[4] = toh(e); F[3] = hmul(F[1], F[2]);
    bst_dev(up0, rws, WS_UX + pofs);
    EMIT2(2, F[2], F[3]);
    e = csq(e); F[8] = toh(e); F[5] = hmul(F[4], F[1]);
    EMIT2(4, F[4], F[5]);
    F[6] = hmul(F[4], F[2]); F[7] = hmul(F[4], F[3]); F[9] = hmul(F[8], F[1]);
    bst_dev(up1, rws, WS_UX + pofs + 4096);
    EMIT2(6, F[6], F[7]);
    pubcs(up0, up1, WS_UC);
    cbar();
    __builtin_amdgcn_s_setprio(PS0);
    STAMP(1);
    EMIT2(8, F[8], F[9]);
    F[10] = hmul(F[8], F[2]); F[11] = hmul(F[8], F[3]);
    e = cexp2(lr, rev, 1.f); W[1] = toh(e);
    EMIT2(10, F[10], F[11]);
    F[12] = hmul(F[8], F[4]); F[13] = hmul(F[8], F[5]);
    e = csq(e); W[2] = toh(e);
    EMIT2(12, F[12], F[13]);
    F[14] = hmul(F[8], F[6]); F[15] = hmul(F[8], F[7]);
    e = csq(e); W[4] = toh(e);
    EMIT2(14, F[14], F[15]);
    u32 ce = max(max(pCr & 0x7fffu, (pCr >> 16) & 0x7fffu), max(pCi & 0x7fffu, (pCi >> 16) & 0x7fffu));
    ce = min(max(wavemax(ce) >> 7, 1u), 253u);
    const float cdn = __uint_as_float((254u - ce) << 23), cup = __uint_as_float(ce << 23) * (2.f * dt);
    G[0] = toh(c2{unpk(pCr) * cdn, unpk(pCi) * cdn});
    t1 = hmul(G[0], W[1]);
    SB();
#pragma unroll
    for (int ks = 0; ks < 8; ++ks) ff[ks] = lds128(tr + ks * 64);
    cbar();
    EMIT2(0, G[0], t1);
    W[3] = hmul(W[1], W[2]); t0 = hmul(G[0], W[2]); t1 = hmul(G[0], W[3]);
    EMIT2(2, t0, t1);
    W[5] = hmul(W[4], W[1]); t0 = hmul(G[0], W[4]); t1 = hmul(G[0], W[5]);
    EMIT2(4, t0, t1);
    W[6] = hmul(W[4], W[2]); W[7] = hmul(W[4], W[3]); t0 = hmul(G[0], W[6]); t1 = hmul(G[0], W[7]);
    EMIT2(6, t0, t1);
    e = cexp2(lr, rev, 128.f); H = toh(e);
    G[1] = hmul(G[0], H); t1 = hmul(G[1], W[1]);
    EMIT2(8, G[1], t1);
    t0 = hmul(G[1], W[2]); t1 = hmul(G[1], W[3]);
    e = csq(e); H = toh(e);
    EMIT2(10, t0, t1);
    t0 = hmul(G[1], W[4]); t1 = hmul(G[1], W[5]); G[2] = hmul(G[0], H);
    EMIT2(12, t0, t1);
    t0 = hmul(G[1], W[6]); t1 = hmul(G[1], W[7]); G[3] = hmul(G[1], H);
    EMIT2(14, t0, t1);
    t1 = hmul(G[2], W[2]);
    e = csq(e); H = toh(e);
    KRD();
    EMIT2(0, G[2], t1);
    t0 = hmul(G[2], W[4]); t1 = hmul(G[2], W[6]); G[4] = hmul(G[0], H);
    EMIT2(2, t0, t1);
    t1 = hmul(G[3], W[2]); G[5] = hmul(G[1], H); KM2(0);
    EMIT2(4, G[3], t1);
    t0 = hmul(G[3], W[4]); t1 = hmul(G[3], W[6]); KM2(1);
    EMIT2(6, t0, t1);
    t1 = hmul(G[4], W[4]); G[6] = hmul(G[2], H); KM2(2);
    EMIT2(8, G[4], t1);
    t1 = hmul(G[5], W[4]); G[7] = hmul(G[3], H); KM2(3);
    EMIT2(10, G[5], t1);
    STAMP(2);
    u32x4 uu[2]; u32 uc[2];
    __builtin_amdgcn_s_setprio(PSV);
#pragma unroll
    for (int i = 0; i < 2; ++i) { uu[i] = bld_dev(rws, cofs, WS_UX + i * 16 * 8192); uc[i] = bld32_dev(rws, ccs, WS_UC + i * 16 * 512); }
    cbar();
    __builtin_amdgcn_s_setprio(PS0);
    t1 = hmul(G[6], W[4]);
    e = csq(e); H = toh(e);
    EMIT2(12, G[6], t1);
    t1 = hmul(G[7], W[4]); G[8] = hmul(G[0], H);
    sts128(kc + (128 * (g >> 1) + 4 * (g & 1) + 8 * r16) * 4, asu(ka * cup));
    EMIT2(14, G[7], t1);
    G[9] = hmul(G[1], H); G[10] = hmul(G[2], H);
    KRD();
    EMIT2(0, G[8], G[9]);
    G[11] = hmul(G[3], H); G[12] = hmul(G[4], H); G[13] = hmul(G[5], H);
    EMIT2(2, G[10], G[11]);
    G[14] = hmul(G[6], H); KM2(0); KM2(1);
    EMIT2(4, G[12], G[13]);
    G[15] = hmul(G[7], H); G[16] = hmul(G[8], H); KM2(2); KM2(3);
    EMIT2(6, G[14], G[15]);
    sts64(tw + 8 * 544, __builtin_bit_cast(u32, G[16].r), __builtin_bit_cast(u32, G[16].i));
    KRD();
    {
        LAS char* k0 = kc + (g < 2 ? 256 + 64 * g + 4 * r16 : 384 + 64 * (g - 2) + 2 * r16) * 4;
        u32x4 kb = asu(ka * cup);
        sts64(k0, kb.x, kb.y);
        sts64(k0 + (g < 2 ? 8 : 128), kb.z, kb.w);
    }
    SB();
    KM2(0); KM2(1); KM2(2); KM2(3);
    pin(ka);
    {
        auto ok = [&](int i) { return rowsum(sum4(uu[i])) + uc[i] == tag; };
        settle<2>(badmask<2>(ok), ok,
                  [&](auto i, u32 bm) { reld<i.v>(uu[i.v], dws, cofs, WS_UX + i.v * 16 * 8192, bm); reld32<i.v>(uc[i.v], dws, ccs, WS_UC + i.v * 16 * 512, bm); }, [&](int i) { pin(uu[i]); pin(uc[i]); });
    }
    {
        LAS float* k0 = (LAS float*)(kc + (512 + 64 * g + r16) * 4);
        k0[0] = ka.x * cup; k0[16] = ka.y * cup; k0[32] = ka.z * cup; k0[48] = ka.w * cup;
    }
#pragma unroll
    for (int i = 0; i < 2; ++i) {
        LAS char* ud = sm + S_UT + 512 + (4 * i + wave) * 512 + r16 * 32 + g * 8;
        sts64(ud, __builtin_amdgcn_perm(uu[i].y, uu[i].x, 0x05040100u), __builtin_amdgcn_perm(uu[i].w, uu[i].z, 0x05040100u));
        sts64(ud + 4608, __builtin_amdgcn_perm(uu[i].y, uu[i].x, 0x07060302u), __builtin_amdgcn_perm(uu[i].w, uu[i].z, 0x07060302u));
    }
    wg_barrier();
    STAMP(3);
    if (j == 0 && tid == 0) __hip_atomic_store((GAS u32*)(a.ws + WS_EPS + ct * 256), tag, RLX_AGENT);
    u32x4 U[2][8];
    auto rdU = [&](int it, int bq) { U[it & 1][bq] = lds128(ubase + 512 * bq + 192 - 64 * it); };
    {
        LAS char* k0 = sm + S_KC + ch * 6144;
        LAS char* Rb = sm + S_P + ch * 2688;
        const int t = tid & 127, e1 = min(t + 128, 167);
        f32x4 a0 = asf(lds128(k0 + t * 16)), b0 = asf(lds128(k0 + 3072 + t * 16)), a1 = asf(lds128(k0 + e1 * 16)), b1 = asf(lds128(k0 + 3072 + e1 * 16));
        float n0 = *(LAS float*)(k0 + t * 16 + 16) + *(LAS float*)(k0 + 3072 + t * 16 + 16);
        float n1 = *(LAS float*)(k0 + e1 * 16 + 16) + *(LAS float*)(k0 + 3072 + e1 * 16 + 16);
#pragma unroll
        for (int bq = 0; bq < 8; ++bq) rdU(3, bq);
        a0 += b0; a1 += b1;
        float p0 = a0.x, p1 = a0.y, p2 = a0.z, p3 = a0.w, q0 = a1.x, q1 = a1.y, q2 = a1.z, q3 = a1.w;
        asm volatile("" : "+v"(p0), "+v"(p1), "+v"(p2), "+v"(p3), "+v"(q0), "+v"(q1), "+v"(q2), "+v"(q3));
        sts128(Rb + (163 - t) * 16, u32x4{pk(n0, p3), pk(p3, p2), pk(p2, p1), pk(p1, p0)});
        u32x4 v1 = {pk(n1, q3), pk(q3, q2), pk(q2, q1), pk(q1, q0)};
        if (t >= 36) v1 = u32x4{0, 0, 0, 0};
        if (t < 39) sts128(Rb + (t < 36 ? 35 - t : 129 + t) * 16, v1);
        if (t == 0) sts128(Rb + 164 * 16, u32x4{pk(p0, 0.f), 0, 0, 0});
    }
    wg_barrier();
    STAMP(4);
    __builtin_amdgcn_s_setprio(PS1);
    f32x4 y[8];
    {
#define Q0(i) P0[102 - (i)]
#define Q1(i) P1[132 - (i)]
#define Q2(i) P2[130 - (i)]
#define Q3(i) P3[108 - (i)]
#define PERM __builtin_amdgcn_perm
        u32x4 T[2][8];
        u32 raw[11], rx[9];
        auto rdT = [&](int it, int k) {
            u32x4& t0 = T[it & 1][0];
            if (k == 0) { t0.x = Q0(32 * it + 6); t0.y = Q0(32 * it + 4); }
            if (k == 1) { t0.z = Q0(32 * it + 2); t0.w = Q0(32 * it); }
            if (k == 2) { raw[0] = Q1(16 * it); raw[1] = Q1(16 * it + 2); raw[2] = Q1(16 * it + 4); }
            if (k == 3) { raw[3] = Q2(8 * it); raw[4] = Q2(8 * it + 2); }
            if (k == 4) { raw[5] = Q2(8 * it + 64); raw[6] = Q2(8 * it + 66); }
            if (k == 5) { raw[7] = Q3(4 * it); raw[8] = Q3(4 * it + 32); }
            if (k == 6) { raw[9] = Q3(4 * it + 64); raw[10] = Q3(4 * it + 96); }
        };
        auto mkT = [&](int it, int aa, int e) {
            u32 v;
            if (aa == 1) v = e < 2 ? PERM(raw[2], raw[1], sel[1][2 + e]) : PERM(raw[1], raw[0], sel[1][e]);
            else if (aa < 4) v = PERM(raw[2 * aa], raw[2 * aa - 1], sel[2][e]);
            else v = PERM(raw[3 + aa], raw[3 + aa], sel[3][e]);
            T[it & 1][aa][e] = v;
        };
        auto rdX = [&](int k) {
            if (k == 0) { rx[0] = P0[102 - 262]; rx[1] = P0[102 - 260]; }
            if (k == 1) { rx[2] = P0[102 - 258]; rx[3] = P0[102 - 256]; }
            if (k == 2) { rx[4] = Q1(128); rx[5] = Q1(130); rx[6] = Q1(132); }
            if (k == 3) { rx[7] = Q2(128); rx[8] = Q2(130); }
        };
        auto fixT = [&](int k, int e) {
            const int aa = 1 << k;
            u32 lo = k == 0 ? rx[e] : k == 1 ? (e < 2 ? PERM(rx[6], rx[5], sel[1][2 + e]) : PERM(rx[5], rx[4], sel[1][e])) : PERM(rx[8], rx[7], sel[2][e]);
            T[0][aa][e] = (lo & msk[e]) | (T[0][aa][e] & ~msk[e]);
        };
#pragma unroll
        for (int k = 0; k < 7; ++k) rdT(3, k);
        SB();
#pragma unroll
        for (int it = 3; it >= 0; --it) {
            int sl = 0;
#pragma unroll
            for (int aa = 0; aa < 8; ++aa)
#pragma unroll
                for (int bq = 0; bq + aa < 8; ++bq, ++sl) {
                    y[aa + bq] = mfma(T[it & 1][aa], U[it & 1][bq], (it == 3 && aa == 0) ? f32x4{0.f, 0.f, 0.f, 0.f} : y[aa + bq]);
                    SB();
                    if (it == 3) {
                        if (sl < 14) { mkT(3, 1 + (sl >> 1), 2 * (sl & 1)); mkT(3, 1 + (sl >> 1), 2 * (sl & 1) + 1); }
                        else if (sl < 21) { rdT(2, sl - 14); rdU(2, sl - 14); }
                        else if (sl == 21) rdU(2, 7);
                        else { mkT(2, 1 + ((sl - 22) >> 1), 2 * (sl & 1)); mkT(2, 1 + ((sl - 22) >> 1), 2 * (sl & 1) + 1); }
                    } else if (it > 0) {
                        if (sl < 7) { rdT(it - 1, sl); rdU(it - 1, sl); }
                        else if (sl == 7) rdU(it - 1, 7);
                        if (it == 1 && sl >= 7 && sl < 11) rdX(sl - 7);
                        if (sl >= 10 && sl < 24) { mkT(it - 1, 1 + ((sl - 10) >> 1), 2 * (sl & 1)); mkT(it - 1, 1 + ((sl - 10) >> 1), 2 * (sl & 1) + 1); }
                        if (it == 1 && sl >= 24) fixT((sl - 24) >> 2, (sl - 24) & 3);
                    }
                    SB();
                }
        }
    }
    STAMP(5);
    __builtin_amdgcn_s_setprio(PSV);
    {
        LAS char* yd = sm + S_TAB + wave * 8192 + lane * 16;
#pragma unroll
        for (int i = 0; i < 8; ++i) sts128(yd + i * 1024, asu(y[i]));
    }
    wg_barrier();
    {
        const float d0 = bflo(dsk), d1 = bfhi(dsk);
        u32x4 o[2];
#pragma unroll
        for (int i = 0; i < 2; ++i) {
            LAS char* ys = sm + S_TAB + (wave + 4 * i) * 1024 + lane * 16;
            f32x4 y0 = asf(lds128(ys)) + asf(lds128(ys + 8192));
            f32x4 y1 = asf(lds128(ys + 16384)) + asf(lds128(ys + 24576));
            o[i] = u32x4{pk(y0.x + bflo(uu[i].x) * d0, y1.x + bfhi(uu[i].x) * d1), pk(y0.y + bflo(uu[i].y) * d0, y1.y + bfhi(uu[i].y) * d1),
                         pk(y0.z + bflo(uu[i].z) * d0, y1.z + bfhi(uu[i].z) * d1), pk(y0.w + bflo(uu[i].w) * d0, y1.w + bfhi(uu[i].w) * d1)};
        }
        bst_dev(o[0], rws, WS_YX + pofs);
        bst_dev(o[1], rws, WS_YX + pofs + 4096);
        pubcs(o[0], o[1], WS_YC);
    }
    STAMP(6);
    WAITV(0);
    STAMP(7);
    {
        u32x4 v[2]; u32 vc[2];
        auto ldy = [&](int i) { v[i] = bld_dev(rws, cofs, WS_YX + i * 16 * 8192); vc[i] = bld32_dev(rws, ccs, WS_YC + i * 16 * 512); };
        ldy(0); ldy(1);
        cbar();
        auto ok = [&](int i) { return rowsum(sum4(v[i])) + vc[i] == tag; };
        settle<2>(badmask<2>(ok), ok,
                  [&](auto i, u32 bm) { reld<i.v>(v[i.v], dws, cofs, WS_YX + i.v * 16 * 8192, bm); reld32<i.v>(vc[i.v], dws, ccs, WS_YC + i.v * 16 * 512, bm); }, [&](int i) { pin(v[i]); pin(vc[i]); });
        STAMP(8);
        sts128(trr, v[0]); sts128(trr + 4 * 288, v[1]);
        wg_barrier();
        u32x2 t0 = *(LAS u32x2*)trw, t1 = *(LAS u32x2*)(trw + 8 * 288), t2 = *(LAS u32x2*)(trw + 16 * 288), t3 = *(LAS u32x2*)(trw + 24 * 288);
        u32x4* dst = (u32x4*)(a.out + (size_t)trow * 896 + 64 * ct) + (tid & 7);
        dst[0] = u32x4{t0.x, t1.x, t2.x, t3.x};
        dst[112] = u32x4{t0.y, t1.y, t2.y, t3.y};
    }
    STAMP(9);
    STAMP_FLUSH(256 + bb);
}

#if defined(__HIP_DEVICE_COMPILE__)
#define NOPK __attribute__((target("no-packed-fp32-ops")))
#else
#define NOPK
#endif
__global__ NOPK __launch_bounds__(256, 2) void fused_kernel(Args a) {
    __shared__ __attribute__((aligned(16))) char smem[81920];
    LAS char* sm = (LAS char*)smem;
    const bool is_attn = SSM_FIRST ? blockIdx.x >= 256 : blockIdx.x < 256;
    const int b = blockIdx.x & 255;
    if (is_attn) attn_role(a, sm, b);
    else ssm_role(a, sm, b);
}

extern "C" void kernel_launch(void* const* d_in, const int* in_sizes, int n_in,
                              void* d_out, int out_size, void* d_ws, size_t ws_size,
                              hipStream_t stream) {
    Args a;
    a.u = (const u16*)d_in[0]; a.logA = (const u16*)d_in[1]; a.Aim = (const u16*)d_in[2];
    a.Cre = (const u16*)d_in[3]; a.Cim = (const u16*)d_in[4]; a.logdt = (const u16*)d_in[5];
    a.Dskip = (const u16*)d_in[6]; a.x = (const u16*)d_in[7]; a.wqkv = (const u16*)d_in[8];
    a.wo = (const u16*)d_in[9]; a.out = (u16*)d_out; a.ws = (char*)d_ws + ((ws_size / 2) & ~(size_t)0xfffff);
    fused_kernel<<<512, 256, 0, stream>>>(a);
}
```

```cpp
#include <hip/hip_runtime.h>
#include <stdint.h>

typedef unsigned short u16;
typedef unsigned int u32;
typedef unsigned long long u64;
typedef __bf16 bf16x8 __attribute__((ext_vector_type(8)));
typedef __bf16 bf16x2 __attribute__((ext_vector_type(2)));
typedef short s16x4 __attribute__((ext_vector_type(4)));
typedef float f32x4 __attribute__((ext_vector_type(4)));
typedef float f32x2 __attribute__((ext_vector_type(2)));
typedef float f32x16 __attribute__((ext_vector_type(16)));
typedef u32 u32x2 __attribute__((ext_vector_type(2)));
typedef u32 u32x4 __attribute__((ext_vector_type(4)));
#define LAS __attribute__((address_space(3)))
#define GAS __attribute__((address_space(1)))
#define DI __device__ __forceinline__
#define RLX_AGENT __ATOMIC_RELAXED, __HIP_MEMORY_SCOPE_AGENT
#define SB() __builtin_amdgcn_sched_barrier(0)
#define WAITV(n) asm volatile("s_waitcnt vmcnt(" #n ")" ::: "memory")
#define KEEP(x) asm volatile("" :: "v"(x))
typedef __amdgpu_buffer_rsrc_t rsrc_t;
#define EXP 0
#define PA0 1
#define PA1 2
#define PS0 1
#define PS1 0
#define POLL_SLEEP 2
#define ABL 0
#define GK0 18
#define PRO 10
#define QS0 44
#define PSV 3
#define NO1 4
#define PRE 10
#define TCH 5
#define SPREAD 1
#define CPF 0
#define ATTN_LIKELY 1
#define CODE_BYTES 28160
#define MAPH 8
#define CA 2
#define CB 1
#define RDA 4
#define OAUX 16
#define OSTAG 1
#define THR 0
#define REMAP 1
#define LATE 1
#define SSM_FIRST 0

#define STAMP(i) do {} while (0)
#define STAMP_DECL u32 dbgv_
#define DBG(i) dbgv_
#define STAMP_FLUSH(slot) do {} while (0)

constexpr u32 WS_EPA = 0;
constexpr u32 WS_EPS = 256;
constexpr u32 WS_UC  = 65536;
constexpr u32 WS_YC  = WS_UC + 131072;
constexpr u32 WS_KVP = 524288;
constexpr u32 WS_OX = WS_KVP + (1u << 20);
constexpr u32 WS_UX  = WS_OX + (2u << 20);
constexpr u32 WS_YX  = WS_UX + (2u << 20);
constexpr u32 WS_END = WS_YX + (2u << 20);
constexpr u32 WS_DBG = 16u << 20;
constexpr int SPIN_CAP = 1 << 16;

struct Args {
    const u16 *u, *logA, *Aim, *Cre, *Cim, *logdt, *Dskip, *x, *wqkv, *wo;
    u16* out;
    char* ws;
};

DI f32x4 mfma(u32x4 a, u32x4 b, f32x4 c) {
    return __builtin_amdgcn_mfma_f32_16x16x32_bf16(__builtin_bit_cast(bf16x8, a), __builtin_bit_cast(bf16x8, b), c, 0, 0, 0);
}
DI u32 pk2(f32x2 v) { return __builtin_bit_cast(u32, __builtin_convertvector(v, bf16x2)); }
DI u32 pk(float lo, float hi) { f32x2 v = {lo, hi}; return pk2(v); }
DI float bflo(u32 v) { return __uint_as_float(v << 16); }
DI float bfhi(u32 v) { return __uint_as_float(v & 0xffff0000u); }
DI float rbf(float f) { return bflo(pk(f, f)); }
DI rsrc_t mkrsrc(const void* p, u32 bytes) { return __builtin_amdgcn_make_buffer_rsrc((void*)p, 0, bytes, 0x00020000); }
DI u32x4 bld_dev(rsrc_t r, u32 voff, u32 soff) { return __builtin_amdgcn_raw_buffer_load_b128(r, voff, soff, 16); }
DI u32 bld32_dev(rsrc_t r, u32 voff, u32 soff) { return __builtin_amdgcn_raw_buffer_load_b32(r, voff, soff, 16); }
DI void bst_dev(u32x4 v, rsrc_t r, u32 voff) { __builtin_amdgcn_raw_buffer_store_b128(v, r, voff, 0, 16); }
DI void bst32_dev(u32 v, rsrc_t r, u32 voff) { __builtin_amdgcn_raw_buffer_store_b32(v, r, voff, 0, 16); }
DI u32x4 mkdesc(const void* p, u32 bytes) { u64 v = (u64)p; return u32x4{(u32)v, (u32)(v >> 32) & 0xffffu, bytes, 0x00020000u}; }
DI void setm0(LAS char* dst) { asm volatile("s_mov_b32 m0, %0\n\ts_nop 0" :: "s"((u32)(uintptr_t)dst) : "memory"); }
template <int OFF>
DI void dma(u32x4 desc, u32 voff, u32 soff) {
    asm volatile("buffer_load_dwordx4 %0, %1, %2 offen offset:%3 lds" :: "v"(voff), "s"(desc), "s"(soff), "i"(OFF) : "memory");
}
DI u32x4 lds128(LAS char* p) { return *(LAS u32x4*)p; }
DI void sts128(LAS char* p, u32x4 v) { *(LAS u32x4*)p = v; }
DI void sts64(LAS char* p, u32 lo, u32 hi) { u32x2 v = {lo, hi}; *(LAS u32x2*)p = v; }
DI u32x4 trfrag(LAS char* p0, LAS char* p1) {
    u32x2 a = __builtin_bit_cast(u32x2, __builtin_amdgcn_ds_read_tr16_b64_v4i16((LAS s16x4*)p0));
    u32x2 b = __builtin_bit_cast(u32x2, __builtin_amdgcn_ds_read_tr16_b64_v4i16((LAS s16x4*)p1));
    u32x4 r = {a.x, a.y, b.x, b.y};
    return r;
}
DI void cbar() { asm volatile("" ::: "memory"); }
DI void wg_barrier() { asm volatile("s_waitcnt lgkmcnt(0)\n\ts_barrier" ::: "memory"); }
DI f32x4 asf(u32x4 v) { return __builtin_bit_cast(f32x4, v); }
DI u32x4 asu(f32x4 v) { return __builtin_bit_cast(u32x4, v); }
DI u32 sum4(u32x4 v) { return v.x + v.y + v.z + v.w; }
DI u32 rowsum(u32 s) {
    s += __builtin_amdgcn_update_dpp(0, s, 0x128, 0xf, 0xf, false);
    s += __builtin_amdgcn_update_dpp(0, s, 0x124, 0xf, 0xf, false);
    s += __builtin_amdgcn_update_dpp(0, s, 0x122, 0xf, 0xf, false);
    s += __builtin_amdgcn_update_dpp(0, s, 0x121, 0xf, 0xf, false);
    return s;
}
DI u32 sum32(u32 s) {
    s = rowsum(s);
    auto t = __builtin_amdgcn_permlane16_swap(s, s, false, false);
    return t[0] + t[1];
}
DI u32 colsum(u32 s) {
    auto r = __builtin_amdgcn_permlane32_swap(s, s, false, false);
    s = r[0] + r[1];
    auto t = __builtin_amdgcn_permlane16_swap(s, s, false, false);
    return t[0] + t[1];
}
DI float phi(float t) {
    float e = __builtin_amdgcn_exp2f(t * 1.44269504089f);
    return __builtin_amdgcn_fmed3f(t, 1e-6f, __builtin_inff()) + __builtin_amdgcn_fmed3f(e, 0.f, 1.f);
}
DI u32x4 phi8(const f32x16& a, int s) { return u32x4{pk(phi(a[8 * s]), phi(a[8 * s + 1])), pk(phi(a[8 * s + 2]), phi(a[8 * s + 3])), pk(phi(a[8 * s + 4]), phi(a[8 * s + 5])), pk(phi(a[8 * s + 6]), phi(a[8 * s + 7]))}; }
DI u32x4 pk8(const f32x16& a, int s) { return u32x4{pk(a[8 * s], a[8 * s + 1]), pk(a[8 * s + 2], a[8 * s + 3]), pk(a[8 * s + 4], a[8 * s + 5]), pk(a[8 * s + 6], a[8 * s + 7])}; }
DI u32x4 quad(const f32x16& a, int r) { return asu(f32x4{a[4 * r], a[4 * r + 1], a[4 * r + 2], a[4 * r + 3]}); }
DI f32x16 zero16() { f32x16 z; for (int i = 0; i < 16; ++i) z[i] = 0.f; return z; }
DI f32x16 mfma32(u32x4 a, u32x4 b, f32x16 c) {
    return __builtin_amdgcn_mfma_f32_32x32x16_bf16(__builtin_bit_cast(bf16x8, a), __builtin_bit_cast(bf16x8, b), c, 0, 0, 0);
}
DI u32 halfsum(u32 s) { auto r = __builtin_amdgcn_permlane32_swap(s, s, false, false); return r[0] + r[1]; }
DI float colsumf(float f) {
    auto r = __builtin_amdgcn_permlane32_swap(__float_as_uint(f), __float_as_uint(f), false, false);
    f = __uint_as_float(r[0]) + __uint_as_float(r[1]);
    auto t = __builtin_amdgcn_permlane16_swap(__float_as_uint(f), __float_as_uint(f), false, false);
    return __uint_as_float(t[0]) + __uint_as_float(t[1]);
}
DI u32 ldepoch(const char* p) {
    u32 v;
    asm volatile("global_load_dword %0, %1, %2 sc1" : "=v"(v) : "v"(0u), "s"(p) : "memory");
    return v;
}
template <int BIT> DI void reld(u32x4& v, u32x4 desc, u32 voff, u32 soff, u32 mask) {
    asm volatile("s_bitcmp1_b32 %4, %5\n\ts_cbranch_scc0 1f\n\tbuffer_load_dwordx4 %0, %1, %2, %3 offen sc1\n1:"
                 : "+v"(v) : "v"(voff), "s"(desc), "s"(soff), "s"(mask), "i"(BIT) : "scc", "memory");
}
template <int BIT> DI void reld32(u32& v, u32x4 desc, u32 voff, u32 soff, u32 mask) {
    asm volatile("s_bitcmp1_b32 %4, %5\n\ts_cbranch_scc0 1f\n\tbuffer_load_dword %0, %1, %2, %3 offen sc1\n1:"
                 : "+v"(v) : "v"(voff), "s"(desc), "s"(soff), "s"(mask), "i"(BIT) : "scc", "memory");
}
DI u32 touch(u32x4 desc, u32 voff) { u32 v; asm volatile("buffer_load_dword %0, %1, %2, 0 offen" : "=v"(v) : "v"(voff), "s"(desc) : "memory"); return v; }
template <class T> DI void pin(T& v) { asm volatile("" : "+v"(v) :: "memory"); }
template <int I> struct IC { static constexpr int v = I; };
template <int N, int I = 0, class F> DI void sfor(F f) { if constexpr (I < N) { f(IC<I>{}); sfor<N, I + 1>(f); } }
template <int N, class OK, class LD, class PIN>
DI u32 settle(u32 bad, OK ok, LD ld, PIN pn) {
    u32 dbg = __builtin_popcount(bad) << 8;
#pragma unroll 1
    for (int sp = 0; bad != 0 && sp < SPIN_CAP; ++sp) {
        ++dbg;
        __builtin_amdgcn_s_sleep(POLL_SLEEP);
        sfor<N>([&](auto i) { ld(i, bad); });
        WAITV(0);
        sfor<N>([&](auto i) { pn(i.v); });
        sfor<N>([&](auto i) { if ((bad >> i.v & 1) && __all(ok(i.v))) bad &= ~(1u << i.v); });
    }
    return dbg;
}
template <int N, class OK> DI u32 badmask(OK ok) { u32 bad = 0; sfor<N>([&](auto i) { if (!__all(ok(i.v))) bad |= 1u << i.v; }); return bad; }

constexpr int A_RED = 32768, A_WQ = 49152, A_KVB = 77824;

template <int OFF>
DI void ldx(u32x4& v, u32x4 desc, u32 voff, u32 soff) {
    asm volatile("buffer_load_dwordx4 %0, %1, %2, %3 offen offset:%4" : "=v"(v) : "v"(voff), "s"(desc), "s"(soff), "i"(OFF) : "memory");
}
DI void ldd(u32x4& v, u32x4 desc, u32 voff, u32 soff) {
    asm volatile("buffer_load_dwordx4 %0, %1, %2, %3 offen sc1" : "=v"(v) : "v"(voff), "s"(desc), "s"(soff) : "memory");
}
template <int N> DI void waitx(u32x4* x) { asm volatile("s_waitcnt vmcnt(%4)" : "+v"(x[0]), "+v"(x[1]), "+v"(x[2]), "+v"(x[3]) : "i"(N) : "memory"); }
#define QSEL(PERMS, o, v, k, mask) asm volatile("s_mov_b64 vcc, %12\n\t" \
    "v_cndmask_b32_dpp %0, %4, %8, vcc " PERMS "\n\tv_cndmask_b32_dpp %1, %5, %9, vcc " PERMS "\n\t" \
    "v_cndmask_b32_dpp %2, %6, %10, vcc " PERMS "\n\tv_cndmask_b32_dpp %3, %7, %11, vcc " PERMS \
    : "=&v"(o[0]), "=&v"(o[1]), "=&v"(o[2]), "=&v"(o[3]) \
    : "v"(v[0]), "v"(v[1]), "v"(v[2]), "v"(v[3]), "v"(k[0]), "v"(k[1]), "v"(k[2]), "v"(k[3]), "s"(mask) : "vcc")
DI void qstep(int k, const u32x4* x, u32 (&t)[4][4], u32x4* o) {
#define QP1 "quad_perm:[1,0,3,2] row_mask:0xf bank_mask:0xf"
#define QP2 "quad_perm:[2,3,0,1] row_mask:0xf bank_mask:0xf"
    u32 r[4];
    if (k == 0) QSEL(QP1, t[0], x[1], x[0], 0x5555555555555555ull);
    if (k == 1) QSEL(QP1, t[2], x[3], x[2], 0x5555555555555555ull);
    if (k == 2) QSEL(QP1, t[1], x[0], x[1], 0xaaaaaaaaaaaaaaaaull);
    if (k == 3) QSEL(QP1, t[3], x[2], x[3], 0xaaaaaaaaaaaaaaaaull);
    if (k == 4) QSEL(QP2, r, t[2], t[0], 0x3333333333333333ull);
    if (k == 5) QSEL(QP2, r, t[3], t[1], 0x3333333333333333ull);
    if (k == 6) QSEL(QP2, r, t[0], t[2], 0xccccccccccccccccull);
    if (k == 7) QSEL(QP2, r, t[1], t[3], 0xccccccccccccccccull);
    if (k >= 4) o[k - 4] = u32x4{r[0], r[1], r[2], r[3]};
}

template <int N> DI void waitv() { asm volatile("s_waitcnt vmcnt(%0)" :: "i"(N) : "memory"); }
constexpr int issued_before(int gs) {
    int n = PRO + 4 + gs;
    if (n <= 44) return n;
    int q = gs - QS0;
    return 44 + (q < 0 ? 0 : q > 7 ? 7 : q);
}

DI void attn_role(const Args& a, LAS char* sm, const int b) {
    STAMP_DECL;
    const int tid = threadIdx.x, lane = tid & 63, wave = __builtin_amdgcn_readfirstlane(tid >> 6);
    const int xcd = b & 7, j = b >> 3;
    const u64 mt = (xcd & 1) ? (j < 16 ? (j < 8 ? 0x1d1b191509050301ull : 0xc0a080706040200ull) : (j < 24 ? 0x1817161412100f0eull : 0x13110d0b1f1e1c1aull))
                             : (j < 16 ? (j < 8 ? 0x1e1c1a160a060402ull : 0xb09080705030100ull) : (j < 24 ? 0x1817151311100f0dull : 0x14120e0c1f1d1b19ull));
    const int me = (int)(mt >> (8 * (j & 7))) & 31;
    const int h = (xcd & 1) * 8 + (me & 7), rb = (xcd >> 1) * 4 + (me >> 3);
    const bool late = j >= 28;
    const int c0 = (CA * rb + CB * h) % 7;
    const int r32 = lane & 31, hh = lane >> 5, blk = (lane >> 4) & 1, q = (lane >> 2) & 3, p = lane & 3;
    const u32x4 rx = mkdesc(a.x, 2048 * 896), rw = mkdesc(a.wqkv, 448 * 2688), rwo = mkdesc(a.wo, 448 * 896);
    const rsrc_t rws = mkrsrc(a.ws, WS_END);
    const u32x4 dws = mkdesc(a.ws, WS_END);
    STAMP(0);
    __builtin_amdgcn_s_setprio(PA0);
    u32 tag = ldepoch(a.ws + WS_EPA);
    u32 tch0 = 0, tch1 = 0;
    cbar();
    const int wch = (lane & 7) ^ (blk << 2);
    const int sh = h == 15;
    const int wun = ((4 * blk + p + sh) & 7) * 8;
    const u32 wv = (16 * wave + (lane >> 3)) * 2688 + (1 + (wch >> 2)) * 896 + h * 56 - 8 * sh + (wch & 3) * 16;
    const u32 xv = (rb * 128 + wave * 32 + (lane & 28)) * 896 + hh * 64 + p * 16;
    auto kchunk = [&](int i) { int kc = i + c0; return kc >= 7 ? kc - 7 : kc; };
    u32x4 xr[7][4], xf[28];
    auto ldX1 = [&](int i, int m) {
        if (m == 0) ldx<0>(xr[i][0], rx, xv, kchunk(i) * 128);
        if (m == 1) ldx<896>(xr[i][1], rx, xv, kchunk(i) * 128);
        if (m == 2) ldx<1792>(xr[i][2], rx, xv, kchunk(i) * 128);
        if (m == 3) ldx<2688>(xr[i][3], rx, xv, kchunk(i) * 128);
    };
    const u32 qv = (16 * wave + (lane >> 2)) * 2688 + h * 56 - 8 * sh + p * 16;
    auto ldQ1 = [&](int n) { setm0(sm + A_WQ + n * 4096 + wave * 1024); dma<0>(rw, qv, kchunk(n) * 64 * 2688); };
    auto ldn = [&](int n) {
        if (n >= 51) return;
        if (n >= 44) { ldQ1(n - 44); return; }
        if (n == 42) { tch0 = touch(dws, !(TCH & 1) ? 0x80000000u : lane < 8 ? WS_KVP + (h * 16 + rb) * 2048 + wave * 512 + (lane & 3) * 128
                                : lane < 24 ? WS_OX + ((rb * 8 + wave * 2 + blk) * 16 + h) * 1024 + (lane & 7) * 128 : 0x80000000u); return; }
        if (n == 43) { tch1 = touch(mkdesc(a.out, 2048 * 1792), (TCH & 2) && lane < 16 && j < 28 ? ((xcd * 4 + j / 7) * 64 + wave * 16 + lane) * 1792 + 896 + (j % 7) * 128 : 0x80000000u); return; }
        const int i = n / 6, m = n % 6;
        if (m < 4) ldX1(i, m);
        if (m == 4) { setm0(sm + (i % 6) * 8192 + wave * 2048); dma<0>(rw, wv, kchunk(i) * 64 * 2688); }
        if (m == 5) dma<1024>(rw, wv, kchunk(i) * 64 * 2688 + 8 * 2688 - 1024);
    };
#pragma unroll
    for (int n = 0; n < PRO; ++n) ldn(n);
    cbar();

    LAS char* wsrc[2];
#pragma unroll
    for (int sg = 0; sg < 2; ++sg) wsrc[sg] = sm + (32 * hh + q) * 128 + ((sg ^ (q >> 1)) * 64) + wun;

    const bool p3 = j < 28;
    const int r64 = p3 ? xcd * 4 + j / 7 : 0, nb = j % 7;
    f32x16 acc[2] = {zero16(), zero16()};
    u32 qt[4][4];
    u32x4 wf[7][4][2];
    auto skk = [](int i, int sl) { return i < 6 ? sl >> 1 : sl & 3; };
    auto ssg = [](int i, int sl) { return i < 6 ? sl & 1 : sl >> 2; };
    auto rdw = [&](int i, int sl) { LAS char* s = wsrc[ssg(i, sl)] + (i % 6) * 8192 + skk(i, sl) * 1024; wf[i][skk(i, sl)][ssg(i, sl)] = trfrag(s, s + 512); };
    waitx<PRO - 4>(xr[0]);
    sfor<8>([&](auto k) { qstep(k.v, xr[0], qt, xf); if (k.v & 1) ldn(PRO + (k.v >> 1)); });
    waitv<PRO + 4 - 6>();
    wg_barrier();
    STAMP(1);
#pragma unroll
    for (int sl = 0; sl < RDA; ++sl) rdw(0, sl);
    u32 pkd[8];
    sfor<56>([&](auto gs) {
        constexpr int i = gs.v >> 3, sl = gs.v & 7;
        constexpr int issued = issued_before(gs.v);
        constexpr int BS = 8 - RDA;
        if (THR && sl == 0 && !((0x2a54 >> h) & 1)) __builtin_amdgcn_s_sleep(THR);
        if (sl == 0 && i < 6) waitx<(issued - (6 * i + 10) > 0 ? issued - (6 * i + 10) : 0)>(xr[i < 6 ? i + 1 : 6]);
        if (sl == BS && i < 6) { waitv<(issued - (6 * i + 12) > 0 ? issued - (6 * i + 12) : 0)>(); if (i == 5) wg_barrier(); else asm volatile("s_barrier" ::: "memory"); }
        static_assert(!(sl == 0 && i < 6) || issued >= 6 * i + 10, "x not issued yet");
        static_assert(!(sl == BS && i < 6) || issued >= 6 * i + 12, "W not issued yet");
        SB();
        acc[ssg(i, sl)] = mfma32(xf[4 * i + skk(i, sl)], wf[i][skk(i, sl)][ssg(i, sl)], acc[ssg(i, sl)]);
        SB();
        if (sl < BS) rdw(i, sl + RDA);
        else if (i < 6) rdw(i < 6 ? i + 1 : 6, sl - BS);
        if (i < 6) qstep(sl, xr[i < 6 ? i + 1 : 6], qt, xf + 4 * i + 4);
        if constexpr (i == 6 && sl >= 5) { pkd[sl - 5] = pk(phi(acc[0][2 * (sl - 5)]), phi(acc[0][2 * (sl - 5) + 1])); pin(pkd[sl - 5]); }
#pragma unroll
        for (int n = issued; n < issued_before(gs.v + 1); ++n) ldn(n);
        SB();
    });
    STAMP(2);
    __builtin_amdgcn_s_setprio(PA1);
    {
        const u32 vm = r32 == 28 ? 0u : ~0u, vo = r32 == 28 ? 0x3f803f80u : 0u;
        pkd[3] = pk(phi(acc[0][6]), phi(acc[0][7]));
        f32x16 kv = mfma32(u32x4{pkd[0], pkd[1], pkd[2], pkd[3]}, (pk8(acc[1], 0) & vm) | vo, zero16());
        SB();
        kv = mfma32(phi8(acc[0], 1), (pk8(acc[1], 1) & vm) | vo, kv);
#pragma unroll
        for (int rq = 0; rq < 4; ++rq) sts128(sm + A_RED + wave * 4096 + rq * 1024 + lane * 16, quad(kv, rq));
    }
    asm volatile("s_waitcnt vmcnt(9)" : "+v"(tag) :: "memory");
    tag += 1;
    wg_barrier();
    const u32 hm = hh ? 0u : ~0u, cm = r32 == 31 ? 0u : ~0u;
    if (wave < 2) {
        LAS char* rs = sm + A_RED + wave * 2048 + lane * 16;
        f32x4 lo = (asf(lds128(rs)) + asf(lds128(rs + 4096))) + (asf(lds128(rs + 8192)) + asf(lds128(rs + 12288)));
        f32x4 hi = (asf(lds128(rs + 1024)) + asf(lds128(rs + 5120))) + (asf(lds128(rs + 9216)) + asf(lds128(rs + 13312)));
        u32x4 v = {pk(lo.x, lo.y), pk(lo.z, lo.w), pk(hi.x, hi.y), pk(hi.z, hi.w) & cm};
        v.w |= (tag - sum32(sum4(v))) & ~cm;
        bst_dev(v, rws, WS_KVP + (h * 16 + rb) * 2048 + tid * 16);
    }
    STAMP(3);
    if (wave < 2) WAITV(1); else WAITV(0);
    KEEP(tch0); KEEP(tch1);
    wg_barrier();
    STAMP(4);
    const u32 ov = (8 * wave + (lane >> 3)) * 896 + nb * 128 + (((lane & 7) ^ ((lane >> 4) << 1)) * 16);
    auto ldO1 = [&](int n) {
        if (OSTAG && n < 12) { n += 3 * (r64 & 3); n = n >= 12 ? n - 12 : n; }
        setm0(sm + n * 4096 + wave * 1024); dma<0>(rwo, ov, n * 32 * 896);
    };
    u32x4 gk[8];
    const u32 kbase = WS_KVP + (h * 16 + (wave >> 1) * 8 + (rb & 7)) * 2048, kvo = (tid & 127) * 16;
    auto ldk = [&](int i) { if (i < 8) ldd(gk[i], dws, kvo, kbase ^ (i * 2048)); };
    f32x16 aq = zero16();
    {
        LAS char* qsrc = sm + A_WQ + (32 * hh + q) * 64 + wun;
        u32x4 wq[28];
        auto rdq = [&](int k) { LAS char* s = qsrc + (k >> 2) * 4096 + (k & 3) * 512; wq[k] = trfrag(s, s + 256); };
#pragma unroll
        for (int k = 0; k < 4; ++k) rdq(k);
        SB();
#pragma unroll
        for (int k = 0; k < 28; ++k) {
            aq = mfma32(wq[k], xf[k], aq);
            SB();
            if (k + 4 < 28) rdq(k + 4);
            if (k < NO1 && !late) ldO1(k);
            if (k >= GK0) ldk(k - GK0);
            SB();
        }
#pragma unroll
        for (int k = 28 - GK0; k < 8; ++k) ldk(k);
    }
    STAMP(5);
    u32 pqd[8];
#pragma unroll
    for (int k = 0; k < 8; ++k) {
        pqd[k] = pk(phi(aq[2 * k]), phi(aq[2 * k + 1]));
        if (k >= 6) pqd[k] &= hm;
        pin(pqd[k]);
        if (NO1 + 2 * k < 12 && !late) { ldO1(NO1 + 2 * k); ldO1(NO1 + 2 * k + 1); }
    }
    {
        if (late) WAITV(0);
        asm volatile("s_waitcnt vmcnt(%8)" : "+v"(gk[0]), "+v"(gk[1]), "+v"(gk[2]), "+v"(gk[3]), "+v"(gk[4]), "+v"(gk[5]), "+v"(gk[6]), "+v"(gk[7]) : "i"(12 - NO1) : "memory");
        u32 t = 0;
#pragma unroll
        for (int i = 0; i < 8; ++i) t += sum4(gk[i]);
        if (!__all(sum32(t) == 8u * tag)) {
            auto ok = [&](int i) { return sum32(sum4(gk[i])) == tag; };
            DBG(0) = settle<8>(badmask<8>(ok), ok, [&](auto i, u32 bm) { reld<i.v>(gk[i.v], dws, kvo, kbase ^ (i.v * 2048), bm); }, [&](int i) { pin(gk[i]); });
        }
    }
    STAMP(6);
    {
        u32x4 v;
#pragma unroll
        for (int c = 0; c < 4; ++c) {
            const float lo = ((bflo(gk[0][c]) + bflo(gk[1][c])) + (bflo(gk[2][c]) + bflo(gk[3][c]))) + ((bflo(gk[4][c]) + bflo(gk[5][c])) + (bflo(gk[6][c]) + bflo(gk[7][c])));
            const float hi = ((bfhi(gk[0][c]) + bfhi(gk[1][c])) + (bfhi(gk[2][c]) + bfhi(gk[3][c]))) + ((bfhi(gk[4][c]) + bfhi(gk[5][c])) + (bfhi(gk[6][c]) + bfhi(gk[7][c])));
            v[c] = pk(lo, hi);
        }
        sts128(sm + A_KVB + tid * 16, v);
    }
    WAITV(0);
    wg_barrier();
    STAMP(7);
    {
        u32x4 kf[4];
#pragma unroll
        for (int i = 0; i < 4; ++i) kf[i] = lds128(sm + A_KVB + i * 1024 + lane * 16);
        const u32x4 pq0 = {pqd[0], pqd[1], pqd[2], pqd[3]}, pq1 = {pqd[4], pqd[5], pqd[6], pqd[7]};
        f32x16 oa = mfma32(kf[3], pq1, mfma32(kf[2], pq0, mfma32(kf[1], pq1, mfma32(kf[0], pq0, zero16()))));
        u32 dn = __float_as_uint(oa[12]);
        auto r = __builtin_amdgcn_permlane32_swap(dn, dn, false, false);
        oa *= __builtin_amdgcn_rcpf(__uint_as_float(r[1]) + 1e-6f);
        u32x4 f0 = pk8(oa, 0);
        const u32 o = WS_OX + ((rb * 8 + wave * 2 + blk) * 16 + h) * 1024 + hh * 256 + (lane & 15) * 16;
        bst_dev(f0, rws, o);
        SB();
        u32x4 f1 = pk8(oa, 1);
        f1.z &= hm; f1.w &= hm;
        f1.w |= (tag - halfsum(sum4(f0) + sum4(f1))) & ~hm;
        bst_dev(f1, rws, o + 512);
    }
    cbar();
    STAMP(8);
    if (p3) {
        const int g = lane >> 4, r16 = lane & 15;
        LAS char *os0[4], *os1[4];
#pragma unroll
        for (int t = 0; t < 4; ++t) {
            os0[t] = sm + (16 * hh + 4 * blk + q) * 128 + (((t ^ (2 * blk + (q >> 1))) & 3) * 32) + 8 * p;
            os1[t] = os0[t] + (g == 3 ? 0 : 1024);
        }
        u32x4 wf[16][4];
        auto rdo = [&](int hd, int t) { const int tt = t ^ ((hd & 1) * 2); wf[hd][t] = trfrag(os0[tt] + hd * 28 * 128, os1[tt] + hd * 28 * 128); };
        ldO1(12); ldO1(13);
#pragma unroll
        for (int k = 0; k < 4 * PRE; ++k) rdo(k >> 2, k & 3);
        SB();
        WAITV(0);
        u32x4 go[16];
        auto ldo = [&](int i) { go[i] = bld_dev(rws, lane * 16, WS_OX + ((r64 * 4 + wave) * 16 + i) * 1024); };
#pragma unroll
        for (int i = 0; i < 16; ++i) ldo(i);
        wg_barrier();
        STAMP(9);
        {
            u32 bad = 0;
#pragma unroll
            for (int i = 0; i < 16; i += 4) {
                auto r0 = __builtin_amdgcn_permlane32_swap(sum4(go[i]), sum4(go[i + 2]), false, false);
                auto r1 = __builtin_amdgcn_permlane32_swap(sum4(go[i + 1]), sum4(go[i + 3]), false, false);
                auto r2 = __builtin_amdgcn_permlane16_swap(r0[0] + r0[1], r1[0] + r1[1], false, false);
                const u64 ne = __ballot(r2[0] + r2[1] != tag);
                if (ne) bad |= (((ne & 0xffffull) != 0) | ((ne & 0xffff0000ull) != 0) << 1 | ((ne & 0xffff00000000ull) != 0) << 2 | ((ne >> 48) != 0) << 3) << i;
            }
            DBG(1) = settle<16>(bad, [&](int i) { return colsum(sum4(go[i])) == tag; },
                       [&](auto i, u32 bm) { reld<i.v>(go[i.v], dws, lane * 16, WS_OX + ((r64 * 4 + wave) * 16 + i.v) * 1024, bm); }, [&](int i) { pin(go[i]); });
        }
        STAMP(10);
        const u32 wmask = g == 3 ? 0u : ~0u;
        f32x4 oa[4];
        go[0].w &= wmask;
        SB();
#pragma unroll
        for (int k = 0; k < 64; ++k) {
            const int hd = k >> 2, t = k & 3;
            oa[t] = mfma(wf[hd][t], go[hd], hd ? oa[t] : f32x4{0.f, 0.f, 0.f, 0.f});
            SB();
            if (hd + PRE < 16) rdo(PRE + hd, t);
            if (t == 0 && hd < 15) go[hd + 1].w &= wmask;
            SB();
        }
        {
            LAS char* ob = sm + 57344 + wave * 2304;
#pragma unroll
            for (int t = 0; t < 4; ++t) sts64(ob + r16 * 144 + t * 32 + g * 8, pk(oa[t].x, oa[t].y), pk(oa[t].z, oa[t].w));
            cbar();
            const rsrc_t ro = mkrsrc(a.out, 2048 * 1792);
            const u32 oo = (r64 * 64 + wave * 16 + (lane >> 3)) * 1792 + 896 + nb * 128 + (lane & 7) * 16;
            const u32x4 v0 = lds128(ob + (lane >> 3) * 144 + (lane & 7) * 16), v1 = lds128(ob + (8 + (lane >> 3)) * 144 + (lane & 7) * 16);
            __builtin_amdgcn_raw_buffer_store_b128(v0, ro, oo, 0, OAUX);
            __builtin_amdgcn_raw_buffer_store_b128(v1, ro, oo + 8 * 1792, 0, OAUX);
        }
        if (b == 0 && tid == 0) __hip_atomic_store((GAS u32*)(a.ws + WS_EPA), tag, RLX_AGENT);
        STAMP(11);
    }
    STAMP_FLUSH(b);
}

constexpr int S_TAB = 0;
constexpr int S_KC = 34816;
constexpr int S_P = 47104;
constexpr int S_UT = 52480;
constexpr int S_TR = 61696;

typedef _Float16 h2 __attribute__((ext_vector_type(2)));
typedef _Float16 h8 __attribute__((ext_vector_type(8)));
struct c2 { f32x2 r, i; };
struct hc { h2 r, i; };
DI c2 csq(c2 a) { return c2{a.r * a.r - a.i * a.i, 2.f * a.r * a.i}; }
DI hc hmul(hc a, hc b) { return hc{a.r * b.r - a.i * b.i, a.r * b.i + a.i * b.r}; }
DI hc toh(c2 a) { return hc{__builtin_convertvector(a.r, h2), __builtin_convertvector(a.i, h2)}; }
DI hc tohc(c2 a) { return hc{__builtin_convertvector(a.r, h2), __builtin_convertvector(-a.i, h2)}; }
DI c2 cexp2(f32x2 lr, f32x2 rev, float pw) {
    f32x2 e = lr * pw, f = rev * pw;
    f32x2 m = {__builtin_amdgcn_exp2f(e.x), __builtin_amdgcn_exp2f(e.y)};
    f.x = __builtin_amdgcn_fractf(f.x); f.y = __builtin_amdgcn_fractf(f.y);
    f32x2 c = {__builtin_amdgcn_cosf(f.x), __builtin_amdgcn_cosf(f.y)}, s = {__builtin_amdgcn_sinf(f.x), __builtin_amdgcn_sinf(f.y)};
    return c2{m * c, m * s};
}
DI f32x2 unpk(u32 v) { return f32x2{bflo(v), bfhi(v)}; }
DI f32x4 mfmah(u32x4 a, u32x4 b, f32x4 c) {
    return __builtin_amdgcn_mfma_f32_16x16x32_f16(__builtin_bit_cast(h8, a), __builtin_bit_cast(h8, b), c, 0, 0, 0);
}
DI u32 wavemax(u32 s) {
    s = max(s, (u32)__builtin_amdgcn_update_dpp(0, s, 0x128, 0xf, 0xf, false));
    s = max(s, (u32)__builtin_amdgcn_update_dpp(0, s, 0x124, 0xf, 0xf, false));
    s = max(s, (u32)__builtin_amdgcn_update_dpp(0, s, 0x122, 0xf, 0xf, false));
    s = max(s, (u32)__builtin_amdgcn_update_dpp(0, s, 0x121, 0xf, 0xf, false));
    auto r = __builtin_amdgcn_permlane32_swap(s, s, false, false);
    s = max(r[0], r[1]);
    auto t = __builtin_amdgcn_permlane16_swap(s, s, false, false);
    return max(t[0], t[1]);
}

DI void ssm_role(const Args& a, LAS char* sm, const int bb) {
    STAMP_DECL;
    const int tid = threadIdx.x, lane = tid & 63, wave = __builtin_amdgcn_readfirstlane(tid >> 6);
    const int sidx = (bb & 7) * 28 + (bb >> 3) - 4;
    const int ct = sidx >> 5, j = sidx & 31;
    const int ch = wave >> 1, hf = wave & 1, d = ct * 64 + 2 * j + ch;
    const int g = lane >> 4, r16 = lane & 15;
    STAMP(0);
    __builtin_amdgcn_s_setprio(PSV);
    const int trow = 256 * (j >> 2) + 16 * (tid >> 4) + 4 * (j & 3) + 2 * ((tid >> 3) & 1);
    const u32 po = (d * 128 + hf * 64 + lane) * 4, uo = trow * 896 + 128 * ct + (tid & 7) * 16;
    const u32 ldt = a.logdt[d];
    const u32 pA = *(const u32*)((const char*)a.logA + po), pI = *(const u32*)((const char*)a.Aim + po);
    const u32x4 ua = *(const u32x4*)((const char*)a.u + uo), ub = *(const u32x4*)((const char*)a.u + uo + 896);
    const u32 pCr = *(const u32*)((const char*)a.Cre + po), pCi = *(const u32*)((const char*)a.Cim + po);
    const u32 dsk = ((const u32*)a.Dskip)[ct * 32 + j];
    cbar();
    u32 tag = ldepoch(a.ws + WS_EPS + ct * 256);
    const rsrc_t rws = mkrsrc(a.ws, WS_END);
    const u32x4 dws = mkdesc(a.ws, WS_END);
    u32 tch0 = 0, tch1 = 0;
    *(LAS u32*)(sm + S_UT + (tid >> 7) * 4608 + (tid & 127) * 4) = 0;
    const u32 pofs = (ct * 32 + j) * 8192 + tid * 16;
    const u32 cofs = (ct * 32 + (tid >> 4)) * 8192 + j * 256 + (tid & 15) * 16;
    const u32 pcs = r16 < 2 ? ((ct * 32 + j) * 4 + wave) * 128 + g * 8 + (tid & 1) * 4 : 0x80000000u;
    const u32 ccs = ((ct * 32 + (tid >> 4)) * 4 + ((j >> 2) & 3)) * 128 + (j & 3) * 8 + (j >> 4) * 4;
    auto pubcs = [&](u32x4 v0, u32x4 v1, u32 cbase) {
        const u32 s0 = rowsum(sum4(v0)), s1 = rowsum(sum4(v1));
        bst32_dev(tag - (r16 ? s1 : s0), rws, cbase + pcs);
    };
    const int w = 16 + r16 - 8 * g;
    u32 sel[4][4], msk[4], pb[4];
    {
        u32 y[4];
#pragma unroll
        for (int e = 0; e < 4; ++e) {
            y[e] = (u32)(w + 64 - 2 * e) * 0x10001u - 0x10000u;
            msk[e] = hf ? 0u : (((y[e] >> 6) & 0x10001u) ^ 0x10001u) * 0xffffu;
        }
        pb[0] = w - 7;
#pragma unroll
        for (int s = 1; s < 4; ++s) {
            u32 r[4];
#pragma unroll
            for (int e = (s == 1 ? 2 : 0); e < 4; ++e)
                r[e] = ((y[e] + ((1u << (s - 1)) - 1u) * 0x10001u + ((y[e] >> s) & 0x10001u)) >> s) & 0x00ff00ffu;
            const u32 base = r[3] >> 16;
            pb[s] = base - (64 >> s);
#pragma unroll
            for (int e = (s == 1 ? 2 : 0); e < 4; ++e) sel[s][e] = (((r[e] - base * 0x10001u) ^ 0x10001u) & 0xfffffu) * 0x202u + 0x01000100u;
        }
        asm volatile("" : "+v"(sel[1][2]), "+v"(sel[1][3]), "+v"(sel[2][0]), "+v"(sel[2][1]), "+v"(sel[2][2]), "+v"(sel[2][3]), "+v"(sel[3][0]), "+v"(sel[3][1]), "+v"(sel[3][2]), "+v"(sel[3][3]));
        asm volatile("" : "+v"(msk[0]), "+v"(msk[1]), "+v"(msk[2]), "+v"(msk[3]));
    }
    LAS u32* const R = (LAS u32*)(sm + S_P + ch * 2688) + 655;
    LAS u32 *P0 = R - (128 * hf + (int)pb[0]) - 102, *P1 = R - (256 + 64 * hf + (int)pb[1]) - 132, *P2 = R - (384 + 32 * hf + (int)pb[2]) - 130, *P3 = R - (512 + 16 * hf + (int)pb[3]) - 108;
    LAS char* ubase = sm + S_UT + ch * 4608 + 480 + r16 * 32 + g * 16 - 256 * hf - 192;
    asm volatile("" : "+v"(P0), "+v"(P1), "+v"(P2), "+v"(P3), "+v"(ubase));
    LAS char* trw = sm + S_TR + (tid & 7) * 288 + (tid >> 3) * 8;
    LAS char* trr = sm + S_TR + (((tid >> 4) & 3) * 8 + (tid >> 6)) * 288 + (tid & 15) * 16;
    LAS char* tab = sm + S_TAB + wave * 8704;
    LAS char* tw = tab + lane * 8;
    LAS char* tr = tab + r16 * 544 + g * 16;
    LAS char* kc = sm + S_KC + wave * 3072;
    SB();
#define EMIT2(row, v0, v1) do { SB(); sts64(tw + (row) * 544, __builtin_bit_cast(u32, (v0).r), __builtin_bit_cast(u32, (v0).i)); \
                                sts64(tw + ((row) + 1) * 544, __builtin_bit_cast(u32, (v1).r), __builtin_bit_cast(u32, (v1).i)); SB(); } while (0)
#define KRD() do { SB(); _Pragma("unroll") for (int ks = 0; ks < 8; ++ks) gf[ks] = lds128(tr + ks * 64); cbar(); SB(); } while (0)
#define KM2(k) do { ka = mfmah(gf[2 * (k)], ff[2 * (k)], (k) ? ka : f32x4{0.f, 0.f, 0.f, 0.f}); ka = mfmah(gf[2 * (k) + 1], ff[2 * (k) + 1], ka); } while (0)
    const float L2E = 1.44269504089f;
    const float dt = rbf(__builtin_amdgcn_exp2f(bflo(ldt) * L2E));
    f32x2 lr = unpk(pA) * L2E;
    lr = unpk(pk(__builtin_amdgcn_exp2f(lr.x), __builtin_amdgcn_exp2f(lr.y))) * dt * -L2E;
    const f32x2 rev = unpk(pI) * dt * 0.15915494309f;
    SB();
    sts64(trw, ua.x, ub.x); sts64(trw + 8 * 288, ua.y, ub.y); sts64(trw + 16 * 288, ua.z, ub.z); sts64(trw + 24 * 288, ua.w, ub.w);
    SB();
    hc W[8], F[16], G[17], H, t0, t1;
    u32x4 ff[8], gf[8];
    f32x4 ka;
    c2 e = cexp2(lr, -rev, 8.f);
    F[1] = toh(e); e = csq(e); F[2] = toh(e);
    asm volatile("s_waitcnt vmcnt(0)" : "+v"(tag) :: "memory");
    tag += 1;
    wg_barrier();
    const u32x4 up0 = lds128(trr), up1 = lds128(trr + 4 * 288);
    F[0] = hc{h2{1.f16, 1.f16}, h2{0.f16, 0.f16}};
    EMIT2(0, F[0], F[1]);
    e = csq(e); F[4] = toh(e); F[3] = hmul(F[1], F[2]);
    bst_dev(up0, rws, WS_UX + pofs);
    EMIT2(2, F[2], F[3]);
    e = csq(e); F[8] = toh(e); F[5] = hmul(F[4], F[1]);
    EMIT2(4, F[4], F[5]);
    F[6] = hmul(F[4], F[2]); F[7] = hmul(F[4], F[3]); F[9] = hmul(F[8], F[1]);
    bst_dev(up1, rws, WS_UX + pofs + 4096);
    EMIT2(6, F[6], F[7]);
    pubcs(up0, up1, WS_UC);
    cbar();
    __builtin_amdgcn_s_setprio(PS0);
    STAMP(1);
    EMIT2(8, F[8], F[9]);
    F[10] = hmul(F[8], F[2]); F[11] = hmul(F[8], F[3]);
    e = cexp2(lr, rev, 1.f); W[1] = toh(e);
    EMIT2(10, F[10], F[11]);
    F[12] = hmul(F[8], F[4]); F[13] = hmul(F[8], F[5]);
    e = csq(e); W[2] = toh(e);
    EMIT2(12, F[12], F[13]);
    F[14] = hmul(F[8], F[6]); F[15] = hmul(F[8], F[7]);
    e = csq(e); W[4] = toh(e);
    EMIT2(14, F[14], F[15]);
    u32 ce = max(max(pCr & 0x7fffu, (pCr >> 16) & 0x7fffu), max(pCi & 0x7fffu, (pCi >> 16) & 0x7fffu));
    ce = min(max(wavemax(ce) >> 7, 1u), 253u);
    const float cdn = __uint_as_float((254u - ce) << 23), cup = __uint_as_float(ce << 23) * (2.f * dt);
    G[0] = toh(c2{unpk(pCr) * cdn, unpk(pCi) * cdn});
    t1 = hmul(G[0], W[1]);
    SB();
#pragma unroll
    for (int ks = 0; ks < 8; ++ks) ff[ks] = lds128(tr + ks * 64);
    cbar();
    EMIT2(0, G[0], t1);
    W[3] = hmul(W[1], W[2]); t0 = hmul(G[0], W[2]); t1 = hmul(G[0], W[3]);
    EMIT2(2, t0, t1);
    W[5] = hmul(W[4], W[1]); t0 = hmul(G[0], W[4]); t1 = hmul(G[0], W[5]);
    EMIT2(4, t0, t1);
    W[6] = hmul(W[4], W[2]); W[7] = hmul(W[4], W[3]); t0 = hmul(G[0], W[6]); t1 = hmul(G[0], W[7]);
    EMIT2(6, t0, t1);
    e = cexp2(lr, rev, 128.f); H = toh(e);
    G[1] = hmul(G[0], H); t1 = hmul(G[1], W[1]);
    EMIT2(8, G[1], t1);
    t0 = hmul(G[1], W[2]); t1 = hmul(G[1], W[3]);
    e = csq(e); H = toh(e);
    EMIT2(10, t0, t1);
    t0 = hmul(G[1], W[4]); t1 = hmul(G[1], W[5]); G[2] = hmul(G[0], H);
    EMIT2(12, t0, t1);
    t0 = hmul(G[1], W[6]); t1 = hmul(G[1], W[7]); G[3] = hmul(G[1], H);
    EMIT2(14, t0, t1);
    t1 = hmul(G[2], W[2]);
    e = csq(e); H = toh(e);
    KRD();
    EMIT2(0, G[2], t1);
    t0 = hmul(G[2], W[4]); t1 = hmul(G[2], W[6]); G[4] = hmul(G[0], H);
    EMIT2(2, t0, t1);
    t1 = hmul(G[3], W[2]); G[5] = hmul(G[1], H); KM2(0);
    EMIT2(4, G[3], t1);
    t0 = hmul(G[3], W[4]); t1 = hmul(G[3], W[6]); KM2(1);
    EMIT2(6, t0, t1);
    t1 = hmul(G[4], W[4]); G[6] = hmul(G[2], H); KM2(2);
    EMIT2(8, G[4], t1);
    t1 = hmul(G[5], W[4]); G[7] = hmul(G[3], H); KM2(3);
    EMIT2(10, G[5], t1);
    STAMP(2);
    u32x4 uu[2]; u32 uc[2];
    __builtin_amdgcn_s_setprio(PSV);
#pragma unroll
    for (int i = 0; i < 2; ++i) { uu[i] = bld_dev(rws, cofs, WS_UX + i * 16 * 8192); uc[i] = bld32_dev(rws, ccs, WS_UC + i * 16 * 512); }
    cbar();
    __builtin_amdgcn_s_setprio(PS0);
    t1 = hmul(G[6], W[4]);
    e = csq(e); H = toh(e);
    EMIT2(12, G[6], t1);
    t1 = hmul(G[7], W[4]); G[8] = hmul(G[0], H);
    sts128(kc + (128 * (g >> 1) + 4 * (g & 1) + 8 * r16) * 4, asu(ka * cup));
    EMIT2(14, G[7], t1);
    G[9] = hmul(G[1], H); G[10] = hmul(G[2], H);
    KRD();
    EMIT2(0, G[8], G[9]);
    G[11] = hmul(G[3], H); G[12] = hmul(G[4], H); G[13] = hmul(G[5], H);
    EMIT2(2, G[10], G[11]);
    G[14] = hmul(G[6], H); KM2(0); KM2(1);
    EMIT2(4, G[12], G[13]);
    G[15] = hmul(G[7], H); G[16] = hmul(G[8], H); KM2(2); KM2(3);
    EMIT2(6, G[14], G[15]);
    sts64(tw + 8 * 544, __builtin_bit_cast(u32, G[16].r), __builtin_bit_cast(u32, G[16].i));
    KRD();
    {
        LAS char* k0 = kc + (g < 2 ? 256 + 64 * g + 4 * r16 : 384 + 64 * (g - 2) + 2 * r16) * 4;
        u32x4 kb = asu(ka * cup);
        sts64(k0, kb.x, kb.y);
        sts64(k0 + (g < 2 ? 8 : 128), kb.z, kb.w);
    }
    SB();
    KM2(0); KM2(1); KM2(2); KM2(3);
    pin(ka);
    {
        auto ok = [&](int i) { return rowsum(sum4(uu[i])) + uc[i] == tag; };
        DBG(0) = settle<2>(badmask<2>(ok), ok,
                  [&](auto i, u32 bm) { reld<i.v>(uu[i.v], dws, cofs, WS_UX + i.v * 16 * 8192, bm); reld32<i.v>(uc[i.v], dws, ccs, WS_UC + i.v * 16 * 512, bm); }, [&](int i) { pin(uu[i]); pin(uc[i]); });
    }
    {
        LAS float* k0 = (LAS float*)(kc + (512 + 64 * g + r16) * 4);
        k0[0] = ka.x * cup; k0[16] = ka.y * cup; k0[32] = ka.z * cup; k0[48] = ka.w * cup;
    }
#pragma unroll
    for (int i = 0; i < 2; ++i) {
        LAS char* ud = sm + S_UT + 512 + (4 * i + wave) * 512 + r16 * 32 + g * 8;
        sts64(ud, __builtin_amdgcn_perm(uu[i].y, uu[i].x, 0x05040100u), __builtin_amdgcn_perm(uu[i].w, uu[i].z, 0x05040100u));
        sts64(ud + 4608, __builtin_amdgcn_perm(uu[i].y, uu[i].x, 0x07060302u), __builtin_amdgcn_perm(uu[i].w, uu[i].z, 0x07060302u));
    }
    wg_barrier();
    STAMP(3);
    if (TCH & 4) tch0 = touch(dws, lane < 16 ? WS_YX + (ct * 32 + j) * 8192 + (lane & 8) * 512 + wave * 1024 + (lane & 7) * 128
                                : lane == 16 ? WS_YC + ((ct * 32 + j) * 4 + wave) * 128 : 0x80000000u);
    if (TCH & 2) tch1 = touch(mkdesc(a.out, 2048 * 1792), (lane & 6) == 0 ? (trow + (lane & 1)) * 1792 + 128 * ct : 0x80000000u);
    if (j == 0 && tid == 0) __hip_atomic_store((GAS u32*)(a.ws + WS_EPS + ct * 256), tag, RLX_AGENT);
    u32x4 U[2][8];
    auto rdU = [&](int it, int bq) { U[it & 1][bq] = lds128(ubase + 512 * bq + 192 - 64 * it); };
    {
        LAS char* k0 = sm + S_KC + ch * 6144;
        LAS char* Rb = sm + S_P + ch * 2688;
        const int t = tid & 127, e1 = min(t + 128, 167);
        f32x4 a0 = asf(lds128(k0 + t * 16)), b0 = asf(lds128(k0 + 3072 + t * 16)), a1 = asf(lds128(k0 + e1 * 16)), b1 = asf(lds128(k0 + 3072 + e1 * 16));
        float n0 = *(LAS float*)(k0 + t * 16 + 16) + *(LAS float*)(k0 + 3072 + t * 16 + 16);
        float n1 = *(LAS float*)(k0 + e1 * 16 + 16) + *(LAS float*)(k0 + 3072 + e1 * 16 + 16);
#pragma unroll
        for (int bq = 0; bq < 8; ++bq) rdU(3, bq);
        a0 += b0; a1 += b1;
        float p0 = a0.x, p1 = a0.y, p2 = a0.z, p3 = a0.w, q0 = a1.x, q1 = a1.y, q2 = a1.z, q3 = a1.w;
        asm volatile("" : "+v"(p0), "+v"(p1), "+v"(p2), "+v"(p3), "+v"(q0), "+v"(q1), "+v"(q2), "+v"(q3));
        sts128(Rb + (163 - t) * 16, u32x4{pk(n0, p3), pk(p3, p2), pk(p2, p1), pk(p1, p0)});
        u32x4 v1 = {pk(n1, q3), pk(q3, q2), pk(q2, q1), pk(q1, q0)};
        if (t >= 36) v1 = u32x4{0, 0, 0, 0};
        if (t < 39) sts128(Rb + (t < 36 ? 35 - t : 129 + t) * 16, v1);
        if (t == 0) sts128(Rb + 164 * 16, u32x4{pk(p0, 0.f), 0, 0, 0});
    }
    wg_barrier();
    STAMP(4);
    __builtin_amdgcn_s_setprio(PS1);
    f32x4 y[8];
    {
#define Q0(i) P0[102 - (i)]
#define Q1(i) P1[132 - (i)]
#define Q2(i) P2[130 - (i)]
#define Q3(i) P3[108 - (i)]
#define PERM __builtin_amdgcn_perm
        u32x4 T[2][8];
        u32 raw[11], rx[9];
        auto rdT = [&](int it, int k) {
            u32x4& t0 = T[it & 1][0];
            if (k == 0) { t0.x = Q0(32 * it + 6); t0.y = Q0(32 * it + 4); }
            if (k == 1) { t0.z = Q0(32 * it + 2); t0.w = Q0(32 * it); }
            if (k == 2) { raw[0] = Q1(16 * it); raw[1] = Q1(16 * it + 2); raw[2] = Q1(16 * it + 4); }
            if (k == 3) { raw[3] = Q2(8 * it); raw[4] = Q2(8 * it + 2); }
            if (k == 4) { raw[5] = Q2(8 * it + 64); raw[6] = Q2(8 * it + 66); }
            if (k == 5) { raw[7] = Q3(4 * it); raw[8] = Q3(4 * it + 32); }
            if (k == 6) { raw[9] = Q3(4 * it + 64); raw[10] = Q3(4 * it + 96); }
        };
        auto mkT = [&](int it, int aa, int e) {
            u32 v;
            if (aa == 1) v = e < 2 ? PERM(raw[2], raw[1], sel[1][2 + e]) : PERM(raw[1], raw[0], sel[1][e]);
            else if (aa < 4) v = PERM(raw[2 * aa], raw[2 * aa - 1], sel[2][e]);
            else v = PERM(raw[3 + aa], raw[3 + aa], sel[3][e]);
            T[it & 1][aa][e] = v;
        };
        auto rdX = [&](int k) {
            if (k == 0) { rx[0] = P0[102 - 262]; rx[1] = P0[102 - 260]; }
            if (k == 1) { rx[2] = P0[102 - 258]; rx[3] = P0[102 - 256]; }
            if (k == 2) { rx[4] = Q1(128); rx[5] = Q1(130); rx[6] = Q1(132); }
            if (k == 3) { rx[7] = Q2(128); rx[8] = Q2(130); }
        };
        auto fixT = [&](int k, int e) {
            const int aa = 1 << k;
            u32 lo = k == 0 ? rx[e] : k == 1 ? (e < 2 ? PERM(rx[6], rx[5], sel[1][2 + e]) : PERM(rx[5], rx[4], sel[1][e])) : PERM(rx[8], rx[7], sel[2][e]);
            T[0][aa][e] = (lo & msk[e]) | (T[0][aa][e] & ~msk[e]);
        };
#pragma unroll
        for (int k = 0; k < 7; ++k) rdT(3, k);
        SB();
#pragma unroll
        for (int it = 3; it >= 0; --it) {
            int sl = 0;
#pragma unroll
            for (int aa = 0; aa < 8; ++aa)
#pragma unroll
                for (int bq = 0; bq + aa < 8; ++bq, ++sl) {
                    y[aa + bq] = mfma(T[it & 1][aa], U[it & 1][bq], (it == 3 && aa == 0) ? f32x4{0.f, 0.f, 0.f, 0.f} : y[aa + bq]);
                    SB();
                    if (it == 3) {
                        if (sl < 14) { mkT(3, 1 + (sl >> 1), 2 * (sl & 1)); mkT(3, 1 + (sl >> 1), 2 * (sl & 1) + 1); }
                        else if (sl < 21) { rdT(2, sl - 14); rdU(2, sl - 14); }
                        else if (sl == 21) rdU(2, 7);
                        else { mkT(2, 1 + ((sl - 22) >> 1), 2 * (sl & 1)); mkT(2, 1 + ((sl - 22) >> 1), 2 * (sl & 1) + 1); }
                    } else if (it > 0) {
                        if (sl < 7) { rdT(it - 1, sl); rdU(it - 1, sl); }
                        else if (sl == 7) rdU(it - 1, 7);
                        if (it == 1 && sl >= 7 && sl < 11) rdX(sl - 7);
                        if (sl >= 10 && sl < 24) { mkT(it - 1, 1 + ((sl - 10) >> 1), 2 * (sl & 1)); mkT(it - 1, 1 + ((sl - 10) >> 1), 2 * (sl & 1) + 1); }
                        if (it == 1 && sl >= 24) fixT((sl - 24) >> 2, (sl - 24) & 3);
                    }
                    SB();
                }
        }
    }
    STAMP(5);
    __builtin_amdgcn_s_setprio(PSV);
    {
        LAS char* yd = sm + S_TAB + wave * 8192 + lane * 16;
#pragma unroll
        for (int i = 0; i < 8; ++i) sts128(yd + i * 1024, asu(y[i]));
    }
    wg_barrier();
    {
        const float d0 = bflo(dsk), d1 = bfhi(dsk);
        u32x4 o[2];
#pragma unroll
        for (int i = 0; i < 2; ++i) {
            LAS char* ys = sm + S_TAB + (wave + 4 * i) * 1024 + lane * 16;
            f32x4 y0 = asf(lds128(ys)) + asf(lds128(ys + 8192));
            f32x4 y1 = asf(lds128(ys + 16384)) + asf(lds128(ys + 24576));
            o[i] = u32x4{pk(y0.x + bflo(uu[i].x) * d0, y1.x + bfhi(uu[i].x) * d1), pk(y0.y + bflo(uu[i].y) * d0, y1.y + bfhi(uu[i].y) * d1),
                         pk(y0.z + bflo(uu[i].z) * d0, y1.z + bfhi(uu[i].z) * d1), pk(y0.w + bflo(uu[i].w) * d0, y1.w + bfhi(uu[i].w) * d1)};
        }
        bst_dev(o[0], rws, WS_YX + pofs);
        bst_dev(o[1], rws, WS_YX + pofs + 4096);
        pubcs(o[0], o[1], WS_YC);
    }
    STAMP(6);
    WAITV(0);
    KEEP(tch0); KEEP(tch1);
    STAMP(7);
    {
        u32x4 v[2]; u32 vc[2];
        auto ldy = [&](int i) { v[i] = bld_dev(rws, cofs, WS_YX + i * 16 * 8192); vc[i] = bld32_dev(rws, ccs, WS_YC + i * 16 * 512); };
        ldy(0); ldy(1);
        cbar();
        auto ok = [&](int i) { return rowsum(sum4(v[i])) + vc[i] == tag; };
        DBG(1) = settle<2>(badmask<2>(ok), ok,
                  [&](auto i, u32 bm) { reld<i.v>(v[i.v], dws, cofs, WS_YX + i.v * 16 * 8192, bm); reld32<i.v>(vc[i.v], dws, ccs, WS_YC + i.v * 16 * 512, bm); }, [&](int i) { pin(v[i]); pin(vc[i]); });
        STAMP(8);
        sts128(trr, v[0]); sts128(trr + 4 * 288, v[1]);
        wg_barrier();
        u32x2 t0 = *(LAS u32x2*)trw, t1 = *(LAS u32x2*)(trw + 8 * 288), t2 = *(LAS u32x2*)(trw + 16 * 288), t3 = *(LAS u32x2*)(trw + 24 * 288);
        const rsrc_t ro = mkrsrc(a.out, 2048 * 1792);
        const u32 oo = trow * 1792 + 128 * ct + (tid & 7) * 16;
        __builtin_amdgcn_raw_buffer_store_b128(u32x4{t0.x, t1.x, t2.x, t3.x}, ro, oo, 0, OAUX);
        __builtin_amdgcn_raw_buffer_store_b128(u32x4{t0.y, t1.y, t2.y, t3.y}, ro, oo + 1792, 0, OAUX);
    }
    STAMP(9);
    STAMP_FLUSH(256 + bb);
}

#if defined(__HIP_DEVICE_COMPILE__)
#define NOPK __attribute__((target("no-packed-fp32-ops")))
#else
#define NOPK
#endif
__global__ NOPK __launch_bounds__(256, 2) void fused_kernel(Args a) {
    __shared__ __attribute__((aligned(16))) char smem[81920];
    LAS char* sm = (LAS char*)smem;
    {
        u32 t = threadIdx.x * 128;
        asm volatile("s_sub_u32 s4, %1, %2\n\t"
                     "s_cmp_lt_u32 s4, 32\n\t"
                     "s_cbranch_scc0 1f\n\t"
                     "s_getpc_b64 s[4:5]\n\t"
                     "s_and_b32 s4, s4, 0xffffff00\n\t"
                     "s_and_b32 s5, s5, 0xffff\n\t"
                     "s_mov_b32 s6, %3\n\t"
                     "s_mov_b32 s7, 0x20000\n\t"
                     "buffer_load_dword %0, %0, s[4:7], 0 offen\n\t"
                     "s_waitcnt vmcnt(0)\n\t"
                     "s_endpgm\n"
                     "1:"
                     : "+v"(t) : "s"(blockIdx.x), "i"(SSM_FIRST ? 0 : 256), "i"(CPF ? CODE_BYTES : 0) : "s4", "s5", "s6", "s7", "scc", "memory");
    }
    const bool is_attn = SSM_FIRST ? blockIdx.x >= 256 : blockIdx.x < 256;
    const int b = blockIdx.x & 255;
    if (__builtin_expect(is_attn, ATTN_LIKELY)) attn_role(a, sm, b);
    else ssm_role(a, sm, b);
}

extern "C" void kernel_launch(void* const* d_in, const int* in_sizes, int n_in,
                              void* d_out, int out_size, void* d_ws, size_t ws_size,
                              hipStream_t stream) {
    Args a;
    a.u = (const u16*)d_in[0]; a.logA = (const u16*)d_in[1]; a.Aim = (const u16*)d_in[2];
    a.Cre = (const u16*)d_in[3]; a.Cim = (const u16*)d_in[4]; a.logdt = (const u16*)d_in[5];
    a.Dskip = (const u16*)d_in[6]; a.x = (const u16*)d_in[7]; a.wqkv = (const u16*)d_in[8];
    a.wo = (const u16*)d_in[9]; a.out = (u16*)d_out; a.ws = (char*)d_ws;
    fused_kernel<<<512, 256, 0, stream>>>(a);
}
```

```cpp
#include <hip/hip_runtime.h>
#include <stdint.h>

typedef unsigned short u16;
typedef unsigned int u32;
typedef unsigned long long u64;
typedef __bf16 bf16x8 __attribute__((ext_vector_type(8)));
typedef __bf16 bf16x2 __attribute__((ext_vector_type(2)));
typedef short s16x4 __attribute__((ext_vector_type(4)));
typedef float f32x4 __attribute__((ext_vector_type(4)));
typedef float f32x2 __attribute__((ext_vector_type(2)));
typedef float f32x16 __attribute__((ext_vector_type(16)));
typedef u32 u32x2 __attribute__((ext_vector_type(2)));
typedef u32 u32x4 __attribute__((ext_vector_type(4)));
#define LAS __attribute__((address_space(3)))
#define GAS __attribute__((address_space(1)))
#define DI __device__ __forceinline__
#define RLX_AGENT __ATOMIC_RELAXED, __HIP_MEMORY_SCOPE_AGENT
#define SB() __builtin_amdgcn_sched_barrier(0)
#define WAITV(n) asm volatile("s_waitcnt vmcnt(" #n ")" ::: "memory")
#define KEEP(x) asm volatile("" :: "v"(x))
typedef __amdgpu_buffer_rsrc_t rsrc_t;
#define EXP 0
#define PA0 1
#define PA1 2
#define PS0 1
#define PS1 0
#define POLL_SLEEP 2
#define ABL 0
#define GK0 14
#define PRO 8
#define QS0 40
#define PSV 3
#define NO1 4
#define PRE 10
#define TCH 5
#define SPREAD 1
#define CPF 0
#define ATTN_LIKELY 1
#define CODE_BYTES 25600
#define JROT 0
#define MAPH 4
#define CA 2
#define CB 2
#define RDA 4
#define OAUX 16
#define OSTAG 1
#define THR 0
#define REMAP 1
#define LATE 1
#define HELP 3
#define HALFBAR 0
#define HPRIO 0
#define SSM_FIRST 0

#define STAMP(i) do {} while (0)
#define STAMP_DECL u32 dbgv_
#define DBG(i) dbgv_
#define STAMP_FLUSH(slot) do {} while (0)

constexpr u32 WS_EPA = 0;
constexpr u32 WS_EPS = 256;
constexpr u32 WS_UC  = 65536;
constexpr u32 WS_YC  = WS_UC + 131072;
constexpr u32 WS_KVP = 524288;
constexpr u32 WS_OX = WS_KVP + (1u << 20);
constexpr u32 WS_UX  = WS_OX + (2u << 20);
constexpr u32 WS_YX  = WS_UX + (2u << 20);
constexpr u32 WS_END = WS_YX + (2u << 20);
constexpr u32 WS_DBG = 16u << 20;
constexpr int SPIN_CAP = 1 << 16;

struct Args {
    const u16 *u, *logA, *Aim, *Cre, *Cim, *logdt, *Dskip, *x, *wqkv, *wo;
    u16* out;
    char* ws;
};

DI f32x4 mfma(u32x4 a, u32x4 b, f32x4 c) {
    return __builtin_amdgcn_mfma_f32_16x16x32_bf16(__builtin_bit_cast(bf16x8, a), __builtin_bit_cast(bf16x8, b), c, 0, 0, 0);
}
DI u32 pk2(f32x2 v) { return __builtin_bit_cast(u32, __builtin_convertvector(v, bf16x2)); }
DI u32 pk(float lo, float hi) { f32x2 v = {lo, hi}; return pk2(v); }
DI float bflo(u32 v) { return __uint_as_float(v << 16); }
DI float bfhi(u32 v) { return __uint_as_float(v & 0xffff0000u); }
DI float rbf(float f) { return bflo(pk(f, f)); }
DI rsrc_t mkrsrc(const void* p, u32 bytes) { return __builtin_amdgcn_make_buffer_rsrc((void*)p, 0, bytes, 0x00020000); }
DI u32x4 bld_dev(rsrc_t r, u32 voff, u32 soff) { return __builtin_amdgcn_raw_buffer_load_b128(r, voff, soff, 16); }
DI u32 bld32_dev(rsrc_t r, u32 voff, u32 soff) { return __builtin_amdgcn_raw_buffer_load_b32(r, voff, soff, 16); }
DI void bst_dev(u32x4 v, rsrc_t r, u32 voff) { __builtin_amdgcn_raw_buffer_store_b128(v, r, voff, 0, 16); }
DI void bst32_dev(u32 v, rsrc_t r, u32 voff) { __builtin_amdgcn_raw_buffer_store_b32(v, r, voff, 0, 16); }
DI u32x4 mkdesc(const void* p, u32 bytes) { u64 v = (u64)p; return u32x4{(u32)v, (u32)(v >> 32) & 0xffffu, bytes, 0x00020000u}; }
DI void setm0(LAS char* dst) { asm volatile("s_mov_b32 m0, %0\n\ts_nop 0" :: "s"((u32)(uintptr_t)dst) : "memory"); }
template <int OFF>
DI void dma(u32x4 desc, u32 voff, u32 soff) {
    asm volatile("buffer_load_dwordx4 %0, %1, %2 offen offset:%3 lds" :: "v"(voff), "s"(desc), "s"(soff), "i"(OFF) : "memory");
}
DI u32x4 lds128(LAS char* p) { return *(LAS u32x4*)p; }
DI void sts128(LAS char* p, u32x4 v) { *(LAS u32x4*)p = v; }
DI void sts64(LAS char* p, u32 lo, u32 hi) { u32x2 v = {lo, hi}; *(LAS u32x2*)p = v; }
DI u32x4 trfrag(LAS char* p0, LAS char* p1) {
    u32x2 a = __builtin_bit_cast(u32x2, __builtin_amdgcn_ds_read_tr16_b64_v4i16((LAS s16x4*)p0));
    u32x2 b = __builtin_bit_cast(u32x2, __builtin_amdgcn_ds_read_tr16_b64_v4i16((LAS s16x4*)p1));
    u32x4 r = {a.x, a.y, b.x, b.y};
    return r;
}
DI void cbar() { asm volatile("" ::: "memory"); }
DI void wg_barrier() { asm volatile("s_waitcnt lgkmcnt(0)\n\ts_barrier" ::: "memory"); }
DI f32x4 asf(u32x4 v) { return __builtin_bit_cast(f32x4, v); }
DI u32x4 asu(f32x4 v) { return __builtin_bit_cast(u32x4, v); }
DI u32 sum4(u32x4 v) { return v.x + v.y + v.z + v.w; }
DI u32 rowsum(u32 s) {
    s += __builtin_amdgcn_update_dpp(0, s, 0x128, 0xf, 0xf, false);
    s += __builtin_amdgcn_update_dpp(0, s, 0x124, 0xf, 0xf, false);
    s += __builtin_amdgcn_update_dpp(0, s, 0x122, 0xf, 0xf, false);
    s += __builtin_amdgcn_update_dpp(0, s, 0x121, 0xf, 0xf, false);
    return s;
}
DI u32 sum32(u32 s) {
    s = rowsum(s);
    auto t = __builtin_amdgcn_permlane16_swap(s, s, false, false);
    return t[0] + t[1];
}
DI u32 colsum(u32 s) {
    auto r = __builtin_amdgcn_permlane32_swap(s, s, false, false);
    s = r[0] + r[1];
    auto t = __builtin_amdgcn_permlane16_swap(s, s, false, false);
    return t[0] + t[1];
}
DI float phi(float t) {
    float e = __builtin_amdgcn_exp2f(t * 1.44269504089f);
    return __int_as_float(max(__float_as_int(t), 0x358637bd)) + __builtin_amdgcn_fmed3f(e, 0.f, 1.f);
}
DI u32x4 phi8(const f32x16& a, int s) { return u32x4{pk(phi(a[8 * s]), phi(a[8 * s + 1])), pk(phi(a[8 * s + 2]), phi(a[8 * s + 3])), pk(phi(a[8 * s + 4]), phi(a[8 * s + 5])), pk(phi(a[8 * s + 6]), phi(a[8 * s + 7]))}; }
DI u32x4 pk8(const f32x16& a, int s) { return u32x4{pk(a[8 * s], a[8 * s + 1]), pk(a[8 * s + 2], a[8 * s + 3]), pk(a[8 * s + 4], a[8 * s + 5]), pk(a[8 * s + 6], a[8 * s + 7])}; }
DI u32x4 quad(const f32x16& a, int r) { return asu(f32x4{a[4 * r], a[4 * r + 1], a[4 * r + 2], a[4 * r + 3]}); }
DI f32x16 zero16() { f32x16 z; for (int i = 0; i < 16; ++i) z[i] = 0.f; return z; }
DI f32x16 mfma32(u32x4 a, u32x4 b, f32x16 c) {
    return __builtin_amdgcn_mfma_f32_32x32x16_bf16(__builtin_bit_cast(bf16x8, a), __builtin_bit_cast(bf16x8, b), c, 0, 0, 0);
}
DI u32 halfsum(u32 s) { auto r = __builtin_amdgcn_permlane32_swap(s, s, false, false); return r[0] + r[1]; }
DI float colsumf(float f) {
    auto r = __builtin_amdgcn_permlane32_swap(__float_as_uint(f), __float_as_uint(f), false, false);
    f = __uint_as_float(r[0]) + __uint_as_float(r[1]);
    auto t = __builtin_amdgcn_permlane16_swap(__float_as_uint(f), __float_as_uint(f), false, false);
    return __uint_as_float(t[0]) + __uint_as_float(t[1]);
}
DI u32 ldepoch(const char* p) {
    u32 v;
    asm volatile("global_load_dword %0, %1, %2 sc1" : "=v"(v) : "v"(0u), "s"(p) : "memory");
    return v;
}
template <int BIT> DI void reld(u32x4& v, u32x4 desc, u32 voff, u32 soff, u32 mask) {
    asm volatile("s_bitcmp1_b32 %4, %5\n\ts_cbranch_scc0 1f\n\tbuffer_load_dwordx4 %0, %1, %2, %3 offen sc1\n1:"
                 : "+v"(v) : "v"(voff), "s"(desc), "s"(soff), "s"(mask), "i"(BIT) : "scc", "memory");
}
template <int BIT> DI void reld32(u32& v, u32x4 desc, u32 voff, u32 soff, u32 mask) {
    asm volatile("s_bitcmp1_b32 %4, %5\n\ts_cbranch_scc0 1f\n\tbuffer_load_dword %0, %1, %2, %3 offen sc1\n1:"
                 : "+v"(v) : "v"(voff), "s"(desc), "s"(soff), "s"(mask), "i"(BIT) : "scc", "memory");
}
DI u32 touch(u32x4 desc, u32 voff) { u32 v; asm volatile("buffer_load_dword %0, %1, %2, 0 offen" : "=v"(v) : "v"(voff), "s"(desc) : "memory"); return v; }
template <class T> DI void pin(T& v) { asm volatile("" : "+v"(v) :: "memory"); }
template <int I> struct IC { static constexpr int v = I; };
template <int N, int I = 0, class F> DI void sfor(F f) { if constexpr (I < N) { f(IC<I>{}); sfor<N, I + 1>(f); } }
template <int N, class OK, class LD, class PIN>
DI u32 settle(u32 bad, OK ok, LD ld, PIN pn) {
    u32 dbg = __builtin_popcount(bad) << 8;
#pragma unroll 1
    for (int sp = 0; bad != 0 && sp < SPIN_CAP; ++sp) {
        ++dbg;
        __builtin_amdgcn_s_sleep(POLL_SLEEP);
        sfor<N>([&](auto i) { ld(i, bad); });
        WAITV(0);
        sfor<N>([&](auto i) { pn(i.v); });
        sfor<N>([&](auto i) { if ((bad >> i.v & 1) && __all(ok(i.v))) bad &= ~(1u << i.v); });
    }
    return dbg;
}
template <int N, class OK> DI u32 badmask(OK ok) { u32 bad = 0; sfor<N>([&](auto i) { if (!__all(ok(i.v))) bad |= 1u << i.v; }); return bad; }

constexpr int A_RED = 0, A_WO = 32768, A_WQ = 98304, A_KVB = 155648;

DI void ldd(u32x4& v, u32x4 desc, u32 voff, u32 soff) {
    asm volatile("buffer_load_dwordx4 %0, %1, %2, %3 offen sc1" : "=v"(v) : "v"(voff), "s"(desc), "s"(soff) : "memory");
}
template <int N> DI void waitv() { asm volatile("s_waitcnt vmcnt(%0)" :: "i"(N) : "memory"); }
template <int N> DI void waitx(u32x4* x) { asm volatile("s_waitcnt vmcnt(%4)" : "+v"(x[0]), "+v"(x[1]), "+v"(x[2]), "+v"(x[3]) : "i"(N) : "memory"); }
constexpr int issued_before(int gs) {
    int n = PRO + gs;
    int cap = 16 + (gs < 12 ? 0 : 4 * ((gs - 12) / 8) + ((gs - 12) % 8 < 4 ? (gs - 12) % 8 : 4));
    if (gs > 32) cap = 29;
    if (cap > 28) cap = 29;
    n = n < cap ? n : cap;
    if (n < 29) return n;
    int q = gs - QS0;
    return 29 + (q < 0 ? 0 : q > 7 ? 7 : q);
}

DI void attn_role(const Args& a, LAS char* sm, const int xcd, const int j) {
    STAMP_DECL;
    const int tid = threadIdx.x, lane = tid & 63, wave = __builtin_amdgcn_readfirstlane(tid >> 6);
    const int rw4 = wave & 3, hs = wave >> 2, t256 = tid & 255;
    constexpr int HG = 8 / MAPH;
    const int hp = (xcd % HG) * MAPH + (j % MAPH), rb = (xcd / HG) * (16 / MAPH) + j / MAPH;
    const int h = 2 * hp + hs;
    const int c0 = (CA * rb + CB * hp) % 7;
    const int r32 = lane & 31, hh = lane >> 5, blk = (lane >> 4) & 1, q = (lane >> 2) & 3, p = lane & 3;
    const u32x4 rx = mkdesc(a.x, 2048 * 896), rw = mkdesc(a.wqkv, 448 * 2688), rwo = mkdesc(a.wo, 448 * 896);
    const rsrc_t rws = mkrsrc(a.ws, WS_END);
    const u32x4 dws = mkdesc(a.ws, WS_END);
    STAMP(0);
    u32 tag = ldepoch(a.ws + WS_EPA);
    u32 tch0 = 0;
    cbar();
    const int widx = (lane & 15) ^ ((lane >> 4) << 2);
    const u32 wv = (8 * wave + (lane >> 4)) * 2688 + (1 + (widx >> 3)) * 896 + hp * 112 + min(widx & 7, 6) * 16;
    const u32 xv0 = (rb * 128 + wave * 16 + (lane >> 3)) * 896 + (((lane & 7) ^ (lane >> 4)) * 16);
    const u32 xv1 = (rb * 128 + wave * 16 + 8 + (lane >> 3)) * 896 + (((lane & 7) ^ (4 + (lane >> 4))) * 16) - 1024;
    const u32 qv = (8 * wave + (lane >> 3)) * 2688 + hp * 112 + min((lane & 7) ^ (blk << 2), 6) * 16;
    auto kchunk = [&](int i) { int kc = i + c0; return kc >= 7 ? kc - 7 : kc; };
    auto ldn = [&](int n) {
        if (n >= 36) return;
        if (n >= 29) { setm0(sm + A_WQ + (n - 29) * 8192 + wave * 1024); dma<0>(rw, qv, kchunk(n - 29) * 64 * 2688); return; }
        if (n == 28) { tch0 = touch(dws, lane < 8 ? WS_KVP + (h * 16 + rb) * 2048 + rw4 * 512 + (lane & 3) * 128
                                : lane < 24 ? WS_OX + ((rb * 8 + rw4 * 2 + blk) * 16 + h) * 1024 + (lane & 7) * 128 : 0x80000000u); return; }
        const int i = n >> 2, m = n & 3;
        if (m == 0) { setm0(sm + (i & 3) * 32768 + 16384 + wave * 2048); dma<0>(rx, xv0, kchunk(i) * 128); }
        if (m == 1) dma<1024>(rx, xv1, kchunk(i) * 128);
        if (m == 2) { setm0(sm + (i & 3) * 32768 + wave * 2048); dma<0>(rw, wv, kchunk(i) * 64 * 2688); }
        if (m == 3) dma<1024>(rw, wv, kchunk(i) * 64 * 2688 + 4 * 2688 - 1024);
    };
#pragma unroll
    for (int n = 0; n < PRO; ++n) ldn(n);
    cbar();

    const int wb = 56 * hs + 32 * blk + 8 * p;
    LAS char *wsrc[2], *xsrc[4];
#pragma unroll
    for (int sg = 0; sg < 2; ++sg) wsrc[sg] = sm + (32 * hh + q) * 256 + (((2 * sg + (wb >> 6)) ^ q) << 6) + (wb & 63);
#pragma unroll
    for (int kk = 0; kk < 4; ++kk) xsrc[kk] = sm + 16384 + (32 * rw4 + r32) * 128 + (((4 * hh + kk) ^ ((r32 >> 1) & 7)) * 16);

    const bool p3 = j < 14;
    const int r128 = p3 ? xcd * 2 + j / 7 : 0, nb = j % 7;
    f32x16 acc[2] = {zero16(), zero16()};
    u32x4 xf[28], wf[7][4][2];
    auto skk = [](int i, int sl) { return i < 6 ? sl >> 1 : sl & 3; };
    auto ssg = [](int i, int sl) { return i < 6 ? sl & 1 : sl >> 2; };
    auto rdw = [&](int i, int sl) { LAS char* s = wsrc[ssg(i, sl)] + (i & 3) * 32768 + skk(i, sl) * 2048; wf[i][skk(i, sl)][ssg(i, sl)] = trfrag(s, s + 1024); };
    auto rdx = [&](int i, int kk) { xf[4 * i + kk] = lds128(xsrc[kk] + (i & 3) * 32768); };
    waitv<PRO - 4>();
    wg_barrier();
    STAMP(1);
#pragma unroll
    for (int sl = 0; sl < 4; ++sl) { rdx(0, sl); rdw(0, sl); }
    u32 pkd[8];
    sfor<56>([&](auto gs) {
        constexpr int i = gs.v >> 3, sl = gs.v & 7;
        constexpr int issued = issued_before(gs.v);
        if (sl == 4 && i < 6) { waitv<(issued - (4 * i + 8) > 0 ? issued - (4 * i + 8) : 0)>(); asm volatile("s_barrier" ::: "memory"); }
        static_assert(!(sl == 4 && i < 6) || issued >= 4 * i + 8, "chunk not issued yet");
        SB();
        acc[ssg(i, sl)] = mfma32(xf[4 * i + skk(i, sl)], wf[i][skk(i, sl)][ssg(i, sl)], acc[ssg(i, sl)]);
        SB();
        if (sl < 4) rdw(i, sl + 4);
        else if (i < 6) { rdx(i < 6 ? i + 1 : 6, sl - 4); rdw(i < 6 ? i + 1 : 6, sl - 4); }
        if constexpr (i == 6 && sl >= 5) { pkd[sl - 5] = pk(phi(acc[0][2 * (sl - 5)]), phi(acc[0][2 * (sl - 5) + 1])); pin(pkd[sl - 5]); }
#pragma unroll
        for (int n = issued; n < issued_before(gs.v + 1); ++n) ldn(n);
        SB();
    });
    STAMP(2);
    {
        u32 vm = r32 == 28 ? 0u : ~0u, vo = r32 == 28 ? 0x3f803f80u : 0u;
        pin(vm); pin(vo);
        pkd[3] = pk(phi(acc[0][6]), phi(acc[0][7]));
        f32x16 kv = mfma32(u32x4{pkd[0], pkd[1], pkd[2], pkd[3]}, (pk8(acc[1], 0) & vm) | vo, zero16());
        SB();
        kv = mfma32(phi8(acc[0], 1), (pk8(acc[1], 1) & vm) | vo, kv);
#pragma unroll
        for (int rq = 0; rq < 4; ++rq) sts128(sm + A_RED + wave * 4096 + rq * 1024 + lane * 16, quad(kv, rq));
    }
    asm volatile("s_waitcnt vmcnt(8)" : "+v"(tag) :: "memory");
    tag += 1;
    wg_barrier();
    u32 hm = hh ? 0u : ~0u, cm = r32 == 31 ? 0u : ~0u;
    pin(hm); pin(cm);
    if (rw4 < 2) {
        LAS char* rs = sm + A_RED + hs * 16384 + rw4 * 2048 + lane * 16;
        f32x4 lo = (asf(lds128(rs)) + asf(lds128(rs + 4096))) + (asf(lds128(rs + 8192)) + asf(lds128(rs + 12288)));
        f32x4 hi = (asf(lds128(rs + 1024)) + asf(lds128(rs + 5120))) + (asf(lds128(rs + 9216)) + asf(lds128(rs + 13312)));
        u32x4 v = {pk(lo.x, lo.y), pk(lo.z, lo.w), pk(hi.x, hi.y), pk(hi.z, hi.w) & cm};
        v.w |= (tag - sum32(sum4(v))) & ~cm;
        bst_dev(v, rws, WS_KVP + (h * 16 + rb) * 2048 + t256 * 16);
    }
    cbar();
    STAMP(3);
    if (rw4 < 2) WAITV(1); else WAITV(0);
    KEEP(tch0);
    asm volatile("s_barrier" ::: "memory");
    STAMP(4);
    const u32 ov = (8 * wave + (lane >> 3)) * 896 + nb * 128 + (((lane & 7) ^ ((lane >> 4) << 1)) * 16);
    auto ldO1 = [&](int n) { setm0(sm + A_WO + n * 8192 + wave * 1024); dma<0>(rwo, ov, n * 64 * 896); };
    u32x4 gk[8];
    const u32 kbase = WS_KVP + (h * 16 + (rw4 >> 1) * 8 + (rb & 7)) * 2048, kvo = (tid & 127) * 16;
    auto ldk = [&](int i) { if (i < 8) ldd(gk[i], dws, kvo, kbase ^ (i * 2048)); };
    f32x16 aq = zero16();
    {
        LAS char* qsrc = sm + A_WQ + (32 * hh + q) * 128 + (((wb >> 6) ^ (q >> 1)) << 6) + (wb & 63);
        u32x4 wq[28];
        auto rdq = [&](int k) { LAS char* s = qsrc + (k >> 2) * 8192 + (k & 3) * 1024; wq[k] = trfrag(s, s + 512); };
#pragma unroll
        for (int k = 0; k < 4; ++k) rdq(k);
        SB();
#pragma unroll
        for (int k = 0; k < 28; ++k) {
            aq = mfma32(wq[k], xf[k], aq);
            SB();
            if (k + 4 < 28) rdq(k + 4);
            if (k < 7) ldO1(k);
            if (k >= GK0) ldk(k - GK0);
            SB();
        }
#pragma unroll
        for (int k = 28 - GK0; k < 8; ++k) ldk(k);
    }
    STAMP(5);
    u32 pqd[8];
#pragma unroll
    for (int k = 0; k < 8; ++k) {
        pqd[k] = pk(phi(aq[2 * k]), phi(aq[2 * k + 1]));
        if (k >= 6) pqd[k] &= hm;
        pin(pqd[k]);
    }
    {
        asm volatile("s_waitcnt vmcnt(0)" : "+v"(gk[0]), "+v"(gk[1]), "+v"(gk[2]), "+v"(gk[3]), "+v"(gk[4]), "+v"(gk[5]), "+v"(gk[6]), "+v"(gk[7]) :: "memory");
        u32 t = 0;
#pragma unroll
        for (int i = 0; i < 8; ++i) t += sum4(gk[i]);
        if (!__all(sum32(t) == 8u * tag)) {
            auto ok = [&](int i) { return sum32(sum4(gk[i])) == tag; };
            DBG(0) = settle<8>(badmask<8>(ok), ok, [&](auto i, u32 bm) { reld<i.v>(gk[i.v], dws, kvo, kbase ^ (i.v * 2048), bm); }, [&](int i) { pin(gk[i]); });
        }
    }
    STAMP(6);
    {
        u32x4 v;
#pragma unroll
        for (int c = 0; c < 4; ++c) {
            const float lo = ((bflo(gk[0][c]) + bflo(gk[1][c])) + (bflo(gk[2][c]) + bflo(gk[3][c]))) + ((bflo(gk[4][c]) + bflo(gk[5][c])) + (bflo(gk[6][c]) + bflo(gk[7][c])));
            const float hi = ((bfhi(gk[0][c]) + bfhi(gk[1][c])) + (bfhi(gk[2][c]) + bfhi(gk[3][c]))) + ((bfhi(gk[4][c]) + bfhi(gk[5][c])) + (bfhi(gk[6][c]) + bfhi(gk[7][c])));
            v[c] = pk(lo, hi);
        }
        sts128(sm + A_KVB + tid * 16, v);
    }
    wg_barrier();
    STAMP(7);
    {
        u32x4 kf[4];
#pragma unroll
        for (int i = 0; i < 4; ++i) kf[i] = lds128(sm + A_KVB + hs * 4096 + i * 1024 + lane * 16);
        const u32x4 pq0 = {pqd[0], pqd[1], pqd[2], pqd[3]}, pq1 = {pqd[4], pqd[5], pqd[6], pqd[7]};
        f32x16 oa = mfma32(kf[3], pq1, mfma32(kf[2], pq0, mfma32(kf[1], pq1, mfma32(kf[0], pq0, zero16()))));
        u32 dn = __float_as_uint(oa[12]);
        auto r = __builtin_amdgcn_permlane32_swap(dn, dn, false, false);
        oa *= __builtin_amdgcn_rcpf(__uint_as_float(r[1]) + 1e-6f);
        u32x4 f0 = pk8(oa, 0);
        const u32 o = WS_OX + ((rb * 8 + rw4 * 2 + blk) * 16 + h) * 1024 + hh * 256 + (lane & 15) * 16;
        bst_dev(f0, rws, o);
        SB();
        u32x4 f1 = pk8(oa, 1);
        f1.z &= hm; f1.w &= hm;
        f1.w |= (tag - halfsum(sum4(f0) + sum4(f1))) & ~hm;
        bst_dev(f1, rws, o + 512);
    }
    cbar();
    STAMP(8);
    if (p3) {
        const int g = lane >> 4, r16 = lane & 15;
        const int st = r128 * 8 + ((wave + nb) & 7);
        LAS char *os0[4], *os1[4];
#pragma unroll
        for (int t = 0; t < 4; ++t) {
            os0[t] = sm + A_WO + (16 * hh + 4 * blk + q) * 128 + (((t ^ (2 * blk + (q >> 1))) & 3) * 32) + 8 * p;
            os1[t] = os0[t] + (g == 3 ? 0 : 1024);
        }
        u32x4 wf[16][4];
        auto rdo = [&](int hd, int t) { const int tt = t ^ ((hd & 1) * 2); wf[hd][t] = trfrag(os0[tt] + hd * 28 * 128, os1[tt] + hd * 28 * 128); };
#pragma unroll
        for (int k = 0; k < 4 * PRE; ++k) rdo(k >> 2, k & 3);
        SB();
        WAITV(0);
        u32x4 go[16];
        auto ldo = [&](int i) { go[i] = bld_dev(rws, lane * 16, WS_OX + (st * 16 + i) * 1024); };
#pragma unroll
        for (int i = 0; i < 16; ++i) ldo(i);
        cbar();
        STAMP(9);
        {
            u32 bad = 0;
#pragma unroll
            for (int i = 0; i < 16; i += 4) {
                auto r0 = __builtin_amdgcn_permlane32_swap(sum4(go[i]), sum4(go[i + 2]), false, false);
                auto r1 = __builtin_amdgcn_permlane32_swap(sum4(go[i + 1]), sum4(go[i + 3]), false, false);
                auto r2 = __builtin_amdgcn_permlane16_swap(r0[0] + r0[1], r1[0] + r1[1], false, false);
                const u64 ne = __ballot(r2[0] + r2[1] != tag);
                if (ne) bad |= (((ne & 0xffffull) != 0) | ((ne & 0xffff0000ull) != 0) << 1 | ((ne & 0xffff00000000ull) != 0) << 2 | ((ne >> 48) != 0) << 3) << i;
            }
            DBG(1) = settle<16>(bad, [&](int i) { return colsum(sum4(go[i])) == tag; },
                       [&](auto i, u32 bm) { reld<i.v>(go[i.v], dws, lane * 16, WS_OX + (st * 16 + i.v) * 1024, bm); }, [&](int i) { pin(go[i]); });
        }
        STAMP(10);
        u32 wmask = g == 3 ? 0u : ~0u;
        pin(wmask);
        f32x4 oa[4];
        go[0].w &= wmask;
        SB();
#pragma unroll
        for (int k = 0; k < 64; ++k) {
            const int hd = k >> 2, t = k & 3;
            oa[t] = mfma(wf[hd][t], go[hd], hd ? oa[t] : f32x4{0.f, 0.f, 0.f, 0.f});
            SB();
            if (hd + PRE < 16) rdo(PRE + hd, t);
            if (t == 0 && hd < 15) go[hd + 1].w &= wmask;
            SB();
        }
        {
            LAS char* ob = sm + wave * 2304;
#pragma unroll
            for (int t = 0; t < 4; ++t) sts64(ob + r16 * 144 + t * 32 + g * 8, pk(oa[t].x, oa[t].y), pk(oa[t].z, oa[t].w));
            cbar();
            const rsrc_t ro = mkrsrc(a.out, 2048 * 1792);
            const u32 oo = (st * 16 + (lane >> 3)) * 1792 + 896 + nb * 128 + (lane & 7) * 16;
            const u32x4 v0 = lds128(ob + (lane >> 3) * 144 + (lane & 7) * 16), v1 = lds128(ob + (8 + (lane >> 3)) * 144 + (lane & 7) * 16);
            __builtin_amdgcn_raw_buffer_store_b128(v0, ro, oo, 0, OAUX);
            __builtin_amdgcn_raw_buffer_store_b128(v1, ro, oo + 8 * 1792, 0, OAUX);
        }
        if (xcd == 0 && j == 0 && tid == 0) __hip_atomic_store((GAS u32*)(a.ws + WS_EPA), tag, RLX_AGENT);
        STAMP(11);
    }
    STAMP_FLUSH(j * 8 + xcd);
}

constexpr int S_TAB = 0;
constexpr int S_KC = 34816;
constexpr int S_P = 47104;
constexpr int S_UT = 52480;
constexpr int S_TR = 61696;
constexpr int S_BAR = 70912;

typedef _Float16 h2 __attribute__((ext_vector_type(2)));
typedef _Float16 h8 __attribute__((ext_vector_type(8)));
struct c2 { f32x2 r, i; };
struct hc { h2 r, i; };
DI c2 csq(c2 a) { return c2{a.r * a.r - a.i * a.i, 2.f * a.r * a.i}; }
DI hc hmul(hc a, hc b) { return hc{a.r * b.r - a.i * b.i, a.r * b.i + a.i * b.r}; }
DI hc toh(c2 a) { return hc{__builtin_convertvector(a.r, h2), __builtin_convertvector(a.i, h2)}; }
DI hc tohc(c2 a) { return hc{__builtin_convertvector(a.r, h2), __builtin_convertvector(-a.i, h2)}; }
DI c2 cexp2(f32x2 lr, f32x2 rev, float pw) {
    f32x2 e = lr * pw, f = rev * pw;
    f32x2 m = {__builtin_amdgcn_exp2f(e.x), __builtin_amdgcn_exp2f(e.y)};
    f.x = __builtin_amdgcn_fractf(f.x); f.y = __builtin_amdgcn_fractf(f.y);
    f32x2 c = {__builtin_amdgcn_cosf(f.x), __builtin_amdgcn_cosf(f.y)}, s = {__builtin_amdgcn_sinf(f.x), __builtin_amdgcn_sinf(f.y)};
    return c2{m * c, m * s};
}
DI f32x2 unpk(u32 v) { return f32x2{bflo(v), bfhi(v)}; }
DI f32x4 mfmah(u32x4 a, u32x4 b, f32x4 c) {
    return __builtin_amdgcn_mfma_f32_16x16x32_f16(__builtin_bit_cast(h8, a), __builtin_bit_cast(h8, b), c, 0, 0, 0);
}
DI u32 wavemax(u32 s) {
    s = max(s, (u32)__builtin_amdgcn_update_dpp(0, s, 0x128, 0xf, 0xf, false));
    s = max(s, (u32)__builtin_amdgcn_update_dpp(0, s, 0x124, 0xf, 0xf, false));
    s = max(s, (u32)__builtin_amdgcn_update_dpp(0, s, 0x122, 0xf, 0xf, false));
    s = max(s, (u32)__builtin_amdgcn_update_dpp(0, s, 0x121, 0xf, 0xf, false));
    auto r = __builtin_amdgcn_permlane32_swap(s, s, false, false);
    s = max(r[0], r[1]);
    auto t = __builtin_amdgcn_permlane16_swap(s, s, false, false);
    return max(t[0], t[1]);
}

DI u32 warm_attn(const Args& a, int xcd, int hi, int wave8, int lane) {
    const int u = hi * 7 + wave8, idx = (u - 56) * 64 + lane;
    u32 t;
    if (u < 56) t = touch(mkdesc(a.x, 2048 * 896), wave8 < 7 ? (xcd >> 1) * 512 * 896 + (u * 64 + lane) * 128 : 0x80000000u);
    else t = touch(mkdesc(a.wqkv, 448 * 2688), wave8 < 7 ? (idx >> 3) * 2688 + (1 + ((idx >> 2) & 1)) * 896 + ((xcd & 1) * 3 + (idx & 3)) * 128 : 0x80000000u);
    return t;
}

DI void half_barrier(LAS char* cnt, u32 target) {
    asm volatile("s_waitcnt lgkmcnt(0)" ::: "memory");
    if ((threadIdx.x & 63) == 0) __hip_atomic_fetch_add((LAS u32*)cnt, 1u, __ATOMIC_RELAXED, __HIP_MEMORY_SCOPE_WORKGROUP);
    while (__builtin_amdgcn_readfirstlane(__hip_atomic_load((LAS u32*)cnt, __ATOMIC_RELAXED, __HIP_MEMORY_SCOPE_WORKGROUP)) < target) __builtin_amdgcn_s_sleep(0);
    cbar();
}
#define HBAR(k) wg_barrier()
#define SPRIO(x) do {} while (0)

DI void ssm_role(const Args& a, LAS char* sm0, const int sb, const int xcd, const int hi) {
    STAMP_DECL;
    const int tid = threadIdx.x & 255, lane = tid & 63, wave8 = __builtin_amdgcn_readfirstlane(threadIdx.x >> 6), wave = wave8 & 3;
    LAS char* sm = sm0 + (wave8 >> 2) * 81920;
    if (HPRIO && wave8 < 4) __builtin_amdgcn_s_setprio(2);
    const int sidx = 2 * sb + (wave8 >> 2);
    const int ct = sidx >> 5, j = sidx & 31;
    const int ch = wave >> 1, hf = wave & 1, d = ct * 64 + 2 * j + ch;
    const int g = lane >> 4, r16 = lane & 15;
    STAMP(0);
    SPRIO(PSV);
    const int trow = 256 * (j >> 2) + 16 * (tid >> 4) + 4 * (j & 3) + 2 * ((tid >> 3) & 1);
    const u32 po = (d * 128 + hf * 64 + lane) * 4, uo = trow * 896 + 128 * ct + (tid & 7) * 16;
    const u32 ldt = a.logdt[d];
    const u32 pA = *(const u32*)((const char*)a.logA + po), pI = *(const u32*)((const char*)a.Aim + po);
    const u32x4 ua = *(const u32x4*)((const char*)a.u + uo), ub = *(const u32x4*)((const char*)a.u + uo + 896);
    const u32 pCr = *(const u32*)((const char*)a.Cre + po), pCi = *(const u32*)((const char*)a.Cim + po);
    const u32 dsk = ((const u32*)a.Dskip)[ct * 32 + j];
    cbar();
    u32 tag = ldepoch(a.ws + WS_EPS + ct * 256);
    u32 tchw = 0;
    if (HELP & 1) tchw = warm_attn(a, xcd, hi, wave8, lane);
    const rsrc_t rws = mkrsrc(a.ws, WS_END);
    const u32x4 dws = mkdesc(a.ws, WS_END);
    u32 tch0 = 0, tch1 = 0;
    *(LAS u32*)(sm + S_UT + (tid >> 7) * 4608 + (tid & 127) * 4) = 0;
    if (tid == 0) *(LAS u32*)(sm + S_BAR) = 0;
    const u32 pofs = (ct * 32 + j) * 8192 + tid * 16;
    const u32 cofs = (ct * 32 + (tid >> 4)) * 8192 + j * 256 + (tid & 15) * 16;
    const u32 pcs = r16 < 2 ? ((ct * 32 + j) * 4 + wave) * 128 + g * 8 + (tid & 1) * 4 : 0x80000000u;
    const u32 ccs = ((ct * 32 + (tid >> 4)) * 4 + ((j >> 2) & 3)) * 128 + (j & 3) * 8 + (j >> 4) * 4;
    auto pubcs = [&](u32x4 v0, u32x4 v1, u32 cbase) {
        const u32 s0 = rowsum(sum4(v0)), s1 = rowsum(sum4(v1));
        bst32_dev(tag - (r16 ? s1 : s0), rws, cbase + pcs);
    };
    const int w = 16 + r16 - 8 * g;
    u32 sel[4][4], msk[4], pb[4];
    {
        u32 y[4];
#pragma unroll
        for (int e = 0; e < 4; ++e) {
            y[e] = (u32)(w + 64 - 2 * e) * 0x10001u - 0x10000u;
            msk[e] = hf ? 0u : (((y[e] >> 6) & 0x10001u) ^ 0x10001u) * 0xffffu;
        }
        pb[0] = w - 7;
#pragma unroll
        for (int s = 1; s < 4; ++s) {
            u32 r[4];
#pragma unroll
            for (int e = (s == 1 ? 2 : 0); e < 4; ++e)
                r[e] = ((y[e] + ((1u << (s - 1)) - 1u) * 0x10001u + ((y[e] >> s) & 0x10001u)) >> s) & 0x00ff00ffu;
            const u32 base = r[3] >> 16;
            pb[s] = base - (64 >> s);
#pragma unroll
            for (int e = (s == 1 ? 2 : 0); e < 4; ++e) sel[s][e] = (((r[e] - base * 0x10001u) ^ 0x10001u) & 0xfffffu) * 0x202u + 0x01000100u;
        }
        asm volatile("" : "+v"(sel[1][2]), "+v"(sel[1][3]), "+v"(sel[2][0]), "+v"(sel[2][1]), "+v"(sel[2][2]), "+v"(sel[2][3]), "+v"(sel[3][0]), "+v"(sel[3][1]), "+v"(sel[3][2]), "+v"(sel[3][3]));
        asm volatile("" : "+v"(msk[0]), "+v"(msk[1]), "+v"(msk[2]), "+v"(msk[3]));
    }
    LAS u32* const R = (LAS u32*)(sm + S_P + ch * 2688) + 655;
    LAS u32 *P0 = R - (128 * hf + (int)pb[0]) - 102, *P1 = R - (256 + 64 * hf + (int)pb[1]) - 132, *P2 = R - (384 + 32 * hf + (int)pb[2]) - 130, *P3 = R - (512 + 16 * hf + (int)pb[3]) - 108;
    LAS char* ubase = sm + S_UT + ch * 4608 + 480 + r16 * 32 + g * 16 - 256 * hf - 192;
    asm volatile("" : "+v"(P0), "+v"(P1), "+v"(P2), "+v"(P3), "+v"(ubase));
    LAS char* trw = sm + S_TR + (tid & 7) * 288 + (tid >> 3) * 8;
    LAS char* trr = sm + S_TR + (((tid >> 4) & 3) * 8 + (tid >> 6)) * 288 + (tid & 15) * 16;
    LAS char* tab = sm + S_TAB + wave * 8704;
    LAS char* tw = tab + lane * 8;
    LAS char* tr = tab + r16 * 544 + g * 16;
    LAS char* kc = sm + S_KC + wave * 3072;
    SB();
#define EMIT2(row, v0, v1) do { SB(); sts64(tw + (row) * 544, __builtin_bit_cast(u32, (v0).r), __builtin_bit_cast(u32, (v0).i)); \
                                sts64(tw + ((row) + 1) * 544, __builtin_bit_cast(u32, (v1).r), __builtin_bit_cast(u32, (v1).i)); SB(); } while (0)
#define KRD() do { SB(); _Pragma("unroll") for (int ks = 0; ks < 8; ++ks) gf[ks] = lds128(tr + ks * 64); cbar(); SB(); } while (0)
#define KM2(k) do { ka = mfmah(gf[2 * (k)], ff[2 * (k)], (k) ? ka : f32x4{0.f, 0.f, 0.f, 0.f}); ka = mfmah(gf[2 * (k) + 1], ff[2 * (k) + 1], ka); } while (0)
    const float L2E = 1.44269504089f;
    const float dt = rbf(__builtin_amdgcn_exp2f(bflo(ldt) * L2E));
    f32x2 lr = unpk(pA) * L2E;
    lr = unpk(pk(__builtin_amdgcn_exp2f(lr.x), __builtin_amdgcn_exp2f(lr.y))) * dt * -L2E;
    const f32x2 rev = unpk(pI) * dt * 0.15915494309f;
    SB();
    sts64(trw, ua.x, ub.x); sts64(trw + 8 * 288, ua.y, ub.y); sts64(trw + 16 * 288, ua.z, ub.z); sts64(trw + 24 * 288, ua.w, ub.w);
    SB();
    hc W[8], F[16], G[17], H, t0, t1;
    u32x4 ff[8], gf[8];
    f32x4 ka;
    c2 e = cexp2(lr, -rev, 8.f);
    F[1] = toh(e); e = csq(e); F[2] = toh(e);
    asm volatile("s_waitcnt vmcnt(%1)" : "+v"(tag) : "i"(HELP & 1) : "memory");
    tag += 1;
    wg_barrier();
    const u32x4 up0 = lds128(trr), up1 = lds128(trr + 4 * 288);
    F[0] = hc{h2{1.f16, 1.f16}, h2{0.f16, 0.f16}};
    EMIT2(0, F[0], F[1]);
    e = csq(e); F[4] = toh(e); F[3] = hmul(F[1], F[2]);
    bst_dev(up0, rws, WS_UX + pofs);
    EMIT2(2, F[2], F[3]);
    e = csq(e); F[8] = toh(e); F[5] = hmul(F[4], F[1]);
    EMIT2(4, F[4], F[5]);
    F[6] = hmul(F[4], F[2]); F[7] = hmul(F[4], F[3]); F[9] = hmul(F[8], F[1]);
    bst_dev(up1, rws, WS_UX + pofs + 4096);
    EMIT2(6, F[6], F[7]);
    pubcs(up0, up1, WS_UC);
    cbar();
    SPRIO(PS0);
    STAMP(1);
    EMIT2(8, F[8], F[9]);
    F[10] = hmul(F[8], F[2]); F[11] = hmul(F[8], F[3]);
    e = cexp2(lr, rev, 1.f); W[1] = toh(e);
    EMIT2(10, F[10], F[11]);
    F[12] = hmul(F[8], F[4]); F[13] = hmul(F[8], F[5]);
    e = csq(e); W[2] = toh(e);
    EMIT2(12, F[12], F[13]);
    F[14] = hmul(F[8], F[6]); F[15] = hmul(F[8], F[7]);
    e = csq(e); W[4] = toh(e);
    EMIT2(14, F[14], F[15]);
    u32 ce = max(max(pCr & 0x7fffu, (pCr >> 16) & 0x7fffu), max(pCi & 0x7fffu, (pCi >> 16) & 0x7fffu));
    ce = min(max(wavemax(ce) >> 7, 1u), 253u);
    const float cdn = __uint_as_float((254u - ce) << 23), cup = __uint_as_float(ce << 23) * (2.f * dt);
    G[0] = toh(c2{unpk(pCr) * cdn, unpk(pCi) * cdn});
    t1 = hmul(G[0], W[1]);
    SB();
#pragma unroll
    for (int ks = 0; ks < 8; ++ks) ff[ks] = lds128(tr + ks * 64);
    cbar();
    EMIT2(0, G[0], t1);
    W[3] = hmul(W[1], W[2]); t0 = hmul(G[0], W[2]); t1 = hmul(G[0], W[3]);
    EMIT2(2, t0, t1);
    W[5] = hmul(W[4], W[1]); t0 = hmul(G[0], W[4]); t1 = hmul(G[0], W[5]);
    EMIT2(4, t0, t1);
    W[6] = hmul(W[4], W[2]); W[7] = hmul(W[4], W[3]); t0 = hmul(G[0], W[6]); t1 = hmul(G[0], W[7]);
    EMIT2(6, t0, t1);
    e = cexp2(lr, rev, 128.f); H = toh(e);
    G[1] = hmul(G[0], H); t1 = hmul(G[1], W[1]);
    EMIT2(8, G[1], t1);
    t0 = hmul(G[1], W[2]); t1 = hmul(G[1], W[3]);
    e = csq(e); H = toh(e);
    EMIT2(10, t0, t1);
    t0 = hmul(G[1], W[4]); t1 = hmul(G[1], W[5]); G[2] = hmul(G[0], H);
    EMIT2(12, t0, t1);
    t0 = hmul(G[1], W[6]); t1 = hmul(G[1], W[7]); G[3] = hmul(G[1], H);
    EMIT2(14, t0, t1);
    t1 = hmul(G[2], W[2]);
    e = csq(e); H = toh(e);
    KRD();
    EMIT2(0, G[2], t1);
    t0 = hmul(G[2], W[4]); t1 = hmul(G[2], W[6]); G[4] = hmul(G[0], H);
    EMIT2(2, t0, t1);
    t1 = hmul(G[3], W[2]); G[5] = hmul(G[1], H); KM2(0);
    EMIT2(4, G[3], t1);
    t0 = hmul(G[3], W[4]); t1 = hmul(G[3], W[6]); KM2(1);
    EMIT2(6, t0, t1);
    t1 = hmul(G[4], W[4]); G[6] = hmul(G[2], H); KM2(2);
    EMIT2(8, G[4], t1);
    t1 = hmul(G[5], W[4]); G[7] = hmul(G[3], H); KM2(3);
    EMIT2(10, G[5], t1);
    STAMP(2);
    u32x4 uu[2]; u32 uc[2];
    SPRIO(PSV);
#pragma unroll
    for (int i = 0; i < 2; ++i) { uu[i] = bld_dev(rws, cofs, WS_UX + i * 16 * 8192); uc[i] = bld32_dev(rws, ccs, WS_UC + i * 16 * 512); }
    cbar();
    SPRIO(PS0);
    t1 = hmul(G[6], W[4]);
    e = csq(e); H = toh(e);
    EMIT2(12, G[6], t1);
    t1 = hmul(G[7], W[4]); G[8] = hmul(G[0], H);
    sts128(kc + (128 * (g >> 1) + 4 * (g & 1) + 8 * r16) * 4, asu(ka * cup));
    EMIT2(14, G[7], t1);
    G[9] = hmul(G[1], H); G[10] = hmul(G[2], H);
    KRD();
    EMIT2(0, G[8], G[9]);
    G[11] = hmul(G[3], H); G[12] = hmul(G[4], H); G[13] = hmul(G[5], H);
    EMIT2(2, G[10], G[11]);
    G[14] = hmul(G[6], H); KM2(0); KM2(1);
    EMIT2(4, G[12], G[13]);
    G[15] = hmul(G[7], H); G[16] = hmul(G[8], H); KM2(2); KM2(3);
    EMIT2(6, G[14], G[15]);
    sts64(tw + 8 * 544, __builtin_bit_cast(u32, G[16].r), __builtin_bit_cast(u32, G[16].i));
    KRD();
    {
        LAS char* k0 = kc + (g < 2 ? 256 + 64 * g + 4 * r16 : 384 + 64 * (g - 2) + 2 * r16) * 4;
        u32x4 kb = asu(ka * cup);
        sts64(k0, kb.x, kb.y);
        sts64(k0 + (g < 2 ? 8 : 128), kb.z, kb.w);
    }
    SB();
    KM2(0); KM2(1); KM2(2); KM2(3);
    pin(ka);
    {
        auto ok = [&](int i) { return rowsum(sum4(uu[i])) + uc[i] == tag; };
        DBG(0) = settle<2>(badmask<2>(ok), ok,
                  [&](auto i, u32 bm) { reld<i.v>(uu[i.v], dws, cofs, WS_UX + i.v * 16 * 8192, bm); reld32<i.v>(uc[i.v], dws, ccs, WS_UC + i.v * 16 * 512, bm); }, [&](int i) { pin(uu[i]); pin(uc[i]); });
    }
    {
        LAS float* k0 = (LAS float*)(kc + (512 + 64 * g + r16) * 4);
        k0[0] = ka.x * cup; k0[16] = ka.y * cup; k0[32] = ka.z * cup; k0[48] = ka.w * cup;
    }
#pragma unroll
    for (int i = 0; i < 2; ++i) {
        LAS char* ud = sm + S_UT + 512 + (4 * i + wave) * 512 + r16 * 32 + g * 8;
        sts64(ud, __builtin_amdgcn_perm(uu[i].y, uu[i].x, 0x05040100u), __builtin_amdgcn_perm(uu[i].w, uu[i].z, 0x05040100u));
        sts64(ud + 4608, __builtin_amdgcn_perm(uu[i].y, uu[i].x, 0x07060302u), __builtin_amdgcn_perm(uu[i].w, uu[i].z, 0x07060302u));
    }
    HBAR(1);
    STAMP(3);
    if (TCH & 4) tch0 = touch(dws, lane < 16 ? WS_YX + (ct * 32 + j) * 8192 + (lane & 8) * 512 + wave * 1024 + (lane & 7) * 128
                                : lane == 16 ? WS_YC + ((ct * 32 + j) * 4 + wave) * 128 : 0x80000000u);
    if (TCH & 2) tch1 = touch(mkdesc(a.out, 2048 * 1792), (lane & 6) == 0 ? (trow + (lane & 1)) * 1792 + 128 * ct : 0x80000000u);
    if (j == 0 && tid == 0) __hip_atomic_store((GAS u32*)(a.ws + WS_EPS + ct * 256), tag, RLX_AGENT);
    u32x4 U[2][8];
    auto rdU = [&](int it, int bq) { U[it & 1][bq] = lds128(ubase + 512 * bq + 192 - 64 * it); };
    {
        LAS char* k0 = sm + S_KC + ch * 6144;
        LAS char* Rb = sm + S_P + ch * 2688;
        const int t = tid & 127, e1 = min(t + 128, 167);
        f32x4 a0 = asf(lds128(k0 + t * 16)), b0 = asf(lds128(k0 + 3072 + t * 16)), a1 = asf(lds128(k0 + e1 * 16)), b1 = asf(lds128(k0 + 3072 + e1 * 16));
        float n0 = *(LAS float*)(k0 + t * 16 + 16) + *(LAS float*)(k0 + 3072 + t * 16 + 16);
        float n1 = *(LAS float*)(k0 + e1 * 16 + 16) + *(LAS float*)(k0 + 3072 + e1 * 16 + 16);
#pragma unroll
        for (int bq = 0; bq < 8; ++bq) rdU(3, bq);
        a0 += b0; a1 += b1;
        float p0 = a0.x, p1 = a0.y, p2 = a0.z, p3 = a0.w, q0 = a1.x, q1 = a1.y, q2 = a1.z, q3 = a1.w;
        asm volatile("" : "+v"(p0), "+v"(p1), "+v"(p2), "+v"(p3), "+v"(q0), "+v"(q1), "+v"(q2), "+v"(q3));
        sts128(Rb + (163 - t) * 16, u32x4{pk(n0, p3), pk(p3, p2), pk(p2, p1), pk(p1, p0)});
        u32x4 v1 = {pk(n1, q3), pk(q3, q2), pk(q2, q1), pk(q1, q0)};
        if (t >= 36) v1 = u32x4{0, 0, 0, 0};
        if (t < 39) sts128(Rb + (t < 36 ? 35 - t : 129 + t) * 16, v1);
        if (t == 0) sts128(Rb + 164 * 16, u32x4{pk(p0, 0.f), 0, 0, 0});
    }
    HBAR(2);
    STAMP(4);
    SPRIO(PS1);
    f32x4 y[8];
    {
#define Q0(i) P0[102 - (i)]
#define Q1(i) P1[132 - (i)]
#define Q2(i) P2[130 - (i)]
#define Q3(i) P3[108 - (i)]
#define PERM __builtin_amdgcn_perm
        u32x4 T[2][8];
        u32 raw[11], rx[9];
        auto rdT = [&](int it, int k) {
            u32x4& t0 = T[it & 1][0];
            if (k == 0) { t0.x = Q0(32 * it + 6); t0.y = Q0(32 * it + 4); }
            if (k == 1) { t0.z = Q0(32 * it + 2); t0.w = Q0(32 * it); }
            if (k == 2) { raw[0] = Q1(16 * it); raw[1] = Q1(16 * it + 2); raw[2] = Q1(16 * it + 4); }
            if (k == 3) { raw[3] = Q2(8 * it); raw[4] = Q2(8 * it + 2); }
            if (k == 4) { raw[5] = Q2(8 * it + 64); raw[6] = Q2(8 * it + 66); }
            if (k == 5) { raw[7] = Q3(4 * it); raw[8] = Q3(4 * it + 32); }
            if (k == 6) { raw[9] = Q3(4 * it + 64); raw[10] = Q3(4 * it + 96); }
        };
        auto mkT = [&](int it, int aa, int e) {
            u32 v;
            if (aa == 1) v = e < 2 ? PERM(raw[2], raw[1], sel[1][2 + e]) : PERM(raw[1], raw[0], sel[1][e]);
            else if (aa < 4) v = PERM(raw[2 * aa], raw[2 * aa - 1], sel[2][e]);
            else v = PERM(raw[3 + aa], raw[3 + aa], sel[3][e]);
            T[it & 1][aa][e] = v;
        };
        auto rdX = [&](int k) {
            if (k == 0) { rx[0] = P0[102 - 262]; rx[1] = P0[102 - 260]; }
            if (k == 1) { rx[2] = P0[102 - 258]; rx[3] = P0[102 - 256]; }
            if (k == 2) { rx[4] = Q1(128); rx[5] = Q1(130); rx[6] = Q1(132); }
            if (k == 3) { rx[7] = Q2(128); rx[8] = Q2(130); }
        };
        auto fixT = [&](int k, int e) {
            const int aa = 1 << k;
            u32 lo = k == 0 ? rx[e] : k == 1 ? (e < 2 ? PERM(rx[6], rx[5], sel[1][2 + e]) : PERM(rx[5], rx[4], sel[1][e])) : PERM(rx[8], rx[7], sel[2][e]);
            T[0][aa][e] = (lo & msk[e]) | (T[0][aa][e] & ~msk[e]);
        };
#pragma unroll
        for (int k = 0; k < 7; ++k) rdT(3, k);
        SB();
#pragma unroll
        for (int it = 3; it >= 0; --it) {
            int sl = 0;
#pragma unroll
            for (int aa = 0; aa < 8; ++aa)
#pragma unroll
                for (int bq = 0; bq + aa < 8; ++bq, ++sl) {
                    y[aa + bq] = mfma(T[it & 1][aa], U[it & 1][bq], (it == 3 && aa == 0) ? f32x4{0.f, 0.f, 0.f, 0.f} : y[aa + bq]);
                    SB();
                    if (it == 3) {
                        if (sl < 14) { mkT(3, 1 + (sl >> 1), 2 * (sl & 1)); mkT(3, 1 + (sl >> 1), 2 * (sl & 1) + 1); }
                        else if (sl < 21) { rdT(2, sl - 14); rdU(2, sl - 14); }
                        else if (sl == 21) rdU(2, 7);
                        else { mkT(2, 1 + ((sl - 22) >> 1), 2 * (sl & 1)); mkT(2, 1 + ((sl - 22) >> 1), 2 * (sl & 1) + 1); }
                    } else if (it > 0) {
                        if (sl < 7) { rdT(it - 1, sl); rdU(it - 1, sl); }
                        else if (sl == 7) rdU(it - 1, 7);
                        if (it == 1 && sl >= 7 && sl < 11) rdX(sl - 7);
                        if (sl >= 10 && sl < 24) { mkT(it - 1, 1 + ((sl - 10) >> 1), 2 * (sl & 1)); mkT(it - 1, 1 + ((sl - 10) >> 1), 2 * (sl & 1) + 1); }
                        if (it == 1 && sl >= 24) fixT((sl - 24) >> 2, (sl - 24) & 3);
                    }
                    SB();
                }
        }
    }
    STAMP(5);
    SPRIO(PSV);
    {
        LAS char* yd = sm + S_TAB + wave * 8192 + lane * 16;
#pragma unroll
        for (int i = 0; i < 8; ++i) sts128(yd + i * 1024, asu(y[i]));
    }
    HBAR(3);
    {
        const float d0 = bflo(dsk), d1 = bfhi(dsk);
        u32x4 o[2];
#pragma unroll
        for (int i = 0; i < 2; ++i) {
            LAS char* ys = sm + S_TAB + (wave + 4 * i) * 1024 + lane * 16;
            f32x4 y0 = asf(lds128(ys)) + asf(lds128(ys + 8192));
            f32x4 y1 = asf(lds128(ys + 16384)) + asf(lds128(ys + 24576));
            o[i] = u32x4{pk(y0.x + bflo(uu[i].x) * d0, y1.x + bfhi(uu[i].x) * d1), pk(y0.y + bflo(uu[i].y) * d0, y1.y + bfhi(uu[i].y) * d1),
                         pk(y0.z + bflo(uu[i].z) * d0, y1.z + bfhi(uu[i].z) * d1), pk(y0.w + bflo(uu[i].w) * d0, y1.w + bfhi(uu[i].w) * d1)};
        }
        bst_dev(o[0], rws, WS_YX + pofs);
        bst_dev(o[1], rws, WS_YX + pofs + 4096);
        pubcs(o[0], o[1], WS_YC);
    }
    STAMP(6);
    WAITV(0);
    KEEP(tch0); KEEP(tch1); KEEP(tchw);
    STAMP(7);
    {
        u32x4 v[2]; u32 vc[2];
        auto ldy = [&](int i) { v[i] = bld_dev(rws, cofs, WS_YX + i * 16 * 8192); vc[i] = bld32_dev(rws, ccs, WS_YC + i * 16 * 512); };
        ldy(0); ldy(1);
        cbar();
        auto ok = [&](int i) { return rowsum(sum4(v[i])) + vc[i] == tag; };
        DBG(1) = settle<2>(badmask<2>(ok), ok,
                  [&](auto i, u32 bm) { reld<i.v>(v[i.v], dws, cofs, WS_YX + i.v * 16 * 8192, bm); reld32<i.v>(vc[i.v], dws, ccs, WS_YC + i.v * 16 * 512, bm); }, [&](int i) { pin(v[i]); pin(vc[i]); });
        STAMP(8);
        sts128(trr, v[0]); sts128(trr + 4 * 288, v[1]);
        HBAR(4);
        u32x2 t0 = *(LAS u32x2*)trw, t1 = *(LAS u32x2*)(trw + 8 * 288), t2 = *(LAS u32x2*)(trw + 16 * 288), t3 = *(LAS u32x2*)(trw + 24 * 288);
        const rsrc_t ro = mkrsrc(a.out, 2048 * 1792);
        const u32 oo = trow * 1792 + 128 * ct + (tid & 7) * 16;
        __builtin_amdgcn_raw_buffer_store_b128(u32x4{t0.x, t1.x, t2.x, t3.x}, ro, oo, 0, OAUX);
        __builtin_amdgcn_raw_buffer_store_b128(u32x4{t0.y, t1.y, t2.y, t3.y}, ro, oo + 1792, 0, OAUX);
    }
    STAMP(9);
    STAMP_FLUSH(256 + sb);
}

#if defined(__HIP_DEVICE_COMPILE__)
#define NOPK __attribute__((target("no-packed-fp32-ops")))
#else
#define NOPK
#endif
__global__ NOPK __launch_bounds__(512, 2) void fused_kernel(Args a) {
    __shared__ __attribute__((aligned(16))) char smem[163840];
    LAS char* sm = (LAS char*)smem;
    const int xcd = blockIdx.x & 7, j = ((blockIdx.x >> 3) + JROT) & 31;
    if (CPF) {
        u32 t = threadIdx.x * 128;
        asm volatile("s_cmp_lg_u32 %1, 29\n\t"
                     "s_cbranch_scc1 1f\n\t"
                     "s_getpc_b64 s[4:5]\n\t"
                     "s_and_b32 s4, s4, 0xffffff00\n\t"
                     "s_and_b32 s5, s5, 0xffff\n\t"
                     "s_mov_b32 s6, %2\n\t"
                     "s_mov_b32 s7, 0x20000\n\t"
                     "s_mov_b32 m0, 0\n\t"
                     "s_nop 0\n\t"
                     "buffer_load_dword %0, s[4:7], 0 offen lds\n"
                     "1:"
                     :: "v"(t), "s"(j), "i"(CODE_BYTES) : "s4", "s5", "s6", "s7", "scc", "memory");
    }
    const bool is_attn = (j & 1) == 0;
    if (__builtin_expect(is_attn, ATTN_LIKELY)) attn_role(a, sm, xcd, j >> 1);
    else if (j < 28) ssm_role(a, sm, xcd * 14 + (j >> 1), xcd, j >> 1);
    else if (HELP) {
        const int lane = threadIdx.x & 63, wave8 = __builtin_amdgcn_readfirstlane(threadIdx.x >> 6), t = (j >> 1 & 1) * 512 + threadIdx.x;
        u32 k[6] = {};
        if (HELP & 1) k[0] = warm_attn(a, xcd, j >> 1, wave8, lane);
        if (HELP & 2) {
#pragma unroll
            for (int i = 0; i < 2; ++i) k[1 + i] = touch(mkdesc(a.wqkv, 448 * 2688), ((t + 1024 * i) >> 2) * 2688 + ((xcd & 1) * 3 + (t & 3)) * 128);
#pragma unroll
            for (int i = 0; i < 3; ++i) k[3 + i] = touch(mkdesc(a.wo, 448 * 896), (t + 1024 * i) * 128);
        }
        WAITV(0);
#pragma unroll
        for (int i = 0; i < 6; ++i) KEEP(k[i]);
    }
}

extern "C" void kernel_launch(void* const* d_in, const int* in_sizes, int n_in,
                              void* d_out, int out_size, void* d_ws, size_t ws_size,
                              hipStream_t stream) {
    Args a;
    a.u = (const u16*)d_in[0]; a.logA = (const u16*)d_in[1]; a.Aim = (const u16*)d_in[2];
    a.Cre = (const u16*)d_in[3]; a.Cim = (const u16*)d_in[4]; a.logdt = (const u16*)d_in[5];
    a.Dskip = (const u16*)d_in[6]; a.x = (const u16*)d_in[7]; a.wqkv = (const u16*)d_in[8];
    a.wo = (const u16*)d_in[9]; a.out = (u16*)d_out; a.ws = (char*)d_ws;
    fused_kernel<<<256, 512, 0, stream>>>(a);
}
```

```cpp
#include <hip/hip_runtime.h>
#include <stdint.h>

typedef unsigned short u16;
typedef unsigned int u32;
typedef unsigned long long u64;
typedef __bf16 bf16x8 __attribute__((ext_vector_type(8)));
typedef __bf16 bf16x2 __attribute__((ext_vector_type(2)));
typedef short s16x4 __attribute__((ext_vector_type(4)));
typedef float f32x4 __attribute__((ext_vector_type(4)));
typedef float f32x2 __attribute__((ext_vector_type(2)));
typedef float f32x16 __attribute__((ext_vector_type(16)));
typedef u32 u32x2 __attribute__((ext_vector_type(2)));
typedef u32 u32x4 __attribute__((ext_vector_type(4)));
#define LAS __attribute__((address_space(3)))
#define GAS __attribute__((address_space(1)))
#define DI __device__ __forceinline__
#define RLX_AGENT __ATOMIC_RELAXED, __HIP_MEMORY_SCOPE_AGENT
#define SB() __builtin_amdgcn_sched_barrier(0)
#define WAITV(n) asm volatile("s_waitcnt vmcnt(" #n ")" ::: "memory")
#define KEEP(x) asm volatile("" :: "v"(x))
typedef __amdgpu_buffer_rsrc_t rsrc_t;
#define EXP 0
#define PA0 1
#define PA1 2
#define PS0 1
#define PS1 0
#define POLL_SLEEP 2
#define ABL 0
#define GK0 14
#define PRO 8
#define QS0 40
#define PSV 3
#define NO1 4
#define PRE 10
#define TCH 5
#define SPREAD 1
#define CPF 0
#define ATTN_LIKELY 1
#define CODE_BYTES 25600
#define JROT 0
#define MAPH 4
#define CA 2
#define CB 2
#define RDA 4
#define OAUX 16
#define OSTAG 1
#define THR 0
#define REMAP 1
#define LATE 1
#define HELP 3
#define HALFBAR 0
#define HPRIO 0
#define XT 0
#define LUMP 1
#define G2A 2
#define HDEL 0
#define DEDUP 1
#define LA 6
#define YPRIO 0
#define EARLYW 1
#define SSM_FIRST 0

#define STAMP(i) do {} while (0)
#define STAMP_DECL u32 dbgv_
#define DBG(i) dbgv_
#define STAMP_FLUSH(slot) do {} while (0)

constexpr u32 WS_EPA = 0;
constexpr u32 WS_EPS = 256;
constexpr u32 WS_UC  = 65536;
constexpr u32 WS_YC  = WS_UC + 131072;
constexpr u32 WS_KVP = 524288;
constexpr u32 WS_OX = WS_KVP + (1u << 20);
constexpr u32 WS_UX  = WS_OX + (2u << 20);
constexpr u32 WS_YX  = WS_UX + (2u << 20);
constexpr u32 WS_END = WS_YX + (2u << 20);
constexpr u32 WS_DBG = 16u << 20;
constexpr int SPIN_CAP = 1 << 16;

struct Args {
    const u16 *u, *logA, *Aim, *Cre, *Cim, *logdt, *Dskip, *x, *wqkv, *wo;
    u16* out;
    char* ws;
};

DI f32x4 mfma(u32x4 a, u32x4 b, f32x4 c) {
    return __builtin_amdgcn_mfma_f32_16x16x32_bf16(__builtin_bit_cast(bf16x8, a), __builtin_bit_cast(bf16x8, b), c, 0, 0, 0);
}
DI u32 pk2(f32x2 v) { return __builtin_bit_cast(u32, __builtin_convertvector(v, bf16x2)); }
DI u32 pk(float lo, float hi) { f32x2 v = {lo, hi}; return pk2(v); }
DI float bflo(u32 v) { return __uint_as_float(v << 16); }
DI float bfhi(u32 v) { return __uint_as_float(v & 0xffff0000u); }
DI float rbf(float f) { return bflo(pk(f, f)); }
DI rsrc_t mkrsrc(const void* p, u32 bytes) { return __builtin_amdgcn_make_buffer_rsrc((void*)p, 0, bytes, 0x00020000); }
DI u32x4 bld_dev(rsrc_t r, u32 voff, u32 soff) { return __builtin_amdgcn_raw_buffer_load_b128(r, voff, soff, 16); }
DI u32 bld32_dev(rsrc_t r, u32 voff, u32 soff) { return __builtin_amdgcn_raw_buffer_load_b32(r, voff, soff, 16); }
DI void bst_dev(u32x4 v, rsrc_t r, u32 voff) { __builtin_amdgcn_raw_buffer_store_b128(v, r, voff, 0, 16); }
DI void bst32_dev(u32 v, rsrc_t r, u32 voff) { __builtin_amdgcn_raw_buffer_store_b32(v, r, voff, 0, 16); }
DI u32x4 mkdesc(const void* p, u32 bytes) { u64 v = (u64)p; return u32x4{(u32)v, (u32)(v >> 32) & 0xffffu, bytes, 0x00020000u}; }
DI void setm0(LAS char* dst) { asm volatile("s_mov_b32 m0, %0\n\ts_nop 0" :: "s"((u32)(uintptr_t)dst) : "memory"); }
template <int OFF>
DI void dma(u32x4 desc, u32 voff, u32 soff) {
    asm volatile("buffer_load_dwordx4 %0, %1, %2 offen offset:%3 lds" :: "v"(voff), "s"(desc), "s"(soff), "i"(OFF) : "memory");
}
DI u32x4 lds128(LAS char* p) { return *(LAS u32x4*)p; }
DI void sts128(LAS char* p, u32x4 v) { *(LAS u32x4*)p = v; }
DI void sts64(LAS char* p, u32 lo, u32 hi) { u32x2 v = {lo, hi}; *(LAS u32x2*)p = v; }
DI u32x4 trfrag(LAS char* p0, LAS char* p1) {
    u32x2 a = __builtin_bit_cast(u32x2, __builtin_amdgcn_ds_read_tr16_b64_v4i16((LAS s16x4*)p0));
    u32x2 b = __builtin_bit_cast(u32x2, __builtin_amdgcn_ds_read_tr16_b64_v4i16((LAS s16x4*)p1));
    u32x4 r = {a.x, a.y, b.x, b.y};
    return r;
}
DI void cbar() { asm volatile("" ::: "memory"); }
DI void wg_barrier() { asm volatile("s_waitcnt lgkmcnt(0)\n\ts_barrier" ::: "memory"); }
DI f32x4 asf(u32x4 v) { return __builtin_bit_cast(f32x4, v); }
DI u32x4 asu(f32x4 v) { return __builtin_bit_cast(u32x4, v); }
DI u32 sum4(u32x4 v) { return v.x + v.y + v.z + v.w; }
DI u32 rowsum(u32 s) {
    s += __builtin_amdgcn_update_dpp(0, s, 0x128, 0xf, 0xf, false);
    s += __builtin_amdgcn_update_dpp(0, s, 0x124, 0xf, 0xf, false);
    s += __builtin_amdgcn_update_dpp(0, s, 0x122, 0xf, 0xf, false);
    s += __builtin_amdgcn_update_dpp(0, s, 0x121, 0xf, 0xf, false);
    return s;
}
DI u32 sum32(u32 s) {
    s = rowsum(s);
    auto t = __builtin_amdgcn_permlane16_swap(s, s, false, false);
    return t[0] + t[1];
}
DI u32 colsum(u32 s) {
    auto r = __builtin_amdgcn_permlane32_swap(s, s, false, false);
    s = r[0] + r[1];
    auto t = __builtin_amdgcn_permlane16_swap(s, s, false, false);
    return t[0] + t[1];
}
DI float phi(float t) {
    float e = __builtin_amdgcn_exp2f(t * 1.44269504089f);
    return __int_as_float(max(__float_as_int(t), 0x358637bd)) + __builtin_amdgcn_fmed3f(e, 0.f, 1.f);
}
DI u32x4 phi8(const f32x16& a, int s) { return u32x4{pk(phi(a[8 * s]), phi(a[8 * s + 1])), pk(phi(a[8 * s + 2]), phi(a[8 * s + 3])), pk(phi(a[8 * s + 4]), phi(a[8 * s + 5])), pk(phi(a[8 * s + 6]), phi(a[8 * s + 7]))}; }
DI u32x4 pk8(const f32x16& a, int s) { return u32x4{pk(a[8 * s], a[8 * s + 1]), pk(a[8 * s + 2], a[8 * s + 3]), pk(a[8 * s + 4], a[8 * s + 5]), pk(a[8 * s + 6], a[8 * s + 7])}; }
DI u32x4 quad(const f32x16& a, int r) { return asu(f32x4{a[4 * r], a[4 * r + 1], a[4 * r + 2], a[4 * r + 3]}); }
DI f32x16 zero16() { f32x16 z; for (int i = 0; i < 16; ++i) z[i] = 0.f; return z; }
DI f32x16 mfma32(u32x4 a, u32x4 b, f32x16 c) {
    return __builtin_amdgcn_mfma_f32_32x32x16_bf16(__builtin_bit_cast(bf16x8, a), __builtin_bit_cast(bf16x8, b), c, 0, 0, 0);
}
DI u32 halfsum(u32 s) { auto r = __builtin_amdgcn_permlane32_swap(s, s, false, false); return r[0] + r[1]; }
DI float colsumf(float f) {
    auto r = __builtin_amdgcn_permlane32_swap(__float_as_uint(f), __float_as_uint(f), false, false);
    f = __uint_as_float(r[0]) + __uint_as_float(r[1]);
    auto t = __builtin_amdgcn_permlane16_swap(__float_as_uint(f), __float_as_uint(f), false, false);
    return __uint_as_float(t[0]) + __uint_as_float(t[1]);
}
DI u32 ldepoch(const char* p) {
    u32 v;
    asm volatile("global_load_dword %0, %1, %2 sc1" : "=v"(v) : "v"(0u), "s"(p) : "memory");
    return v;
}
template <int BIT> DI void reld(u32x4& v, u32x4 desc, u32 voff, u32 soff, u32 mask) {
    asm volatile("s_bitcmp1_b32 %4, %5\n\ts_cbranch_scc0 1f\n\tbuffer_load_dwordx4 %0, %1, %2, %3 offen sc1\n1:"
                 : "+v"(v) : "v"(voff), "s"(desc), "s"(soff), "s"(mask), "i"(BIT) : "scc", "memory");
}
template <int BIT> DI void reld32(u32& v, u32x4 desc, u32 voff, u32 soff, u32 mask) {
    asm volatile("s_bitcmp1_b32 %4, %5\n\ts_cbranch_scc0 1f\n\tbuffer_load_dword %0, %1, %2, %3 offen sc1\n1:"
                 : "+v"(v) : "v"(voff), "s"(desc), "s"(soff), "s"(mask), "i"(BIT) : "scc", "memory");
}
DI u32 touch(u32x4 desc, u32 voff) { u32 v; asm volatile("buffer_load_dword %0, %1, %2, 0 offen" : "=v"(v) : "v"(voff), "s"(desc) : "memory"); return v; }
template <class T> DI void pin(T& v) { asm volatile("" : "+v"(v) :: "memory"); }
template <int I> struct IC { static constexpr int v = I; };
template <int N, int I = 0, class F> DI void sfor(F f) { if constexpr (I < N) { f(IC<I>{}); sfor<N, I + 1>(f); } }
template <int N, class OK, class LD, class PIN>
DI u32 settle(u32 bad, OK ok, LD ld, PIN pn) {
    u32 dbg = __builtin_popcount(bad) << 8;
#pragma unroll 1
    for (int sp = 0; bad != 0 && sp < SPIN_CAP; ++sp) {
        ++dbg;
        __builtin_amdgcn_s_sleep(POLL_SLEEP);
        sfor<N>([&](auto i) { ld(i, bad); });
        WAITV(0);
        sfor<N>([&](auto i) { pn(i.v); });
        sfor<N>([&](auto i) { if ((bad >> i.v & 1) && __all(ok(i.v))) bad &= ~(1u << i.v); });
    }
    return dbg;
}
template <int N, class OK> DI u32 badmask(OK ok) { u32 bad = 0; sfor<N>([&](auto i) { if (!__all(ok(i.v))) bad |= 1u << i.v; }); return bad; }

constexpr int A_RED = 0, A_WO = 32768, A_WQ = 98304, A_KVB = 155648;

DI void ldd(u32x4& v, u32x4 desc, u32 voff, u32 soff) {
    asm volatile("buffer_load_dwordx4 %0, %1, %2, %3 offen sc1" : "=v"(v) : "v"(voff), "s"(desc), "s"(soff) : "memory");
}
template <int N> DI void waitv() { asm volatile("s_waitcnt vmcnt(%0)" :: "i"(N) : "memory"); }
template <int N> DI void waitx(u32x4* x) { asm volatile("s_waitcnt vmcnt(%4)" : "+v"(x[0]), "+v"(x[1]), "+v"(x[2]), "+v"(x[3]) : "i"(N) : "memory"); }
constexpr int issued_before(int gs) {
    int n = PRO + gs;
    constexpr int B0 = 16 - LA;
    int cap = 16 + (gs < B0 ? 0 : 4 * ((gs - B0) / 8) + ((gs - B0) % 8 < 4 ? (gs - B0) % 8 : 4));
    if (gs > B0 + 20) cap = 29;
    if (cap > 28) cap = 29;
    n = n < cap ? n : cap;
    if (n < 29) return n;
    int q = gs - QS0;
    return 29 + (q < 0 ? 0 : q > 7 ? 7 : q);
}

constexpr int slot_ch(int gs) { return gs < 40 ? gs >> 3 : 5 + ((gs >> 2) & 1); }
constexpr int slot_kk(int gs) { return gs < 40 ? (gs & 7) >> 1 : gs & 3; }
constexpr int slot_sg(int gs) { return gs < 40 ? gs & 1 : gs >= 48; }

DI void attn_role(const Args& a, LAS char* sm, const int xcd, const int j) {
    STAMP_DECL;
    const int tid = threadIdx.x, lane = tid & 63, wave = __builtin_amdgcn_readfirstlane(tid >> 6);
    const int rw4 = wave & 3, hs = wave >> 2, t256 = tid & 255;
    constexpr int HG = 8 / MAPH;
    const int hp = (xcd % HG) * MAPH + (j % MAPH), rb = (xcd / HG) * (16 / MAPH) + j / MAPH;
    const int h = 2 * hp + hs;
    const int c0 = (CA * rb + CB * hp) % 7;
    const int r32 = lane & 31, hh = lane >> 5, blk = (lane >> 4) & 1, q = (lane >> 2) & 3, p = lane & 3;
    const u32x4 rx = mkdesc(a.x, 2048 * 896), rw = mkdesc(a.wqkv, 448 * 2688), rwo = mkdesc(a.wo, 448 * 896);
    const rsrc_t rws = mkrsrc(a.ws, WS_END);
    const u32x4 dws = mkdesc(a.ws, WS_END);
    STAMP(0);
    u32 tag = ldepoch(a.ws + WS_EPA);
    u32 tch0 = 0;
    cbar();
    const int widx = (lane & 15) ^ ((lane >> 4) << 2);
    const u32 wv = (8 * wave + (lane >> 4)) * 2688 + (1 + (widx >> 3)) * 896 + hp * 112 + min(widx & 7, 6) * 16;
    const u32 xv0 = (rb * 128 + wave * 16 + (lane >> 3)) * 896 + (((lane & 7) ^ (lane >> 4)) * 16);
    const u32 xv1 = (rb * 128 + wave * 16 + 8 + (lane >> 3)) * 896 + (((lane & 7) ^ (4 + (lane >> 4))) * 16) - 1024;
    const u32 qv = (8 * wave + (lane >> 3)) * 2688 + hp * 112 + min((lane & 7) ^ (blk << 2), 6) * 16;
    auto kchunk = [&](int i) { int kc = i + c0; return kc >= 7 ? kc - 7 : kc; };
    auto ldn = [&](int n) {
        if (n >= 36) return;
        if (n >= 29) { setm0(sm + A_WQ + (n - 29) * 8192 + wave * 1024); dma<0>(rw, qv, kchunk(n - 29) * 64 * 2688); return; }
        if (n == 28) { tch0 = touch(dws, XT ? 0x80000000u : lane < 8 ? WS_KVP + (h * 16 + rb) * 2048 + rw4 * 512 + (lane & 3) * 128
                                : lane < 24 ? WS_OX + ((rb * 4 + rw4) * 16 + h) * 2048 + (lane - 8) * 128 : 0x80000000u); return; }
        const int i = n >> 2, m = n & 3;
        if (m == 0) { setm0(sm + (i & 3) * 32768 + 16384 + wave * 2048); dma<0>(rx, xv0, kchunk(i) * 128); }
        if (m == 1) dma<1024>(rx, xv1, kchunk(i) * 128);
        if (m == 2) { setm0(sm + (i & 3) * 32768 + wave * 2048); dma<0>(rw, wv, kchunk(i) * 64 * 2688); }
        if (m == 3) dma<1024>(rw, wv, kchunk(i) * 64 * 2688 + 4 * 2688 - 1024);
    };
#pragma unroll
    for (int n = 0; n < PRO; ++n) ldn(n);
    cbar();

    const int wb = 56 * hs + 32 * blk + 8 * p;
    LAS char *wsrc[2], *xsrc[4];
#pragma unroll
    for (int sg = 0; sg < 2; ++sg) wsrc[sg] = sm + (32 * hh + q) * 256 + (((2 * sg + (wb >> 6)) ^ q) << 6) + (wb & 63);
#pragma unroll
    for (int kk = 0; kk < 4; ++kk) xsrc[kk] = sm + 16384 + (32 * rw4 + r32) * 128 + (((4 * hh + kk) ^ ((r32 >> 1) & 7)) * 16);

    const bool p3 = j < 14;
    const int r128 = p3 ? xcd * 2 + j / 7 : 0, nb = j % 7;
    f32x16 acc[2] = {zero16(), zero16()};
    u32x4 xf[28], wf[56];
    auto rdw = [&](int gs) { LAS char* s = wsrc[slot_sg(gs)] + (slot_ch(gs) & 3) * 32768 + slot_kk(gs) * 2048; wf[gs] = trfrag(s, s + 1024); };
    auto rdx = [&](int i, int kk) { xf[4 * i + kk] = lds128(xsrc[kk] + (i & 3) * 32768); };
    waitv<PRO - 4>();
    wg_barrier();
    STAMP(1);
#pragma unroll
    for (int sl = 0; sl < LA; ++sl) { if (sl < 4) rdx(0, sl); rdw(sl); }
    u32 pkd[8];
    u32 vm = r32 == 28 ? 0u : ~0u, vo = r32 == 28 ? 0x3f803f80u : 0u;
    pin(vm); pin(vo);
    if (YPRIO && hs) __builtin_amdgcn_s_setprio(1);
    sfor<56>([&](auto gs) {
        constexpr int issued = issued_before(gs.v);
        constexpr int fu = gs.v + LA, c = fu == 44 ? 6 : (fu <= 40 && (fu & 7) == 0) ? fu >> 3 : 0;
        if (c) { waitv<(issued - (4 * c + 4) > 0 ? issued - (4 * c + 4) : 0)>(); asm volatile("s_barrier" ::: "memory"); }
        static_assert(!c || issued >= 4 * c + 4, "chunk not issued yet");
        SB();
        acc[slot_sg(gs.v)] = mfma32(xf[4 * slot_ch(gs.v) + slot_kk(gs.v)], wf[gs.v], acc[slot_sg(gs.v)]);
        SB();
        if (fu < 40 && fu >= 8 && (fu & 1) == 0) rdx(fu >> 3, (fu & 7) >> 1);
        if (fu >= 40 && fu < 48) rdx(slot_ch(fu < 48 ? fu : 0), fu & 3);
        if (fu < 56) rdw(fu < 56 ? fu : 0);
        if (LA > 4 && gs.v == 44) asm volatile("s_barrier" ::: "memory");
        if constexpr (gs.v >= 49) { pkd[gs.v - 49] = pk(phi(acc[0][2 * (gs.v - 49)]), phi(acc[0][2 * (gs.v - 49) + 1])); pin(pkd[gs.v - 49]); }
#pragma unroll
        for (int n = issued; n < issued_before(gs.v + 1); ++n) ldn(n);
        SB();
    });
    STAMP(2);
    if (YPRIO && hs) __builtin_amdgcn_s_setprio(0);
    {
        pkd[7] = pk(phi(acc[0][14]), phi(acc[0][15]));
        pin(pkd[7]);
        f32x16 kv = mfma32(u32x4{pkd[0], pkd[1], pkd[2], pkd[3]}, (pk8(acc[1], 0) & vm) | vo, zero16());
        SB();
        kv = mfma32(u32x4{pkd[4], pkd[5], pkd[6], pkd[7]}, (pk8(acc[1], 1) & vm) | vo, kv);
#pragma unroll
        for (int rq = 0; rq < 4; ++rq) sts128(sm + A_RED + wave * 4096 + rq * 1024 + lane * 16, quad(kv, rq));
    }
    asm volatile("s_waitcnt vmcnt(8)" : "+v"(tag) :: "memory");
    tag += 1;
    wg_barrier();
    u32 hm = hh ? 0u : ~0u, cm = r32 == 31 ? 0u : ~0u;
    pin(hm); pin(cm);
    const bool red = (rw4 >> 1) == hs;
    if (red) {
        LAS char* rs = sm + A_RED + hs * 16384 + (rw4 & 1) * 2048 + lane * 16;
        f32x4 lo = (asf(lds128(rs)) + asf(lds128(rs + 4096))) + (asf(lds128(rs + 8192)) + asf(lds128(rs + 12288)));
        f32x4 hi = (asf(lds128(rs + 1024)) + asf(lds128(rs + 5120))) + (asf(lds128(rs + 9216)) + asf(lds128(rs + 13312)));
        u32x4 v = {pk(lo.x, lo.y), pk(lo.z, lo.w), pk(hi.x, hi.y), pk(hi.z, hi.w) & cm};
        v.w |= (tag - sum32(sum4(v))) & ~cm;
        bst_dev(v, rws, WS_KVP + (h * 16 + rb) * 2048 + (tid & 127) * 16);
    }
    cbar();
    STAMP(3);
    if (red) WAITV(1); else WAITV(0);
    KEEP(tch0);
    asm volatile("s_barrier" ::: "memory");
    STAMP(4);
    const u32 ov = (8 * wave + (lane >> 3)) * 896 + nb * 128 + (((lane & 7) ^ (blk << 2)) * 16);
    auto ldO1 = [&](int n) { setm0(sm + A_WO + n * 8192 + wave * 1024); dma<0>(rwo, ov, n * 64 * 896); };
    u32x4 gk[8];
    const u32 kbase = WS_KVP + (h * 16 + (rw4 >> 1) * 8 + (rb & 7)) * 2048, kvo = (tid & 127) * 16;
    auto ldk = [&](int i) { if (i < 8) ldd(gk[i], dws, kvo, kbase ^ (i * 2048)); };
    f32x16 aq = zero16();
    {
        LAS char* qsrc = sm + A_WQ + (32 * hh + q) * 128 + (((wb >> 6) ^ (q >> 1)) << 6) + (wb & 63);
        u32x4 wq[28];
        auto rdq = [&](int k) { LAS char* s = qsrc + (k >> 2) * 8192 + (k & 3) * 1024; wq[k] = trfrag(s, s + 512); };
#pragma unroll
        for (int k = 0; k < 4; ++k) rdq(k);
        SB();
#pragma unroll
        for (int k = 0; k < 28; ++k) {
            aq = mfma32(wq[k], xf[k], aq);
            SB();
            if (k + 4 < 28) rdq(k + 4);
            if (k < 7) ldO1(k);
            if (k >= GK0) ldk(k - GK0);
            SB();
        }
#pragma unroll
        for (int k = 28 - GK0; k < 8; ++k) ldk(k);
    }
    STAMP(5);
    u32 pqd[8];
#pragma unroll
    for (int k = 0; k < 8; ++k) {
        pqd[k] = pk(phi(aq[2 * k]), phi(aq[2 * k + 1]));
        if (k >= 6) pqd[k] &= hm;
        pin(pqd[k]);
    }
    {
        asm volatile("s_waitcnt vmcnt(0)" : "+v"(gk[0]), "+v"(gk[1]), "+v"(gk[2]), "+v"(gk[3]), "+v"(gk[4]), "+v"(gk[5]), "+v"(gk[6]), "+v"(gk[7]) :: "memory");
        u32 t = 0;
#pragma unroll
        for (int i = 0; i < 8; ++i) t += sum4(gk[i]);
        if (!__all(sum32(t) == 8u * tag)) {
            auto ok = [&](int i) { return sum32(sum4(gk[i])) == tag; };
            DBG(0) = settle<8>(badmask<8>(ok), ok, [&](auto i, u32 bm) { reld<i.v>(gk[i.v], dws, kvo, kbase ^ (i.v * 2048), bm); }, [&](int i) { pin(gk[i]); });
        }
    }
    STAMP(6);
    {
        u32x4 v;
#pragma unroll
        for (int c = 0; c < 4; ++c) {
            const float lo = ((bflo(gk[0][c]) + bflo(gk[1][c])) + (bflo(gk[2][c]) + bflo(gk[3][c]))) + ((bflo(gk[4][c]) + bflo(gk[5][c])) + (bflo(gk[6][c]) + bflo(gk[7][c])));
            const float hi = ((bfhi(gk[0][c]) + bfhi(gk[1][c])) + (bfhi(gk[2][c]) + bfhi(gk[3][c]))) + ((bfhi(gk[4][c]) + bfhi(gk[5][c])) + (bfhi(gk[6][c]) + bfhi(gk[7][c])));
            v[c] = pk(lo, hi);
        }
        sts128(sm + A_KVB + tid * 16, v);
    }
    wg_barrier();
    STAMP(7);
    {
        u32x4 kf[4];
#pragma unroll
        for (int i = 0; i < 4; ++i) kf[i] = lds128(sm + A_KVB + hs * 4096 + i * 1024 + lane * 16);
        const u32x4 pq0 = {pqd[0], pqd[1], pqd[2], pqd[3]}, pq1 = {pqd[4], pqd[5], pqd[6], pqd[7]};
        f32x16 oa = mfma32(kf[3], pq1, mfma32(kf[2], pq0, mfma32(kf[1], pq1, mfma32(kf[0], pq0, zero16()))));
        u32 dn = __float_as_uint(oa[12]);
        auto r = __builtin_amdgcn_permlane32_swap(dn, dn, false, false);
        oa *= __builtin_amdgcn_rcpf(__uint_as_float(r[1]) + 1e-6f);
        u32x4 f0 = pk8(oa, 0);
        const u32 o = WS_OX + ((rb * 4 + rw4) * 16 + h) * 2048 + lane * 16;
        bst_dev(f0, rws, o);
        SB();
        u32x4 f1 = pk8(oa, 1);
        f1.z &= hm; f1.w &= hm;
        f1.w |= (tag - halfsum(sum4(f0) + sum4(f1))) & ~hm;
        bst_dev(f1, rws, o + 1024);
    }
    cbar();
    STAMP(8);
    if (p3) {
        LAS char* osrc[2];
#pragma unroll
        for (int nt = 0; nt < 2; ++nt) osrc[nt] = sm + A_WO + (224 * hs + 4 * hh + q) * 128 + ((nt ^ (q >> 1)) * 64) + blk * 32 + 8 * p;
        const int padoff = hh ? 0 : 1024;
        u32x4 wf[8][2][2];
#pragma unroll
        for (int k = 0; k < 32; ++k) {
            const int hl = k >> 2, ks = (k >> 1) & 1, nt = k & 1;
            LAS char* s = osrc[nt] + (hl * 28 + 16 * ks) * 128;
            wf[hl][ks][nt] = trfrag(s, s + (ks ? padoff : 1024));
        }
        SB();
        WAITV(0);
        u32x4 go[16];
        const u32 ob0 = WS_OX + ((r128 * 4 + rw4) * 16 + 8 * hs) * 2048;
        auto ldo = [&](int i) { ldd(go[i], dws, lane * 16, ob0 + i * 1024); };
#pragma unroll
        for (int i = 0; i < 4 * G2A; ++i) ldo(i);
        STAMP(9);
        f32x16 ac[2];
        sfor<4>([&](auto gi) {
            constexpr int i = 4 * gi.v, nis = gi.v + G2A < 4 ? gi.v + G2A : 4;
            waitx<4 * (nis - gi.v - 1)>(go + i);
            {
                auto r0 = __builtin_amdgcn_permlane32_swap(sum4(go[i]) + sum4(go[i + 1]), sum4(go[i + 2]) + sum4(go[i + 3]), false, false);
                const u64 ne = __ballot(r0[0] + r0[1] != tag);
                if (ne) {
                    DBG(1) = settle<2>(((u32)ne != 0) | ((ne >> 32) != 0) << 1, [&](int k) { return halfsum(sum4(go[i + 2 * k]) + sum4(go[i + 2 * k + 1])) == tag; },
                               [&](auto k, u32 bm) { reld<k.v>(go[i + 2 * k.v], dws, lane * 16, ob0 + (i + 2 * k.v) * 1024, bm); reld<k.v>(go[i + 2 * k.v + 1], dws, lane * 16, ob0 + (i + 2 * k.v + 1) * 1024, bm); },
                               [&](int k) { pin(go[i + 2 * k]); pin(go[i + 2 * k + 1]); });
                }
            }
            if (gi.v == 3) STAMP(10);
            go[i + 1].w &= hm; go[i + 3].w &= hm;
            SB();
#pragma unroll
            for (int k = 0; k < 8; ++k) {
                const int hl = 2 * gi.v + (k >> 2), ks = (k >> 1) & 1, nt = k & 1;
                ac[nt] = mfma32(wf[hl][ks][nt], go[2 * hl + ks], (gi.v == 0 && k < 2) ? zero16() : ac[nt]);
                SB();
                if ((k & 1) == 0 && gi.v + G2A < 4) ldo(4 * (gi.v + G2A) + (k >> 1));
                SB();
            }
        });
        {
            LAS char* xb = sm + wave * 4096 + lane * 16;
            if (hs) {
#pragma unroll
                for (int rq = 0; rq < 4; ++rq) sts128(xb + rq * 1024, quad(ac[0], rq));
                ac[0] = ac[1];
            } else {
#pragma unroll
                for (int rq = 0; rq < 4; ++rq) sts128(xb + rq * 1024, quad(ac[1], rq));
            }
            wg_barrier();
            LAS char* pb = sm + (wave ^ 4) * 4096 + lane * 16;
            LAS char* ob = sm + A_WQ + rw4 * 4608;
#pragma unroll
            for (int rq = 0; rq < 4; ++rq) {
                const f32x4 v = asf(quad(ac[0], rq)) + asf(lds128(pb + rq * 1024));
                sts64(ob + r32 * 144 + hs * 64 + rq * 16 + hh * 8, pk(v.x, v.y), pk(v.z, v.w));
            }
            wg_barrier();
            const rsrc_t ro = mkrsrc(a.out, 2048 * 1792);
            const int orow = 16 * hs + (lane >> 3);
            const u32 oo = (r128 * 128 + rw4 * 32 + orow) * 1792 + 896 + nb * 128 + (lane & 7) * 16;
            const u32x4 v0 = lds128(ob + orow * 144 + (lane & 7) * 16), v1 = lds128(ob + (8 + orow) * 144 + (lane & 7) * 16);
            __builtin_amdgcn_raw_buffer_store_b128(v0, ro, oo, 0, OAUX);
            __builtin_amdgcn_raw_buffer_store_b128(v1, ro, oo + 8 * 1792, 0, OAUX);
        }
        if (xcd == 0 && j == 0 && tid == 0) __hip_atomic_store((GAS u32*)(a.ws + WS_EPA), tag, RLX_AGENT);
        STAMP(11);
    }
    STAMP_FLUSH(j * 8 + xcd);
}

constexpr int S_TAB = 0;
constexpr int S_KC = 34816;
constexpr int S_P = 47104;
constexpr int S_UT = 52480;
constexpr int S_TR = 61696;
constexpr int S_BAR = 70912;

typedef _Float16 h2 __attribute__((ext_vector_type(2)));
typedef _Float16 h8 __attribute__((ext_vector_type(8)));
struct c2 { f32x2 r, i; };
struct hc { h2 r, i; };
DI c2 csq(c2 a) { return c2{a.r * a.r - a.i * a.i, 2.f * a.r * a.i}; }
DI hc hmul(hc a, hc b) { return hc{a.r * b.r - a.i * b.i, a.r * b.i + a.i * b.r}; }
DI hc toh(c2 a) { return hc{__builtin_convertvector(a.r, h2), __builtin_convertvector(a.i, h2)}; }
DI hc tohc(c2 a) { return hc{__builtin_convertvector(a.r, h2), __builtin_convertvector(-a.i, h2)}; }
DI c2 cexp2(f32x2 lr, f32x2 rev, float pw) {
    f32x2 e = lr * pw, f = rev * pw;
    f32x2 m = {__builtin_amdgcn_exp2f(e.x), __builtin_amdgcn_exp2f(e.y)};
    f.x = __builtin_amdgcn_fractf(f.x); f.y = __builtin_amdgcn_fractf(f.y);
    f32x2 c = {__builtin_amdgcn_cosf(f.x), __builtin_amdgcn_cosf(f.y)}, s = {__builtin_amdgcn_sinf(f.x), __builtin_amdgcn_sinf(f.y)};
    return c2{m * c, m * s};
}
DI f32x2 unpk(u32 v) { return f32x2{bflo(v), bfhi(v)}; }
DI f32x4 mfmah(u32x4 a, u32x4 b, f32x4 c) {
    return __builtin_amdgcn_mfma_f32_16x16x32_f16(__builtin_bit_cast(h8, a), __builtin_bit_cast(h8, b), c, 0, 0, 0);
}
constexpr int tri_a(int sl) { int a = 0; while (sl >= 8 - a) { sl -= 8 - a; ++a; } return a; }
constexpr int tri_b(int sl) { int a = 0; while (sl >= 8 - a) { sl -= 8 - a; ++a; } return sl; }
DI u32 wavemax(u32 s) {
    s = max(s, (u32)__builtin_amdgcn_update_dpp(0, s, 0x128, 0xf, 0xf, false));
    s = max(s, (u32)__builtin_amdgcn_update_dpp(0, s, 0x124, 0xf, 0xf, false));
    s = max(s, (u32)__builtin_amdgcn_update_dpp(0, s, 0x122, 0xf, 0xf, false));
    s = max(s, (u32)__builtin_amdgcn_update_dpp(0, s, 0x121, 0xf, 0xf, false));
    auto r = __builtin_amdgcn_permlane32_swap(s, s, false, false);
    s = max(r[0], r[1]);
    auto t = __builtin_amdgcn_permlane16_swap(s, s, false, false);
    return max(t[0], t[1]);
}

DI void touch_lds(u32x4 desc, u32 voff) { asm volatile("buffer_load_dword %0, %1, 0 offen lds" :: "v"(voff), "s"(desc) : "memory"); }
DI void warm_attn(const Args& a, LAS char* sm, int xcd, int hi) {
    const int lane = threadIdx.x & 63, wave8 = __builtin_amdgcn_readfirstlane(threadIdx.x >> 6);
    const int u = hi * 5 + wave8, idx = (u - 24) * 64 + lane;
    if (wave8 >= 5) return;
    setm0(sm + 163840 - 256);
    if (u < 24) {
        const int r = u / 6, rbl = (xcd >> 1) * 4 + r, kc = (2 * (rbl + (xcd & 1) * 4) + 1 + 2 * ((u % 6) >> 1)) % 7;
        touch_lds(mkdesc(a.x, 2048 * 896), (rbl * 128 + (u & 1) * 64 + lane) * 896 + kc * 128);
    } else touch_lds(mkdesc(a.wqkv, 448 * 2688), (idx >> 3) * 2688 + (1 + ((idx >> 2) & 1)) * 896 + ((xcd & 1) * 3 + (idx & 3)) * 128);
}

DI void half_barrier(LAS char* cnt, u32 target) {
    asm volatile("s_waitcnt lgkmcnt(0)" ::: "memory");
    if ((threadIdx.x & 63) == 0) __hip_atomic_fetch_add((LAS u32*)cnt, 1u, __ATOMIC_RELAXED, __HIP_MEMORY_SCOPE_WORKGROUP);
    while (__builtin_amdgcn_readfirstlane(__hip_atomic_load((LAS u32*)cnt, __ATOMIC_RELAXED, __HIP_MEMORY_SCOPE_WORKGROUP)) < target) __builtin_amdgcn_s_sleep(0);
    cbar();
}
#define HBAR(k) wg_barrier()
#define SPRIO(x) do {} while (0)

DI void ssm_role(const Args& a, LAS char* sm0, const int sb, const int xcd, const int hi) {
    STAMP_DECL;
    const int tid = threadIdx.x & 255, lane = tid & 63, wave8 = __builtin_amdgcn_readfirstlane(threadIdx.x >> 6), wave = wave8 & 3;
    LAS char* sm = sm0 + (wave8 >> 2) * 81920;
    if (HPRIO && wave8 < 4) __builtin_amdgcn_s_setprio(2);
    const int sidx = 2 * sb + (wave8 >> 2);
    const int ct = sidx >> 5, j = sidx & 31;
    const int ch = wave >> 1, hf = wave & 1, d = ct * 64 + 2 * j + ch;
    const int g = lane >> 4, r16 = lane & 15;
    STAMP(0);
    SPRIO(PSV);
    const int trow = 256 * (j >> 2) + 16 * (tid >> 4) + 4 * (j & 3) + 2 * ((tid >> 3) & 1);
    const u32 po = (d * 128 + hf * 64 + lane) * 4, uo = trow * 896 + 128 * ct + (tid & 7) * 16;
    const u32 ldt = a.logdt[d];
    const u32 pA = *(const u32*)((const char*)a.logA + po), pI = *(const u32*)((const char*)a.Aim + po);
    const u32x4 ua = *(const u32x4*)((const char*)a.u + uo), ub = *(const u32x4*)((const char*)a.u + uo + 896);
    const u32 pCr = *(const u32*)((const char*)a.Cre + po), pCi = *(const u32*)((const char*)a.Cim + po);
    const u32 dsk = ((const u32*)a.Dskip)[ct * 32 + j];
    cbar();
    u32 tag = ldepoch(a.ws + WS_EPS + ct * 256);
    if (!EARLYW) warm_attn(a, sm0, xcd, hi);
    const rsrc_t rws = mkrsrc(a.ws, WS_END);
    const u32x4 dws = mkdesc(a.ws, WS_END);
    u32 tch0 = 0, tch1 = 0;
    *(LAS u32*)(sm + S_UT + (tid >> 7) * 4608 + (tid & 127) * 4) = 0;
    if (tid == 0) *(LAS u32*)(sm + S_BAR) = 0;
    const u32 pofs = (ct * 32 + j) * 8192 + tid * 16;
    const u32 cofs = (ct * 32 + (tid >> 4)) * 8192 + j * 256 + (tid & 15) * 16;
    const u32 pcs = r16 < 2 ? ((ct * 32 + j) * 4 + wave) * 128 + g * 8 + (tid & 1) * 4 : 0x80000000u;
    const u32 ccs = ((ct * 32 + (tid >> 4)) * 4 + ((j >> 2) & 3)) * 128 + (j & 3) * 8 + (j >> 4) * 4;
    auto pubcs = [&](u32x4 v0, u32x4 v1, u32 cbase) {
        const u32 s0 = rowsum(sum4(v0)), s1 = rowsum(sum4(v1));
        bst32_dev(tag - (r16 ? s1 : s0), rws, cbase + pcs);
    };
    const int w = 16 + r16 - 8 * g;
    u32x4 bE, bO, mkb, mkd;
    u32 sel[4][4], msk[4], pb[4];
    {
        u32 y[4];
#pragma unroll
        for (int e = 0; e < 4; ++e) {
            y[e] = (u32)(w + 64 - 2 * e) * 0x10001u - 0x10000u;
            msk[e] = hf ? 0u : (((y[e] >> 6) & 0x10001u) ^ 0x10001u) * 0xffffu;
        }
        pb[0] = w - 7;
#pragma unroll
        for (int s = 1; s < 3; ++s) {
            u32 r[4];
#pragma unroll
            for (int e = (s == 1 ? 2 : 0); e < 4; ++e)
                r[e] = ((y[e] + ((1u << (s - 1)) - 1u) * 0x10001u + ((y[e] >> s) & 0x10001u)) >> s) & 0x00ff00ffu;
            const u32 base = r[3] >> 16;
            pb[s] = base - (64 >> s);
#pragma unroll
            for (int e = (s == 1 ? 2 : 0); e < 4; ++e) sel[s][e] = (((r[e] - base * 0x10001u) ^ 0x10001u) & 0xfffffu) * 0x202u + 0x01000100u;
        }
#pragma unroll
        for (int e = 0; e < 4; ++e) {
            const int z = min(max(16 - w + 2 * e, 0), 20);
            auto two = [&](u32 m) { const u32 b2 = (m >> z) & 3u; return (b2 | (b2 << 15)) & 0x10001u; };
            bE[e] = two(0x1ff0u) * 0x3f80u; bO[e] = two(0x0fe0u) * 0x3f80u; mkb[e] = two(0x1f000u) * 0xffffu;
            mkd[e] = r16 - 4 * g - e >= 0 ? ~0u : 0u;
        }
        asm volatile("" : "+v"(sel[1][2]), "+v"(sel[1][3]), "+v"(sel[2][0]), "+v"(sel[2][1]), "+v"(sel[2][2]), "+v"(sel[2][3]), "+v"(bE), "+v"(bO), "+v"(mkb), "+v"(mkd));
        asm volatile("" : "+v"(msk[0]), "+v"(msk[1]), "+v"(msk[2]), "+v"(msk[3]));
    }
    LAS u32* const R = (LAS u32*)(sm + S_P + ch * 2688) + 655;
    LAS u32 *P0 = R - (128 * hf + (int)pb[0]) - 102, *P1 = R - (256 + 64 * hf + (int)pb[1]) - 132, *P2 = R - (384 + 32 * hf + (int)pb[2]) - 130, *P3 = R - (385 + 2 * (r16 - 4 * g)) - 224;
    LAS char* ubase = sm + S_UT + ch * 4608 + 480 + r16 * 32 + g * 16 - 256 * hf - 192;
    asm volatile("" : "+v"(P0), "+v"(P1), "+v"(P2), "+v"(P3), "+v"(ubase));
    LAS char* trw = sm + S_TR + (tid & 7) * 288 + (tid >> 3) * 8;
    LAS char* trr = sm + S_TR + (((tid >> 4) & 3) * 8 + (tid >> 6)) * 288 + (tid & 15) * 16;
    LAS char* tab = sm + S_TAB + wave * 8704;
    LAS char* tw = tab + lane * 8;
    LAS char* tr = tab + r16 * 544 + g * 16;
    LAS char* kc = sm + S_KC + wave * 3072;
    SB();
#define EMIT2(row, v0, v1) do { SB(); sts64(tw + (row) * 544, __builtin_bit_cast(u32, (v0).r), __builtin_bit_cast(u32, (v0).i)); \
                                sts64(tw + ((row) + 1) * 544, __builtin_bit_cast(u32, (v1).r), __builtin_bit_cast(u32, (v1).i)); SB(); } while (0)
#define KRD() do { SB(); _Pragma("unroll") for (int ks = 0; ks < 8; ++ks) gf[ks] = lds128(tr + ks * 64); cbar(); SB(); } while (0)
#define KM2(k) do { ka = mfmah(gf[2 * (k)], ff[2 * (k)], (k) ? ka : f32x4{0.f, 0.f, 0.f, 0.f}); ka = mfmah(gf[2 * (k) + 1], ff[2 * (k) + 1], ka); } while (0)
    const float L2E = 1.44269504089f;
    const float dt = rbf(__builtin_amdgcn_exp2f(bflo(ldt) * L2E));
    f32x2 lr = unpk(pA) * L2E;
    lr = unpk(pk(__builtin_amdgcn_exp2f(lr.x), __builtin_amdgcn_exp2f(lr.y))) * dt * -L2E;
    const f32x2 rev = unpk(pI) * dt * 0.15915494309f;
    SB();
    sts64(trw, ua.x, ub.x); sts64(trw + 8 * 288, ua.y, ub.y); sts64(trw + 16 * 288, ua.z, ub.z); sts64(trw + 24 * 288, ua.w, ub.w);
    SB();
    hc W[8], F[16], G[17], H, t0, t1;
    u32x4 ff[8], gf[8];
    f32x4 ka;
    c2 e = cexp2(lr, -rev, 8.f);
    F[1] = toh(e); e = csq(e); F[2] = toh(e);
    asm volatile("s_waitcnt vmcnt(%1)" : "+v"(tag) : "i"(0) : "memory");
    tag += 1;
    wg_barrier();
    const u32x4 up0 = lds128(trr), up1 = lds128(trr + 4 * 288);
    F[0] = hc{h2{1.f16, 1.f16}, h2{0.f16, 0.f16}};
    EMIT2(0, F[0], F[1]);
    e = csq(e); F[4] = toh(e); F[3] = hmul(F[1], F[2]);
    bst_dev(up0, rws, WS_UX + pofs);
    EMIT2(2, F[2], F[3]);
    e = csq(e); F[8] = toh(e); F[5] = hmul(F[4], F[1]);
    EMIT2(4, F[4], F[5]);
    F[6] = hmul(F[4], F[2]); F[7] = hmul(F[4], F[3]); F[9] = hmul(F[8], F[1]);
    bst_dev(up1, rws, WS_UX + pofs + 4096);
    EMIT2(6, F[6], F[7]);
    pubcs(up0, up1, WS_UC);
    u32 tchx = 0;
    if (XT) {
        const int u = hi * 3 + wave8, un = u * 64 + lane, v = un - 512;
        const int h0 = (xcd & 1) * 8, r0 = (xcd >> 1) * 4;
        tchx = touch(dws, wave8 >= 3 || un >= 2560 ? 0x80000000u
                        : un < 512 ? WS_KVP + ((h0 + (un >> 6)) * 16 + r0) * 2048 + (un & 63) * 128
                                   : WS_OX + ((r0 * 8 + (v >> 6)) * 16 + h0) * 1024 + (v & 63) * 128);
    }
    cbar();
    SPRIO(PS0);
    STAMP(1);
    EMIT2(8, F[8], F[9]);
    F[10] = hmul(F[8], F[2]); F[11] = hmul(F[8], F[3]);
    e = cexp2(lr, rev, 1.f); W[1] = toh(e);
    EMIT2(10, F[10], F[11]);
    F[12] = hmul(F[8], F[4]); F[13] = hmul(F[8], F[5]);
    e = csq(e); W[2] = toh(e);
    EMIT2(12, F[12], F[13]);
    F[14] = hmul(F[8], F[6]); F[15] = hmul(F[8], F[7]);
    e = csq(e); W[4] = toh(e);
    EMIT2(14, F[14], F[15]);
    u32 ce = max(max(pCr & 0x7fffu, (pCr >> 16) & 0x7fffu), max(pCi & 0x7fffu, (pCi >> 16) & 0x7fffu));
    ce = min(max(wavemax(ce) >> 7, 1u), 253u);
    const float cdn = __uint_as_float((254u - ce) << 23), cup = __uint_as_float(ce << 23) * (2.f * dt);
    G[0] = toh(c2{unpk(pCr) * cdn, unpk(pCi) * cdn});
    t1 = hmul(G[0], W[1]);
    SB();
#pragma unroll
    for (int ks = 0; ks < 8; ++ks) ff[ks] = lds128(tr + ks * 64);
    cbar();
    EMIT2(0, G[0], t1);
    W[3] = hmul(W[1], W[2]); t0 = hmul(G[0], W[2]); t1 = hmul(G[0], W[3]);
    EMIT2(2, t0, t1);
    W[5] = hmul(W[4], W[1]); t0 = hmul(G[0], W[4]); t1 = hmul(G[0], W[5]);
    EMIT2(4, t0, t1);
    W[6] = hmul(W[4], W[2]); W[7] = hmul(W[4], W[3]); t0 = hmul(G[0], W[6]); t1 = hmul(G[0], W[7]);
    EMIT2(6, t0, t1);
    e = cexp2(lr, rev, 128.f); H = toh(e);
    G[1] = hmul(G[0], H); t1 = hmul(G[1], W[1]);
    EMIT2(8, G[1], t1);
    t0 = hmul(G[1], W[2]); t1 = hmul(G[1], W[3]);
    e = csq(e); H = toh(e);
    EMIT2(10, t0, t1);
    t0 = hmul(G[1], W[4]); t1 = hmul(G[1], W[5]); G[2] = hmul(G[0], H);
    EMIT2(12, t0, t1);
    t0 = hmul(G[1], W[6]); t1 = hmul(G[1], W[7]); G[3] = hmul(G[1], H);
    EMIT2(14, t0, t1);
    t1 = hmul(G[2], W[2]);
    e = csq(e); H = toh(e);
    KRD();
    EMIT2(0, G[2], t1);
    t0 = hmul(G[2], W[4]); t1 = hmul(G[2], W[6]); G[4] = hmul(G[0], H);
    EMIT2(2, t0, t1);
    t1 = hmul(G[3], W[2]); G[5] = hmul(G[1], H); KM2(0);
    EMIT2(4, G[3], t1);
    t0 = hmul(G[3], W[4]); t1 = hmul(G[3], W[6]); KM2(1);
    EMIT2(6, t0, t1);
    t1 = hmul(G[4], W[4]); G[6] = hmul(G[2], H); KM2(2);
    EMIT2(8, G[4], t1);
    t1 = hmul(G[5], W[4]); G[7] = hmul(G[3], H); KM2(3);
    EMIT2(10, G[5], t1);
    STAMP(2);
    u32x4 uu[2]; u32 uc[2];
    SPRIO(PSV);
#pragma unroll
    for (int i = 0; i < 2; ++i) { uu[i] = bld_dev(rws, cofs, WS_UX + i * 16 * 8192); uc[i] = bld32_dev(rws, ccs, WS_UC + i * 16 * 512); }
    cbar();
    SPRIO(PS0);
    t1 = hmul(G[6], W[4]);
    e = csq(e); H = toh(e);
    EMIT2(12, G[6], t1);
    t1 = hmul(G[7], W[4]); G[8] = hmul(G[0], H);
    sts128(kc + (128 * (g >> 1) + 4 * (g & 1) + 8 * r16) * 4, asu(ka * cup));
    EMIT2(14, G[7], t1);
    G[9] = hmul(G[1], H); G[10] = hmul(G[2], H);
    KRD();
    EMIT2(0, G[8], G[9]);
    G[11] = hmul(G[3], H); G[12] = hmul(G[4], H); G[13] = hmul(G[5], H);
    EMIT2(2, G[10], G[11]);
    G[14] = hmul(G[6], H); KM2(0); KM2(1);
    EMIT2(4, G[12], G[13]);
    G[15] = hmul(G[7], H); G[16] = hmul(G[8], H); KM2(2); KM2(3);
    EMIT2(6, G[14], G[15]);
    sts64(tw + 8 * 544, __builtin_bit_cast(u32, G[16].r), __builtin_bit_cast(u32, G[16].i));
    KRD();
    {
        LAS char* k0 = kc + (g < 2 ? 256 + 64 * g + 4 * r16 : 384 + 64 * (g - 2) + 2 * r16) * 4;
        u32x4 kb = asu(ka * cup);
        sts64(k0, kb.x, kb.y);
        sts64(k0 + (g < 2 ? 8 : 128), kb.z, kb.w);
    }
    SB();
    KM2(0); KM2(1); KM2(2); KM2(3);
    pin(ka);
    {
        auto ok = [&](int i) { return rowsum(sum4(uu[i])) + uc[i] == tag; };
        DBG(0) = settle<2>(badmask<2>(ok), ok,
                  [&](auto i, u32 bm) { reld<i.v>(uu[i.v], dws, cofs, WS_UX + i.v * 16 * 8192, bm); reld32<i.v>(uc[i.v], dws, ccs, WS_UC + i.v * 16 * 512, bm); }, [&](int i) { pin(uu[i]); pin(uc[i]); });
    }
    {
        LAS float* k0 = (LAS float*)(kc + (512 + 64 * g + r16) * 4);
        k0[0] = ka.x * cup; k0[16] = ka.y * cup; k0[32] = ka.z * cup; k0[48] = ka.w * cup;
    }
#pragma unroll
    for (int i = 0; i < 2; ++i) {
        LAS char* ud = sm + S_UT + 512 + (4 * i + wave) * 512 + r16 * 32 + g * 8;
        sts64(ud, __builtin_amdgcn_perm(uu[i].y, uu[i].x, 0x05040100u), __builtin_amdgcn_perm(uu[i].w, uu[i].z, 0x05040100u));
        sts64(ud + 4608, __builtin_amdgcn_perm(uu[i].y, uu[i].x, 0x07060302u), __builtin_amdgcn_perm(uu[i].w, uu[i].z, 0x07060302u));
    }
    HBAR(1);
    STAMP(3);
    if (TCH & 4) tch0 = touch(dws, lane < 16 ? WS_YX + (ct * 32 + j) * 8192 + (lane & 8) * 512 + wave * 1024 + (lane & 7) * 128
                                : lane == 16 ? WS_YC + ((ct * 32 + j) * 4 + wave) * 128 : 0x80000000u);
    if (TCH & 2) tch1 = touch(mkdesc(a.out, 2048 * 1792), (lane & 6) == 0 ? (trow + (lane & 1)) * 1792 + 128 * ct : 0x80000000u);
    if (j == 0 && tid == 0) __hip_atomic_store((GAS u32*)(a.ws + WS_EPS + ct * 256), tag, RLX_AGENT);
    u32x4 U[2][8];
    auto rdU = [&](int it, int bq) { U[it & 1][bq] = lds128(ubase + 512 * bq + 192 - 64 * it); };
    {
        LAS char* k0 = sm + S_KC + ch * 6144;
        LAS char* Rb = sm + S_P + ch * 2688;
        const int t = tid & 127, e1 = min(t + 128, 167);
        f32x4 a0 = asf(lds128(k0 + t * 16)), b0 = asf(lds128(k0 + 3072 + t * 16)), a1 = asf(lds128(k0 + e1 * 16)), b1 = asf(lds128(k0 + 3072 + e1 * 16));
        float n0 = *(LAS float*)(k0 + t * 16 + 16) + *(LAS float*)(k0 + 3072 + t * 16 + 16);
        float n1 = *(LAS float*)(k0 + e1 * 16 + 16) + *(LAS float*)(k0 + 3072 + e1 * 16 + 16);
#pragma unroll
        for (int bq = 0; bq < 8; ++bq) rdU(3, bq);
        a0 += b0; a1 += b1;
        float p0 = a0.x, p1 = a0.y, p2 = a0.z, p3 = a0.w, q0 = a1.x, q1 = a1.y, q2 = a1.z, q3 = a1.w;
        asm volatile("" : "+v"(p0), "+v"(p1), "+v"(p2), "+v"(p3), "+v"(q0), "+v"(q1), "+v"(q2), "+v"(q3));
        sts128(Rb + (163 - t) * 16, u32x4{pk(n0, p3), pk(p3, p2), pk(p2, p1), pk(p1, p0)});
        u32x4 v1 = {pk(n1, q3), pk(q3, q2), pk(q2, q1), pk(q1, q0)};
        if (t >= 36) v1 = u32x4{0, 0, 0, 0};
        if (t < 39) sts128(Rb + (t < 36 ? 35 - t : 129 + t) * 16, v1);
        if (t == 0) sts128(Rb + 164 * 16, u32x4{pk(p0, 0.f), 0, 0, 0});
    }
    HBAR(2);
    STAMP(4);
    SPRIO(PS1);
    f32x4 y[8];
    {
#define Q0(i) P0[102 - (i)]
#define Q1(i) P1[132 - (i)]
#define Q2(i) P2[130 - (i)]
#define Q3(i) P3[224 - (i)]
#define PERM __builtin_amdgcn_perm
        u32x4 T[2][4], T32, TB[4], UW[4], WA[2];
        f32x4 wd[4];
        u32 raw[7], rx[10];
        auto rdT = [&](int it, int k) {
            u32x4& t0 = T[it & 1][0];
            if (k == 0) { t0.x = Q0(32 * it + 6); t0.y = Q0(32 * it + 4); }
            if (k == 1) { t0.z = Q0(32 * it + 2); t0.w = Q0(32 * it); }
            if (k == 2) { raw[0] = Q1(16 * it); raw[1] = Q1(16 * it + 2); raw[2] = Q1(16 * it + 4); }
            if (k == 3) { raw[3] = Q2(8 * it); raw[4] = Q2(8 * it + 2); }
            if (k == 4) { raw[5] = Q2(8 * it + 64); raw[6] = Q2(8 * it + 66); }
        };
        auto mkT = [&](int it, int aa, int e) {
            T[it & 1][aa][e] = aa == 1 ? (e < 2 ? PERM(raw[2], raw[1], sel[1][2 + e]) : PERM(raw[1], raw[0], sel[1][e])) : PERM(raw[2 * aa], raw[2 * aa - 1], sel[2][e]);
        };
        auto rdX = [&](int k) {
            if (k == 0) { rx[0] = P0[102 - 262]; rx[1] = P0[102 - 260]; }
            if (k == 1) { rx[2] = P0[102 - 258]; rx[3] = P0[102 - 256]; }
            if (k == 2) { rx[4] = Q1(128); rx[5] = Q1(130); rx[6] = Q1(132); }
            if (k == 3) { rx[7] = Q2(128); rx[8] = Q2(130); rx[9] = R[-512]; }
        };
        auto fixT = [&](int k, int e) {
            if (k < 2) {
                const u32 lo = k == 0 ? rx[e] : (e < 2 ? PERM(rx[6], rx[5], sel[1][2 + e]) : PERM(rx[5], rx[4], sel[1][e]));
                T[0][1 + k][e] = (lo & msk[e]) | (T[0][1 + k][e] & ~msk[e]);
            } else T32[e] = (PERM(rx[8], rx[7], sel[2][e]) & msk[e]) | (PERM(rx[9], rx[9], 0x03020302u) & mkb[e]);
        };
        LAS char* uw = ubase + 192 + 256 * hf + 1024 * (1 - hf);
        auto rdC = [&](int k) {
            if (k < 4) UW[k] = lds128(uw + 512 * (k >> 1) - 16 * (k & 1));
            else { const int dl = (k - 4) >> 1, e = 2 * (k & 1); TB[dl][e] = Q3(128 + 32 * dl - 2 * e); TB[dl][e + 1] = Q3(128 + 32 * dl - 2 * e - 2); }
        };
#pragma unroll
        for (int k = 0; k < 5; ++k) rdT(3, k);
        SB();
        sfor<104>([&](auto gs) {
            constexpr int it = 3 - gs.v / 26, sl = gs.v % 26, aa = tri_a(sl), bq = tri_b(sl);
            y[aa + bq] = mfma(T[it & 1][aa], U[it & 1][bq], (it == 3 && aa == 0) ? f32x4{0.f, 0.f, 0.f, 0.f} : y[aa + bq]);
            SB();
            if constexpr (it == 3) {
                if constexpr (sl < 6) { mkT(3, 1 + (sl >> 1), 2 * (sl & 1)); mkT(3, 1 + (sl >> 1), 2 * (sl & 1) + 1); }
                else if constexpr (sl < 14) { if (sl < 11) rdT(2, sl - 6); rdU(2, sl - 6); }
                else mkT(2, 1 + ((sl - 14) >> 2), (sl - 14) & 3);
            } else if constexpr (it > 0) {
                if constexpr (sl < 8) { if (sl < 5) rdT(it - 1, sl); rdU(it - 1, sl); }
                if constexpr (it == 1 && sl >= 5 && sl < 9) rdX(sl - 5);
                if constexpr (it == 2 && sl >= 10 && sl < 22) mkT(1, 1 + ((sl - 10) >> 2), (sl - 10) & 3);
                if constexpr (it == 1 && sl >= 10 && sl < 16) { mkT(0, 1 + ((sl - 10) >> 1), 2 * (sl & 1)); mkT(0, 1 + ((sl - 10) >> 1), 2 * (sl & 1) + 1); }
                if constexpr (it == 1 && sl >= 16 && sl < 24) fixT((sl - 16) >> 2, (sl - 16) & 3);
            } else {
                if constexpr (sl < 12) rdC(sl);
                if constexpr (sl < 4) fixT(2, sl);
                if constexpr (sl >= 10 && sl < 14) wd[sl - 10] = mfma(UW[sl - 10], (sl & 1) ? bE : bO, f32x4{0.f, 0.f, 0.f, 0.f});
                if constexpr (sl >= 16 && sl < 24) WA[(sl - 16) >> 2][sl & 3] = pk(wd[2 * ((sl - 16) >> 2) + 1][sl & 3], wd[2 * ((sl - 16) >> 2)][sl & 3]);
                if constexpr (sl == 24) TB[0] &= mkd;
            }
            SB();
        });
        if (hf) {
#pragma unroll
            for (int k = 0; k < 4; ++k) y[4 + k] = mfma(WA[0], TB[k], y[4 + k]);
#pragma unroll
            for (int k = 0; k < 3; ++k) y[5 + k] = mfma(WA[1], TB[k], y[5 + k]);
        } else {
#pragma unroll
            for (int k = 0; k < 4; ++k) y[4 + k] = mfma(T32, U[0][k], y[4 + k]);
            y[6] = mfma(WA[0], TB[0], y[6]); y[7] = mfma(WA[0], TB[1], y[7]); y[7] = mfma(WA[1], TB[0], y[7]);
        }
    }
    STAMP(5);
    SPRIO(PSV);
    {
        LAS char* yd = sm + S_TAB + wave * 8192 + lane * 16;
#pragma unroll
        for (int i = 0; i < 8; ++i) sts128(yd + i * 1024, asu(y[i]));
    }
    HBAR(3);
    {
        const float d0 = bflo(dsk), d1 = bfhi(dsk);
        u32x4 o[2];
#pragma unroll
        for (int i = 0; i < 2; ++i) {
            LAS char* ys = sm + S_TAB + (wave + 4 * i) * 1024 + lane * 16;
            f32x4 y0 = asf(lds128(ys)) + asf(lds128(ys + 8192));
            f32x4 y1 = asf(lds128(ys + 16384)) + asf(lds128(ys + 24576));
            o[i] = u32x4{pk(y0.x + bflo(uu[i].x) * d0, y1.x + bfhi(uu[i].x) * d1), pk(y0.y + bflo(uu[i].y) * d0, y1.y + bfhi(uu[i].y) * d1),
                         pk(y0.z + bflo(uu[i].z) * d0, y1.z + bfhi(uu[i].z) * d1), pk(y0.w + bflo(uu[i].w) * d0, y1.w + bfhi(uu[i].w) * d1)};
        }
        bst_dev(o[0], rws, WS_YX + pofs);
        bst_dev(o[1], rws, WS_YX + pofs + 4096);
        pubcs(o[0], o[1], WS_YC);
    }
    STAMP(6);
    WAITV(0);
    KEEP(tch0); KEEP(tch1); KEEP(tchx);
    STAMP(7);
    {
        u32x4 v[2]; u32 vc[2];
        auto ldy = [&](int i) { v[i] = bld_dev(rws, cofs, WS_YX + i * 16 * 8192); vc[i] = bld32_dev(rws, ccs, WS_YC + i * 16 * 512); };
        ldy(0); ldy(1);
        cbar();
        auto ok = [&](int i) { return rowsum(sum4(v[i])) + vc[i] == tag; };
        DBG(1) = settle<2>(badmask<2>(ok), ok,
                  [&](auto i, u32 bm) { reld<i.v>(v[i.v], dws, cofs, WS_YX + i.v * 16 * 8192, bm); reld32<i.v>(vc[i.v], dws, ccs, WS_YC + i.v * 16 * 512, bm); }, [&](int i) { pin(v[i]); pin(vc[i]); });
        STAMP(8);
        sts128(trr, v[0]); sts128(trr + 4 * 288, v[1]);
        HBAR(4);
        u32x2 t0 = *(LAS u32x2*)trw, t1 = *(LAS u32x2*)(trw + 8 * 288), t2 = *(LAS u32x2*)(trw + 16 * 288), t3 = *(LAS u32x2*)(trw + 24 * 288);
        const rsrc_t ro = mkrsrc(a.out, 2048 * 1792);
        const u32 oo = trow * 1792 + 128 * ct + (tid & 7) * 16;
        __builtin_amdgcn_raw_buffer_store_b128(u32x4{t0.x, t1.x, t2.x, t3.x}, ro, oo, 0, OAUX);
        __builtin_amdgcn_raw_buffer_store_b128(u32x4{t0.y, t1.y, t2.y, t3.y}, ro, oo + 1792, 0, OAUX);
    }
    STAMP(9);
    STAMP_FLUSH(256 + sb);
}

#if defined(__HIP_DEVICE_COMPILE__)
#define NOPK __attribute__((target("no-packed-fp32-ops")))
#else
#define NOPK
#endif
__global__ NOPK __launch_bounds__(512, 2) void fused_kernel(Args a) {
    __shared__ __attribute__((aligned(16))) char smem[163840];
    LAS char* sm = (LAS char*)smem;
    const int xcd = blockIdx.x & 7, j = ((blockIdx.x >> 3) + JROT) & 31;
    if (CPF) {
        u32 t = threadIdx.x * 128;
        asm volatile("s_cmp_lg_u32 %1, 29\n\t"
                     "s_cbranch_scc1 1f\n\t"
                     "s_getpc_b64 s[4:5]\n\t"
                     "s_and_b32 s4, s4, 0xffffff00\n\t"
                     "s_and_b32 s5, s5, 0xffff\n\t"
                     "s_mov_b32 s6, %2\n\t"
                     "s_mov_b32 s7, 0x20000\n\t"
                     "s_mov_b32 m0, 0\n\t"
                     "s_nop 0\n\t"
                     "buffer_load_dword %0, s[4:7], 0 offen lds\n"
                     "1:"
                     :: "v"(t), "s"(j), "i"(CODE_BYTES) : "s4", "s5", "s6", "s7", "scc", "memory");
    }
    const bool is_attn = (j & 1) == 0;
    if (EARLYW && __builtin_expect(!is_attn, 1)) warm_attn(a, sm, xcd, j >> 1);
    cbar();
    if (__builtin_expect(is_attn, ATTN_LIKELY)) attn_role(a, sm, xcd, j >> 1);
    else if (j < 28) ssm_role(a, sm, xcd * 14 + (j >> 1), xcd, j >> 1);
    else if (HELP) {
        const int lane = threadIdx.x & 63, wave8 = __builtin_amdgcn_readfirstlane(threadIdx.x >> 6), t = (j >> 1 & 1) * 512 + threadIdx.x;
        STAMP_DECL;
        STAMP(0);
        u32 k[6] = {};
        if (!EARLYW) warm_attn(a, sm, xcd, j >> 1);
        if (HELP & 2) {
            if (HDEL) { WAITV(0); __builtin_amdgcn_s_sleep(HDEL); }
#pragma unroll
            for (int i = 0; i < 2; ++i) k[1 + i] = touch(mkdesc(a.wqkv, 448 * 2688), ((t + 1024 * i) >> 2) * 2688 + ((xcd & 1) * 3 + (t & 3)) * 128);
#pragma unroll
            for (int i = 0; i < 3; ++i) k[3 + i] = touch(mkdesc(a.wo, 448 * 896), (t + 1024 * i) * 128);
        }
        WAITV(0);
#pragma unroll
        for (int i = 0; i < 6; ++i) KEEP(k[i]);
        STAMP(12);
        STAMP_FLUSH(128 + xcd * 2 + ((j >> 1) & 1));
    }
}

extern "C" void kernel_launch(void* const* d_in, const int* in_sizes, int n_in,
                              void* d_out, int out_size, void* d_ws, size_t ws_size,
                              hipStream_t stream) {
    Args a;
    a.u = (const u16*)d_in[0]; a.logA = (const u16*)d_in[1]; a.Aim = (const u16*)d_in[2];
    a.Cre = (const u16*)d_in[3]; a.Cim = (const u16*)d_in[4]; a.logdt = (const u16*)d_in[5];
    a.Dskip = (const u16*)d_in[6]; a.x = (const u16*)d_in[7]; a.wqkv = (const u16*)d_in[8];
    a.wo = (const u16*)d_in[9]; a.out = (u16*)d_out; a.ws = (char*)d_ws;
    fused_kernel<<<256, 512, 0, stream>>>(a);
}
```

```cpp
#include <hip/hip_runtime.h>
#include <stdint.h>

typedef unsigned short u16;
typedef unsigned int u32;
typedef unsigned long long u64;
typedef __bf16 bf16x8 __attribute__((ext_vector_type(8)));
typedef __bf16 bf16x2 __attribute__((ext_vector_type(2)));
typedef _Float16 h2 __attribute__((ext_vector_type(2)));
typedef _Float16 h8 __attribute__((ext_vector_type(8)));
typedef short s16x4 __attribute__((ext_vector_type(4)));
typedef float f32x2 __attribute__((ext_vector_type(2)));
typedef float f32x4 __attribute__((ext_vector_type(4)));
typedef float f32x16 __attribute__((ext_vector_type(16)));
typedef u32 u32x2 __attribute__((ext_vector_type(2)));
typedef u32 u32x4 __attribute__((ext_vector_type(4)));
typedef __amdgpu_buffer_rsrc_t rsrc_t;
#define LAS __attribute__((address_space(3)))
#define GAS __attribute__((address_space(1)))
#define DI __device__ __forceinline__
#define SB() __builtin_amdgcn_sched_barrier(0)
#define KEEP(x) asm volatile("" :: "v"(x))
#define PERM __builtin_amdgcn_perm

constexpr u32 WS_EPA = 0;
constexpr u32 WS_EPS = 256;
constexpr u32 WS_UC  = 65536;
constexpr u32 WS_YC  = WS_UC + 131072;
constexpr u32 WS_KVP = 524288;
constexpr u32 WS_OX  = WS_KVP + (1u << 20);
constexpr u32 WS_UX  = WS_OX + (2u << 20);
constexpr u32 WS_YX  = WS_UX + (2u << 20);
constexpr u32 WS_END = WS_YX + (2u << 20);
constexpr u32 OOB = 0x80000000u;
constexpr int SPIN_CAP = 1 << 16;

struct Args {
    const u16 *u, *logA, *Aim, *Cre, *Cim, *logdt, *Dskip, *x, *wqkv, *wo;
    u16* out;
    char* ws;
};

DI f32x4 mfma(u32x4 a, u32x4 b, f32x4 c) { return __builtin_amdgcn_mfma_f32_16x16x32_bf16(__builtin_bit_cast(bf16x8, a), __builtin_bit_cast(bf16x8, b), c, 0, 0, 0); }
DI f32x4 mfmah(u32x4 a, u32x4 b, f32x4 c) { return __builtin_amdgcn_mfma_f32_16x16x32_f16(__builtin_bit_cast(h8, a), __builtin_bit_cast(h8, b), c, 0, 0, 0); }
DI f32x16 mfma32(u32x4 a, u32x4 b, f32x16 c) { return __builtin_amdgcn_mfma_f32_32x32x16_bf16(__builtin_bit_cast(bf16x8, a), __builtin_bit_cast(bf16x8, b), c, 0, 0, 0); }
DI u32 pk(float lo, float hi) { f32x2 v = {lo, hi}; return __builtin_bit_cast(u32, __builtin_convertvector(v, bf16x2)); }
DI float bflo(u32 v) { return __uint_as_float(v << 16); }
DI float bfhi(u32 v) { return __uint_as_float(v & 0xffff0000u); }
DI f32x2 unpk(u32 v) { return f32x2{bflo(v), bfhi(v)}; }
DI f32x4 asf(u32x4 v) { return __builtin_bit_cast(f32x4, v); }
DI u32x4 asu(f32x4 v) { return __builtin_bit_cast(u32x4, v); }
DI f32x4 zero4() { return f32x4{0.f, 0.f, 0.f, 0.f}; }
DI f32x16 zero16() { f32x16 z; for (int i = 0; i < 16; ++i) z[i] = 0.f; return z; }
DI u32x4 pk8(const f32x16& a, int s) { return u32x4{pk(a[8 * s], a[8 * s + 1]), pk(a[8 * s + 2], a[8 * s + 3]), pk(a[8 * s + 4], a[8 * s + 5]), pk(a[8 * s + 6], a[8 * s + 7])}; }
DI u32x4 quad(const f32x16& a, int r) { return asu(f32x4{a[4 * r], a[4 * r + 1], a[4 * r + 2], a[4 * r + 3]}); }
template <class T> DI void pin(T& v) { asm volatile("" : "+v"(v) :: "memory"); }
template <int I> struct IC { static constexpr int v = I; };
template <int N, int I = 0, class F> DI void sfor(F f) { if constexpr (I < N) { f(IC<I>{}); sfor<N, I + 1>(f); } }
DI void cbar() { asm volatile("" ::: "memory"); }
DI void raw_barrier() { asm volatile("s_barrier" ::: "memory"); }
DI void wg_barrier() { asm volatile("s_waitcnt lgkmcnt(0)\n\ts_barrier" ::: "memory"); }
template <int N> DI void waitv() { asm volatile("s_waitcnt vmcnt(%0)" :: "i"(N) : "memory"); }
template <int N> DI void waitx(u32x4* x) { asm volatile("s_waitcnt vmcnt(%4)" : "+v"(x[0]), "+v"(x[1]), "+v"(x[2]), "+v"(x[3]) : "i"(N) : "memory"); }

DI u32x4 lds128(LAS char* p) { return *(LAS u32x4*)p; }
DI void sts128(LAS char* p, u32x4 v) { *(LAS u32x4*)p = v; }
DI void sts64(LAS char* p, u32 lo, u32 hi) { u32x2 v = {lo, hi}; *(LAS u32x2*)p = v; }
DI u32x4 trfrag(LAS char* p0, LAS char* p1) {
    u32x2 a = __builtin_bit_cast(u32x2, __builtin_amdgcn_ds_read_tr16_b64_v4i16((LAS s16x4*)p0));
    u32x2 b = __builtin_bit_cast(u32x2, __builtin_amdgcn_ds_read_tr16_b64_v4i16((LAS s16x4*)p1));
    return u32x4{a.x, a.y, b.x, b.y};
}

DI rsrc_t mkrsrc(const void* p, u32 bytes) { return __builtin_amdgcn_make_buffer_rsrc((void*)p, 0, bytes, 0x00020000); }
DI u32x4 mkdesc(const void* p, u32 bytes) { u64 v = (u64)p; return u32x4{(u32)v, (u32)(v >> 32) & 0xffffu, bytes, 0x00020000u}; }
DI u32x4 bld_dev(rsrc_t r, u32 voff, u32 soff) { return __builtin_amdgcn_raw_buffer_load_b128(r, voff, soff, 16); }
DI u32 bld32_dev(rsrc_t r, u32 voff, u32 soff) { return __builtin_amdgcn_raw_buffer_load_b32(r, voff, soff, 16); }
DI void bst_dev(u32x4 v, rsrc_t r, u32 voff) { __builtin_amdgcn_raw_buffer_store_b128(v, r, voff, 0, 16); }
DI void bst32_dev(u32 v, rsrc_t r, u32 voff) { __builtin_amdgcn_raw_buffer_store_b32(v, r, voff, 0, 16); }
DI void st_epoch(char* p, u32 v) { __hip_atomic_store((GAS u32*)p, v, __ATOMIC_RELAXED, __HIP_MEMORY_SCOPE_AGENT); }
DI void setm0(LAS char* dst) { asm volatile("s_mov_b32 m0, %0\n\ts_nop 0" :: "s"((u32)(uintptr_t)dst) : "memory"); }
template <int OFF>
DI void dma(u32x4 desc, u32 voff, u32 soff) { asm volatile("buffer_load_dwordx4 %0, %1, %2 offen offset:%3 lds" :: "v"(voff), "s"(desc), "s"(soff), "i"(OFF) : "memory"); }
DI void ldd(u32x4& v, u32x4 desc, u32 voff, u32 soff) { asm volatile("buffer_load_dwordx4 %0, %1, %2, %3 offen sc1" : "=v"(v) : "v"(voff), "s"(desc), "s"(soff) : "memory"); }
DI u32 ld_epoch(const char* p) { u32 v; asm volatile("global_load_dword %0, %1, %2 sc1" : "=v"(v) : "v"(0u), "s"(p) : "memory"); return v; }
DI u32 touch(u32x4 desc, u32 voff) { u32 v; asm volatile("buffer_load_dword %0, %1, %2, 0 offen" : "=v"(v) : "v"(voff), "s"(desc) : "memory"); return v; }
DI void touch_lds(u32x4 desc, u32 voff) { asm volatile("buffer_load_dword %0, %1, 0 offen lds" :: "v"(voff), "s"(desc) : "memory"); }
template <int BIT> DI void reld(u32x4& v, u32x4 desc, u32 voff, u32 soff, u32 mask) {
    asm volatile("s_bitcmp1_b32 %4, %5\n\ts_cbranch_scc0 1f\n\tbuffer_load_dwordx4 %0, %1, %2, %3 offen sc1\n1:"
                 : "+v"(v) : "v"(voff), "s"(desc), "s"(soff), "s"(mask), "i"(BIT) : "scc", "memory");
}
template <int BIT> DI void reld32(u32& v, u32x4 desc, u32 voff, u32 soff, u32 mask) {
    asm volatile("s_bitcmp1_b32 %4, %5\n\ts_cbranch_scc0 1f\n\tbuffer_load_dword %0, %1, %2, %3 offen sc1\n1:"
                 : "+v"(v) : "v"(voff), "s"(desc), "s"(soff), "s"(mask), "i"(BIT) : "scc", "memory");
}
template <int N, class OK, class LD, class PIN>
DI void settle(u32 bad, OK ok, LD ld, PIN pn) {
#pragma unroll 1
    for (int sp = 0; bad != 0 && sp < SPIN_CAP; ++sp) {
        __builtin_amdgcn_s_sleep(2);
        sfor<N>([&](auto i) { ld(i, bad); });
        waitv<0>();
        sfor<N>([&](auto i) { pn(i.v); });
        sfor<N>([&](auto i) { if ((bad >> i.v & 1) && __all(ok(i.v))) bad &= ~(1u << i.v); });
    }
}
template <int N, class OK> DI u32 badmask(OK ok) { u32 bad = 0; sfor<N>([&](auto i) { if (!__all(ok(i.v))) bad |= 1u << i.v; }); return bad; }

DI u32 sum4(u32x4 v) { return v.x + v.y + v.z + v.w; }
DI u32 rowsum(u32 s) {
    s += __builtin_amdgcn_update_dpp(0, s, 0x128, 0xf, 0xf, false);
    s += __builtin_amdgcn_update_dpp(0, s, 0x124, 0xf, 0xf, false);
    s += __builtin_amdgcn_update_dpp(0, s, 0x122, 0xf, 0xf, false);
    s += __builtin_amdgcn_update_dpp(0, s, 0x121, 0xf, 0xf, false);
    return s;
}
DI u32 sum32(u32 s) { s = rowsum(s); auto t = __builtin_amdgcn_permlane16_swap(s, s, false, false); return t[0] + t[1]; }
DI u32 halfsum(u32 s) { auto r = __builtin_amdgcn_permlane32_swap(s, s, false, false); return r[0] + r[1]; }
DI u32 wavemax(u32 s) {
    s = max(s, (u32)__builtin_amdgcn_update_dpp(0, s, 0x128, 0xf, 0xf, false));
    s = max(s, (u32)__builtin_amdgcn_update_dpp(0, s, 0x124, 0xf, 0xf, false));
    s = max(s, (u32)__builtin_amdgcn_update_dpp(0, s, 0x122, 0xf, 0xf, false));
    s = max(s, (u32)__builtin_amdgcn_update_dpp(0, s, 0x121, 0xf, 0xf, false));
    auto r = __builtin_amdgcn_permlane32_swap(s, s, false, false);
    s = max(r[0], r[1]);
    auto t = __builtin_amdgcn_permlane16_swap(s, s, false, false);
    return max(t[0], t[1]);
}

DI float phi(float t) {
    const float e = __builtin_amdgcn_exp2f(t * 1.44269504089f);
    return __int_as_float(max(__float_as_int(t), 0x358637bd)) + __builtin_amdgcn_fmed3f(e, 0.f, 1.f);
}

constexpr int A_RED = 0, A_WO = 32768, A_WQ = 98304, A_KVB = 155648;
constexpr int PRO = 8;
constexpr int LA = 6;
constexpr int QS0 = 40;
constexpr int GK0 = 14;
constexpr int G2A = 2;
constexpr int issued_before(int gs) {
    constexpr int B0 = 16 - LA;
    int n = PRO + gs;
    int cap = 16 + (gs < B0 ? 0 : 4 * ((gs - B0) / 8) + ((gs - B0) % 8 < 4 ? (gs - B0) % 8 : 4));
    if (gs > B0 + 20) cap = 29;
    n = n < cap ? n : cap;
    if (n < 29) return n;
    const int q = gs - QS0;
    return 29 + (q < 0 ? 0 : q > 7 ? 7 : q);
}
static_assert(QS0 >= 40 - LA, "Wq overwrites ring slot 3");
constexpr int slot_ch(int gs) { return gs < 40 ? gs >> 3 : 5 + ((gs >> 2) & 1); }
constexpr int slot_kk(int gs) { return gs < 40 ? (gs & 7) >> 1 : gs & 3; }
constexpr int slot_sg(int gs) { return gs < 40 ? gs & 1 : gs >= 48; }

DI void attn_role(const Args& a, LAS char* sm, const int xcd, const int j) {
    const int tid = threadIdx.x, lane = tid & 63, wave = __builtin_amdgcn_readfirstlane(tid >> 6);
    const int rw4 = wave & 3, hs = wave >> 2;
    const int hp = (xcd & 1) * 4 + (j & 3), rb = (xcd >> 1) * 4 + (j >> 2);
    const int h = 2 * hp + hs;
    const int c0 = (2 * rb + 2 * hp) % 7;
    const int r32 = lane & 31, hh = lane >> 5, blk = (lane >> 4) & 1, q = (lane >> 2) & 3, p = lane & 3;
    const u32x4 rx = mkdesc(a.x, 2048 * 896), rw = mkdesc(a.wqkv, 448 * 2688), rwo = mkdesc(a.wo, 448 * 896), dws = mkdesc(a.ws, WS_END);
    const rsrc_t rws = mkrsrc(a.ws, WS_END);
    u32 tag = ld_epoch(a.ws + WS_EPA), tch = 0;
    const int widx = (lane & 15) ^ ((lane >> 4) << 2);
    const u32 wv = (8 * wave + (lane >> 4)) * 2688 + (1 + (widx >> 3)) * 896 + hp * 112 + min(widx & 7, 6) * 16;
    const u32 xv0 = (rb * 128 + wave * 16 + (lane >> 3)) * 896 + (((lane & 7) ^ (lane >> 4)) * 16);
    const u32 xv1 = (rb * 128 + wave * 16 + 8 + (lane >> 3)) * 896 + (((lane & 7) ^ (4 + (lane >> 4))) * 16) - 1024;
    const u32 qv = (8 * wave + (lane >> 3)) * 2688 + hp * 112 + min((lane & 7) ^ (blk << 2), 6) * 16;
    auto kchunk = [&](int i) { int kc = i + c0; return kc >= 7 ? kc - 7 : kc; };
    auto ldn = [&](int n) {
        if (n >= 29) { setm0(sm + A_WQ + (n - 29) * 8192 + wave * 1024); dma<0>(rw, qv, kchunk(n - 29) * 64 * 2688); return; }
        if (n == 28) {
            tch = touch(dws, lane < 8 ? WS_KVP + (h * 16 + rb) * 2048 + rw4 * 512 + (lane & 3) * 128
                           : lane < 24 ? WS_OX + ((rb * 4 + rw4) * 16 + h) * 2048 + (lane - 8) * 128 : OOB);
            return;
        }
        const int i = n >> 2, m = n & 3;
        if (m == 0) { setm0(sm + (i & 3) * 32768 + 16384 + wave * 2048); dma<0>(rx, xv0, kchunk(i) * 128); }
        if (m == 1) dma<1024>(rx, xv1, kchunk(i) * 128);
        if (m == 2) { setm0(sm + (i & 3) * 32768 + wave * 2048); dma<0>(rw, wv, kchunk(i) * 64 * 2688); }
        if (m == 3) dma<1024>(rw, wv, kchunk(i) * 64 * 2688 + 4 * 2688 - 1024);
    };
#pragma unroll
    for (int n = 0; n < PRO; ++n) ldn(n);

    const int wb = 56 * hs + 32 * blk + 8 * p;
    LAS char *wsrc[2], *xsrc[4];
#pragma unroll
    for (int sg = 0; sg < 2; ++sg) wsrc[sg] = sm + (32 * hh + q) * 256 + (((2 * sg + (wb >> 6)) ^ q) << 6) + (wb & 63);
#pragma unroll
    for (int kk = 0; kk < 4; ++kk) xsrc[kk] = sm + 16384 + (32 * rw4 + r32) * 128 + (((4 * hh + kk) ^ ((r32 >> 1) & 7)) * 16);

    const bool p3 = j < 14;
    const int r128 = p3 ? xcd * 2 + j / 7 : 0, nb = j % 7;
    f32x16 acc[2] = {zero16(), zero16()};
    u32x4 xf[28], wf[56];
    auto rdw = [&](int gs) { LAS char* s = wsrc[slot_sg(gs)] + (slot_ch(gs) & 3) * 32768 + slot_kk(gs) * 2048; wf[gs] = trfrag(s, s + 1024); };
    auto rdx = [&](int i, int kk) { xf[4 * i + kk] = lds128(xsrc[kk] + (i & 3) * 32768); };
    waitv<PRO - 4>();
    wg_barrier();
#pragma unroll
    for (int sl = 0; sl < LA; ++sl) { if (sl < 4) rdx(0, sl); rdw(sl); }
    u32 pkd[8];
    u32 vm = r32 == 28 ? 0u : ~0u, vo = r32 == 28 ? 0x3f803f80u : 0u;
    pin(vm); pin(vo);
    sfor<56>([&](auto gs) {
        constexpr int issued = issued_before(gs.v);
        constexpr int fu = gs.v + LA, c = fu == 44 ? 6 : (fu <= 40 && (fu & 7) == 0) ? fu >> 3 : 0;
        static_assert(!c || issued >= 4 * c + 4, "chunk not issued yet");
        if constexpr (c != 0) { waitv<issued - (4 * c + 4)>(); raw_barrier(); }
        SB();
        acc[slot_sg(gs.v)] = mfma32(xf[4 * slot_ch(gs.v) + slot_kk(gs.v)], wf[gs.v], acc[slot_sg(gs.v)]);
        SB();
        if constexpr (fu < 40 && fu >= 8 && (fu & 1) == 0) rdx(fu >> 3, (fu & 7) >> 1);
        if constexpr (fu >= 40 && fu < 48) rdx(slot_ch(fu), fu & 3);
        if constexpr (fu < 56) rdw(fu);
        if constexpr (gs.v == 44) raw_barrier();
        if constexpr (gs.v >= 49) { pkd[gs.v - 49] = pk(phi(acc[0][2 * (gs.v - 49)]), phi(acc[0][2 * (gs.v - 49) + 1])); pin(pkd[gs.v - 49]); }
#pragma unroll
        for (int n = issued; n < issued_before(gs.v + 1); ++n) ldn(n);
        SB();
    });
    {
        pkd[7] = pk(phi(acc[0][14]), phi(acc[0][15]));
        pin(pkd[7]);
        f32x16 kv = mfma32(u32x4{pkd[0], pkd[1], pkd[2], pkd[3]}, (pk8(acc[1], 0) & vm) | vo, zero16());
        SB();
        kv = mfma32(u32x4{pkd[4], pkd[5], pkd[6], pkd[7]}, (pk8(acc[1], 1) & vm) | vo, kv);
#pragma unroll
        for (int rq = 0; rq < 4; ++rq) sts128(sm + A_RED + wave * 4096 + rq * 1024 + lane * 16, quad(kv, rq));
    }
    asm volatile("s_waitcnt vmcnt(8)" : "+v"(tag) :: "memory");
    tag += 1;
    wg_barrier();
    u32 hm = hh ? 0u : ~0u, cm = r32 == 31 ? 0u : ~0u;
    pin(hm); pin(cm);
    const bool red = (rw4 >> 1) == hs;
    if (red) {
        LAS char* rs = sm + A_RED + hs * 16384 + (rw4 & 1) * 2048 + lane * 16;
        f32x4 lo = (asf(lds128(rs)) + asf(lds128(rs + 4096))) + (asf(lds128(rs + 8192)) + asf(lds128(rs + 12288)));
        f32x4 hi = (asf(lds128(rs + 1024)) + asf(lds128(rs + 5120))) + (asf(lds128(rs + 9216)) + asf(lds128(rs + 13312)));
        u32x4 v = {pk(lo.x, lo.y), pk(lo.z, lo.w), pk(hi.x, hi.y), pk(hi.z, hi.w) & cm};
        v.w |= (tag - sum32(sum4(v))) & ~cm;
        bst_dev(v, rws, WS_KVP + (h * 16 + rb) * 2048 + (tid & 127) * 16);
    }
    cbar();
    if (red) waitv<1>(); else waitv<0>();
    KEEP(tch);
    raw_barrier();
    const u32 ov = (8 * wave + (lane >> 3)) * 896 + nb * 128 + (((lane & 7) ^ (blk << 2)) * 16);
    u32x4 gk[8];
    const u32 kbase = WS_KVP + (h * 16 + (rw4 >> 1) * 8 + (rb & 7)) * 2048, kvo = (tid & 127) * 16;
    f32x16 aq = zero16();
    {
        LAS char* qsrc = sm + A_WQ + (32 * hh + q) * 128 + (((wb >> 6) ^ (q >> 1)) << 6) + (wb & 63);
        u32x4 wq[28];
        auto rdq = [&](int k) { LAS char* s = qsrc + (k >> 2) * 8192 + (k & 3) * 1024; wq[k] = trfrag(s, s + 512); };
#pragma unroll
        for (int k = 0; k < 4; ++k) rdq(k);
        SB();
#pragma unroll
        for (int k = 0; k < 28; ++k) {
            aq = mfma32(wq[k], xf[k], aq);
            SB();
            if (k + 4 < 28) rdq(k + 4);
            if (k < 7) { setm0(sm + A_WO + k * 8192 + wave * 1024); dma<0>(rwo, ov, k * 64 * 896); }
            if (k >= GK0 && k < GK0 + 8) ldd(gk[k - GK0], dws, kvo, kbase ^ ((k - GK0) * 2048));
            SB();
        }
    }
    u32 pqd[8];
#pragma unroll
    for (int k = 0; k < 8; ++k) {
        pqd[k] = pk(phi(aq[2 * k]), phi(aq[2 * k + 1]));
        if (k >= 6) pqd[k] &= hm;
        pin(pqd[k]);
    }
    {
        waitx<0>(gk); waitx<0>(gk + 4);
        u32 t = 0;
#pragma unroll
        for (int i = 0; i < 8; ++i) t += sum4(gk[i]);
        if (!__all(sum32(t) == 8u * tag)) {
            auto ok = [&](int i) { return sum32(sum4(gk[i])) == tag; };
            settle<8>(badmask<8>(ok), ok, [&](auto i, u32 bm) { reld<i.v>(gk[i.v], dws, kvo, kbase ^ (i.v * 2048), bm); }, [&](int i) { pin(gk[i]); });
        }
    }
    {
        u32x4 v;
#pragma unroll
        for (int c = 0; c < 4; ++c) {
            const float lo = ((bflo(gk[0][c]) + bflo(gk[1][c])) + (bflo(gk[2][c]) + bflo(gk[3][c]))) + ((bflo(gk[4][c]) + bflo(gk[5][c])) + (bflo(gk[6][c]) + bflo(gk[7][c])));
            const float hi = ((bfhi(gk[0][c]) + bfhi(gk[1][c])) + (bfhi(gk[2][c]) + bfhi(gk[3][c]))) + ((bfhi(gk[4][c]) + bfhi(gk[5][c])) + (bfhi(gk[6][c]) + bfhi(gk[7][c])));
            v[c] = pk(lo, hi);
        }
        sts128(sm + A_KVB + tid * 16, v);
    }
    wg_barrier();
    {
        u32x4 kf[4];
#pragma unroll
        for (int i = 0; i < 4; ++i) kf[i] = lds128(sm + A_KVB + hs * 4096 + i * 1024 + lane * 16);
        const u32x4 pq0 = {pqd[0], pqd[1], pqd[2], pqd[3]}, pq1 = {pqd[4], pqd[5], pqd[6], pqd[7]};
        f32x16 oa = mfma32(kf[3], pq1, mfma32(kf[2], pq0, mfma32(kf[1], pq1, mfma32(kf[0], pq0, zero16()))));
        const u32 dn = __float_as_uint(oa[12]);
        auto r = __builtin_amdgcn_permlane32_swap(dn, dn, false, false);
        oa *= __builtin_amdgcn_rcpf(__uint_as_float(r[1]) + 1e-6f);
        const u32x4 f0 = pk8(oa, 0);
        const u32 o = WS_OX + ((rb * 4 + rw4) * 16 + h) * 2048 + lane * 16;
        bst_dev(f0, rws, o);
        SB();
        u32x4 f1 = pk8(oa, 1);
        f1.z &= hm; f1.w &= hm;
        f1.w |= (tag - halfsum(sum4(f0) + sum4(f1))) & ~hm;
        bst_dev(f1, rws, o + 1024);
    }
    cbar();
    if (p3) {
        LAS char* osrc[2];
#pragma unroll
        for (int nt = 0; nt < 2; ++nt) osrc[nt] = sm + A_WO + (224 * hs + 4 * hh + q) * 128 + ((nt ^ (q >> 1)) * 64) + blk * 32 + 8 * p;
        const int padoff = hh ? 0 : 1024;
        u32x4 wf3[8][2][2];
#pragma unroll
        for (int k = 0; k < 32; ++k) {
            const int hl = k >> 2, ks = (k >> 1) & 1, nt = k & 1;
            LAS char* s = osrc[nt] + (hl * 28 + 16 * ks) * 128;
            wf3[hl][ks][nt] = trfrag(s, s + (ks ? padoff : 1024));
        }
        SB();
        waitv<0>();
        u32x4 go[16];
        const u32 ob0 = WS_OX + ((r128 * 4 + rw4) * 16 + 8 * hs) * 2048;
        auto ldo = [&](int i) { ldd(go[i], dws, lane * 16, ob0 + i * 1024); };
#pragma unroll
        for (int i = 0; i < 4 * G2A; ++i) ldo(i);
        f32x16 ac[2];
        sfor<4>([&](auto gi) {
            constexpr int i = 4 * gi.v, nis = gi.v + G2A < 4 ? gi.v + G2A : 4;
            waitx<4 * (nis - gi.v - 1)>(go + i);
            {
                auto r0 = __builtin_amdgcn_permlane32_swap(sum4(go[i]) + sum4(go[i + 1]), sum4(go[i + 2]) + sum4(go[i + 3]), false, false);
                const u64 ne = __ballot(r0[0] + r0[1] != tag);
                if (ne)
                    settle<2>(((u32)ne != 0) | ((ne >> 32) != 0) << 1, [&](int k) { return halfsum(sum4(go[i + 2 * k]) + sum4(go[i + 2 * k + 1])) == tag; },
                              [&](auto k, u32 bm) { reld<k.v>(go[i + 2 * k.v], dws, lane * 16, ob0 + (i + 2 * k.v) * 1024, bm);
                                                    reld<k.v>(go[i + 2 * k.v + 1], dws, lane * 16, ob0 + (i + 2 * k.v + 1) * 1024, bm); },
                              [&](int k) { pin(go[i + 2 * k]); pin(go[i + 2 * k + 1]); });
            }
            go[i + 1].w &= hm; go[i + 3].w &= hm;
            SB();
#pragma unroll
            for (int k = 0; k < 8; ++k) {
                const int hl = 2 * gi.v + (k >> 2), ks = (k >> 1) & 1, nt = k & 1;
                ac[nt] = mfma32(wf3[hl][ks][nt], go[2 * hl + ks], (gi.v == 0 && k < 2) ? zero16() : ac[nt]);
                SB();
                if ((k & 1) == 0 && gi.v + G2A < 4) ldo(4 * (gi.v + G2A) + (k >> 1));
                SB();
            }
        });
        LAS char* xb = sm + wave * 4096 + lane * 16;
        if (hs) {
#pragma unroll
            for (int rq = 0; rq < 4; ++rq) sts128(xb + rq * 1024, quad(ac[0], rq));
            ac[0] = ac[1];
        } else {
#pragma unroll
            for (int rq = 0; rq < 4; ++rq) sts128(xb + rq * 1024, quad(ac[1], rq));
        }
        wg_barrier();
        LAS char* pb = sm + (wave ^ 4) * 4096 + lane * 16;
        LAS char* ob = sm + A_WQ + rw4 * 4608;
#pragma unroll
        for (int rq = 0; rq < 4; ++rq) {
            const f32x4 v = asf(quad(ac[0], rq)) + asf(lds128(pb + rq * 1024));
            sts64(ob + r32 * 144 + hs * 64 + rq * 16 + hh * 8, pk(v.x, v.y), pk(v.z, v.w));
        }
        wg_barrier();
        const rsrc_t ro = mkrsrc(a.out, 2048 * 1792);
        const int orow = 16 * hs + (lane >> 3);
        const u32 oo = (r128 * 128 + rw4 * 32 + orow) * 1792 + 896 + nb * 128 + (lane & 7) * 16;
        const u32x4 v0 = lds128(ob + orow * 144 + (lane & 7) * 16), v1 = lds128(ob + (8 + orow) * 144 + (lane & 7) * 16);
        bst_dev(v0, ro, oo);
        bst_dev(v1, ro, oo + 8 * 1792);
        if (xcd == 0 && j == 0 && tid == 0) st_epoch(a.ws + WS_EPA, tag);
    }
}

constexpr int S_TAB = 0;
constexpr int S_KC = 34816;
constexpr int S_P = 47104;
constexpr int S_UT = 52480;
constexpr int S_TR = 61696;

struct c2 { f32x2 r, i; };
struct hc { h2 r, i; };
DI c2 csq(c2 a) { return c2{a.r * a.r - a.i * a.i, 2.f * a.r * a.i}; }
DI hc hmul(hc a, hc b) { return hc{a.r * b.r - a.i * b.i, a.r * b.i + a.i * b.r}; }
DI hc toh(c2 a) { return hc{__builtin_convertvector(a.r, h2), __builtin_convertvector(a.i, h2)}; }
DI c2 cexp2(f32x2 lr, f32x2 rev, float pw) {
    f32x2 e = lr * pw, f = rev * pw;
    f32x2 m = {__builtin_amdgcn_exp2f(e.x), __builtin_amdgcn_exp2f(e.y)};
    f.x = __builtin_amdgcn_fractf(f.x); f.y = __builtin_amdgcn_fractf(f.y);
    f32x2 c = {__builtin_amdgcn_cosf(f.x), __builtin_amdgcn_cosf(f.y)}, s = {__builtin_amdgcn_sinf(f.x), __builtin_amdgcn_sinf(f.y)};
    return c2{m * c, m * s};
}
constexpr int tri_a(int sl) { int a = 0; while (sl >= 8 - a) { sl -= 8 - a; ++a; } return a; }
constexpr int tri_b(int sl) { int a = 0; while (sl >= 8 - a) { sl -= 8 - a; ++a; } return sl; }

DI void ssm_role(const Args& a, LAS char* sm0, const int sb) {
    const int tid = threadIdx.x & 255, lane = tid & 63, wave8 = __builtin_amdgcn_readfirstlane(threadIdx.x >> 6), wave = wave8 & 3;
    LAS char* sm = sm0 + (wave8 >> 2) * 81920;
    const int sidx = 2 * sb + (wave8 >> 2);
    const int ct = sidx >> 5, j = sidx & 31;
    const int ch = wave >> 1, hf = wave & 1, d = ct * 64 + 2 * j + ch;
    const int g = lane >> 4, r16 = lane & 15;
    const int trow = 256 * (j >> 2) + 16 * (tid >> 4) + 4 * (j & 3) + 2 * ((tid >> 3) & 1);
    const u32 po = (d * 128 + hf * 64 + lane) * 4, uo = trow * 896 + 128 * ct + (tid & 7) * 16;
    const u32 ldt = a.logdt[d];
    const u32 pA = *(const u32*)((const char*)a.logA + po), pI = *(const u32*)((const char*)a.Aim + po);
    const u32x4 ua = *(const u32x4*)((const char*)a.u + uo), ub = *(const u32x4*)((const char*)a.u + uo + 896);
    const u32 pCr = *(const u32*)((const char*)a.Cre + po), pCi = *(const u32*)((const char*)a.Cim + po);
    const u32 dsk = ((const u32*)a.Dskip)[ct * 32 + j];
    cbar();
    u32 tag = ld_epoch(a.ws + WS_EPS + ct * 256);
    const rsrc_t rws = mkrsrc(a.ws, WS_END);
    const u32x4 dws = mkdesc(a.ws, WS_END);
    *(LAS u32*)(sm + S_UT + (tid >> 7) * 4608 + (tid & 127) * 4) = 0;
    const u32 pofs = sidx * 8192 + tid * 16;
    const u32 cofs = (ct * 32 + (tid >> 4)) * 8192 + j * 256 + (tid & 15) * 16;
    const u32 pcs = r16 < 2 ? (sidx * 4 + wave) * 128 + g * 8 + (tid & 1) * 4 : OOB;
    const u32 ccs = ((ct * 32 + (tid >> 4)) * 4 + ((j >> 2) & 3)) * 128 + (j & 3) * 8 + (j >> 4) * 4;
    auto pubcs = [&](u32x4 v0, u32x4 v1, u32 cbase) {
        const u32 s0 = rowsum(sum4(v0)), s1 = rowsum(sum4(v1));
        bst32_dev(tag - (r16 ? s1 : s0), rws, cbase + pcs);
    };
    const int w = 16 + r16 - 8 * g;
    u32x4 bE, bO, mkb, mkd;
    u32 sel[3][4], msk[4], pb[3];
    {
        u32 y[4];
#pragma unroll
        for (int e = 0; e < 4; ++e) {
            y[e] = (u32)(w + 64 - 2 * e) * 0x10001u - 0x10000u;
            msk[e] = hf ? 0u : (((y[e] >> 6) & 0x10001u) ^ 0x10001u) * 0xffffu;
        }
        pb[0] = w - 7;
#pragma unroll
        for (int s = 1; s < 3; ++s) {
            u32 r[4];
#pragma unroll
            for (int e = (s == 1 ? 2 : 0); e < 4; ++e)
                r[e] = ((y[e] + ((1u << (s - 1)) - 1u) * 0x10001u + ((y[e] >> s) & 0x10001u)) >> s) & 0x00ff00ffu;
            const u32 base = r[3] >> 16;
            pb[s] = base - (64 >> s);
#pragma unroll
            for (int e = (s == 1 ? 2 : 0); e < 4; ++e) sel[s][e] = (((r[e] - base * 0x10001u) ^ 0x10001u) & 0xfffffu) * 0x202u + 0x01000100u;
        }
#pragma unroll
        for (int e = 0; e < 4; ++e) {
            const int z = min(max(16 - w + 2 * e, 0), 20);
            auto two = [&](u32 m) { const u32 b2 = (m >> z) & 3u; return (b2 | (b2 << 15)) & 0x10001u; };
            bE[e] = two(0x1ff0u) * 0x3f80u; bO[e] = two(0x0fe0u) * 0x3f80u; mkb[e] = two(0x1f000u) * 0xffffu;
            mkd[e] = r16 - 4 * g - e >= 0 ? ~0u : 0u;
        }
        asm volatile("" : "+v"(sel[1][2]), "+v"(sel[1][3]), "+v"(sel[2][0]), "+v"(sel[2][1]), "+v"(sel[2][2]), "+v"(sel[2][3]), "+v"(bE), "+v"(bO), "+v"(mkb), "+v"(mkd));
        asm volatile("" : "+v"(msk[0]), "+v"(msk[1]), "+v"(msk[2]), "+v"(msk[3]));
    }
    LAS u32* const R = (LAS u32*)(sm + S_P + ch * 2688) + 655;
    LAS u32 *P0 = R - (128 * hf + (int)pb[0]) - 102, *P1 = R - (256 + 64 * hf + (int)pb[1]) - 132, *P2 = R - (384 + 32 * hf + (int)pb[2]) - 130,
            *P3 = R - (385 + 2 * (r16 - 4 * g)) - 224;
    LAS char* ubase = sm + S_UT + ch * 4608 + 480 + r16 * 32 + g * 16 - 256 * hf - 192;
    asm volatile("" : "+v"(P0), "+v"(P1), "+v"(P2), "+v"(P3), "+v"(ubase));
    LAS char* trw = sm + S_TR + (tid & 7) * 288 + (tid >> 3) * 8;
    LAS char* trr = sm + S_TR + (((tid >> 4) & 3) * 8 + (tid >> 6)) * 288 + (tid & 15) * 16;
    LAS char* tab = sm + S_TAB + wave * 8704;
    LAS char* tw = tab + lane * 8;
    LAS char* tr = tab + r16 * 544 + g * 16;
    LAS char* kc = sm + S_KC + wave * 3072;
    SB();
#define EMIT1(row, v) sts64(tw + (row) * 544, __builtin_bit_cast(u32, (v).r), __builtin_bit_cast(u32, (v).i))
#define EMIT2(row, v0, v1) do { SB(); EMIT1(row, v0); EMIT1((row) + 1, v1); SB(); } while (0)
#define KRD() do { SB(); _Pragma("unroll") for (int ks = 0; ks < 8; ++ks) gf[ks] = lds128(tr + ks * 64); cbar(); SB(); } while (0)
#define KM2(k) do { ka = mfmah(gf[2 * (k)], ff[2 * (k)], (k) ? ka : zero4()); ka = mfmah(gf[2 * (k) + 1], ff[2 * (k) + 1], ka); } while (0)
    const float L2E = 1.44269504089f;
    const float edt = __builtin_amdgcn_exp2f(bflo(ldt) * L2E);
    const float dt = bflo(pk(edt, edt));
    f32x2 lr = unpk(pA) * L2E;
    lr = unpk(pk(__builtin_amdgcn_exp2f(lr.x), __builtin_amdgcn_exp2f(lr.y))) * dt * -L2E;
    const f32x2 rev = unpk(pI) * dt * 0.15915494309f;
    SB();
    sts64(trw, ua.x, ub.x); sts64(trw + 8 * 288, ua.y, ub.y); sts64(trw + 16 * 288, ua.z, ub.z); sts64(trw + 24 * 288, ua.w, ub.w);
    SB();
    hc W[8], F[16], G[17], H, t0, t1;
    u32x4 ff[8], gf[8];
    f32x4 ka;
    c2 e = cexp2(lr, -rev, 8.f);
    F[1] = toh(e); e = csq(e); F[2] = toh(e);
    asm volatile("s_waitcnt vmcnt(0)" : "+v"(tag) :: "memory");
    tag += 1;
    wg_barrier();
    const u32x4 up0 = lds128(trr), up1 = lds128(trr + 4 * 288);
    F[0] = hc{h2{1.f16, 1.f16}, h2{0.f16, 0.f16}};
    EMIT2(0, F[0], F[1]);
    e = csq(e); F[4] = toh(e); F[3] = hmul(F[1], F[2]);
    bst_dev(up0, rws, WS_UX + pofs);
    EMIT2(2, F[2], F[3]);
    e = csq(e); F[8] = toh(e); F[5] = hmul(F[4], F[1]);
    EMIT2(4, F[4], F[5]);
    F[6] = hmul(F[4], F[2]); F[7] = hmul(F[4], F[3]); F[9] = hmul(F[8], F[1]);
    bst_dev(up1, rws, WS_UX + pofs + 4096);
    EMIT2(6, F[6], F[7]);
    pubcs(up0, up1, WS_UC);
    cbar();
    EMIT2(8, F[8], F[9]);
    F[10] = hmul(F[8], F[2]); F[11] = hmul(F[8], F[3]);
    e = cexp2(lr, rev, 1.f); W[1] = toh(e);
    EMIT2(10, F[10], F[11]);
    F[12] = hmul(F[8], F[4]); F[13] = hmul(F[8], F[5]);
    e = csq(e); W[2] = toh(e);
    EMIT2(12, F[12], F[13]);
    F[14] = hmul(F[8], F[6]); F[15] = hmul(F[8], F[7]);
    e = csq(e); W[4] = toh(e);
    EMIT2(14, F[14], F[15]);
    u32 ce = max(max(pCr & 0x7fffu, (pCr >> 16) & 0x7fffu), max(pCi & 0x7fffu, (pCi >> 16) & 0x7fffu));
    ce = min(max(wavemax(ce) >> 7, 1u), 253u);
    const float cdn = __uint_as_float((254u - ce) << 23), cup = __uint_as_float(ce << 23) * (2.f * dt);
    G[0] = toh(c2{unpk(pCr) * cdn, unpk(pCi) * cdn});
    t1 = hmul(G[0], W[1]);
    SB();
#pragma unroll
    for (int ks = 0; ks < 8; ++ks) ff[ks] = lds128(tr + ks * 64);
    cbar();
    EMIT2(0, G[0], t1);
    W[3] = hmul(W[1], W[2]); t0 = hmul(G[0], W[2]); t1 = hmul(G[0], W[3]);
    EMIT2(2, t0, t1);
    W[5] = hmul(W[4], W[1]); t0 = hmul(G[0], W[4]); t1 = hmul(G[0], W[5]);
    EMIT2(4, t0, t1);
    W[6] = hmul(W[4], W[2]); W[7] = hmul(W[4], W[3]); t0 = hmul(G[0], W[6]); t1 = hmul(G[0], W[7]);
    EMIT2(6, t0, t1);
    e = cexp2(lr, rev, 128.f); H = toh(e);
    G[1] = hmul(G[0], H); t1 = hmul(G[1], W[1]);
    EMIT2(8, G[1], t1);
    t0 = hmul(G[1], W[2]); t1 = hmul(G[1], W[3]);
    e = csq(e); H = toh(e);
    EMIT2(10, t0, t1);
    t0 = hmul(G[1], W[4]); t1 = hmul(G[1], W[5]); G[2] = hmul(G[0], H);
    EMIT2(12, t0, t1);
    t0 = hmul(G[1], W[6]); t1 = hmul(G[1], W[7]); G[3] = hmul(G[1], H);
    EMIT2(14, t0, t1);
    t1 = hmul(G[2], W[2]);
    e = csq(e); H = toh(e);
    KRD();
    EMIT2(0, G[2], t1);
    t0 = hmul(G[2], W[4]); t1 = hmul(G[2], W[6]); G[4] = hmul(G[0], H);
    EMIT2(2, t0, t1);
    t1 = hmul(G[3], W[2]); G[5] = hmul(G[1], H); KM2(0);
    EMIT2(4, G[3], t1);
    t0 = hmul(G[3], W[4]); t1 = hmul(G[3], W[6]); KM2(1);
    EMIT2(6, t0, t1);
    t1 = hmul(G[4], W[4]); G[6] = hmul(G[2], H); KM2(2);
    EMIT2(8, G[4], t1);
    t1 = hmul(G[5], W[4]); G[7] = hmul(G[3], H); KM2(3);
    EMIT2(10, G[5], t1);
    u32x4 uu[2]; u32 uc[2];
#pragma unroll
    for (int i = 0; i < 2; ++i) { uu[i] = bld_dev(rws, cofs, WS_UX + i * 16 * 8192); uc[i] = bld32_dev(rws, ccs, WS_UC + i * 16 * 512); }
    cbar();
    t1 = hmul(G[6], W[4]);
    e = csq(e); H = toh(e);
    EMIT2(12, G[6], t1);
    t1 = hmul(G[7], W[4]); G[8] = hmul(G[0], H);
    sts128(kc + (128 * (g >> 1) + 4 * (g & 1) + 8 * r16) * 4, asu(ka * cup));
    EMIT2(14, G[7], t1);
    G[9] = hmul(G[1], H); G[10] = hmul(G[2], H);
    KRD();
    EMIT2(0, G[8], G[9]);
    G[11] = hmul(G[3], H); G[12] = hmul(G[4], H); G[13] = hmul(G[5], H);
    EMIT2(2, G[10], G[11]);
    G[14] = hmul(G[6], H); KM2(0); KM2(1);
    EMIT2(4, G[12], G[13]);
    G[15] = hmul(G[7], H); G[16] = hmul(G[8], H); KM2(2); KM2(3);
    EMIT2(6, G[14], G[15]);
    EMIT1(8, G[16]);
    KRD();
    {
        LAS char* k0 = kc + (g < 2 ? 256 + 64 * g + 4 * r16 : 384 + 64 * (g - 2) + 2 * r16) * 4;
        const u32x4 kb = asu(ka * cup);
        sts64(k0, kb.x, kb.y);
        sts64(k0 + (g < 2 ? 8 : 128), kb.z, kb.w);
    }
    SB();
    KM2(0); KM2(1); KM2(2); KM2(3);
    pin(ka);
    {
        auto ok = [&](int i) { return rowsum(sum4(uu[i])) + uc[i] == tag; };
        settle<2>(badmask<2>(ok), ok,
                  [&](auto i, u32 bm) { reld<i.v>(uu[i.v], dws, cofs, WS_UX + i.v * 16 * 8192, bm); reld32<i.v>(uc[i.v], dws, ccs, WS_UC + i.v * 16 * 512, bm); },
                  [&](int i) { pin(uu[i]); pin(uc[i]); });
    }
    {
        LAS float* k0 = (LAS float*)(kc + (512 + 64 * g + r16) * 4);
        k0[0] = ka.x * cup; k0[16] = ka.y * cup; k0[32] = ka.z * cup; k0[48] = ka.w * cup;
    }
#pragma unroll
    for (int i = 0; i < 2; ++i) {
        LAS char* ud = sm + S_UT + 512 + (4 * i + wave) * 512 + r16 * 32 + g * 8;
        sts64(ud, PERM(uu[i].y, uu[i].x, 0x05040100u), PERM(uu[i].w, uu[i].z, 0x05040100u));
        sts64(ud + 4608, PERM(uu[i].y, uu[i].x, 0x07060302u), PERM(uu[i].w, uu[i].z, 0x07060302u));
    }
    wg_barrier();
    u32 tch = touch(dws, lane < 16 ? WS_YX + sidx * 8192 + (lane & 8) * 512 + wave * 1024 + (lane & 7) * 128
                       : lane == 16 ? WS_YC + (sidx * 4 + wave) * 128 : OOB);
    if (j == 0 && tid == 0) st_epoch(a.ws + WS_EPS + ct * 256, tag);
    u32x4 U[2][8];
    auto rdU = [&](int it, int bq) { U[it & 1][bq] = lds128(ubase + 512 * bq + 192 - 64 * it); };
    {
        LAS char* k0 = sm + S_KC + ch * 6144;
        LAS char* Rb = sm + S_P + ch * 2688;
        const int t = tid & 127, e1 = min(t + 128, 167);
        f32x4 a0 = asf(lds128(k0 + t * 16)), b0 = asf(lds128(k0 + 3072 + t * 16)), a1 = asf(lds128(k0 + e1 * 16)), b1 = asf(lds128(k0 + 3072 + e1 * 16));
        const float n0 = *(LAS float*)(k0 + t * 16 + 16) + *(LAS float*)(k0 + 3072 + t * 16 + 16);
        const float n1 = *(LAS float*)(k0 + e1 * 16 + 16) + *(LAS float*)(k0 + 3072 + e1 * 16 + 16);
#pragma unroll
        for (int bq = 0; bq < 8; ++bq) rdU(3, bq);
        a0 += b0; a1 += b1;
        float p0 = a0.x, p1 = a0.y, p2 = a0.z, p3 = a0.w, q0 = a1.x, q1 = a1.y, q2 = a1.z, q3 = a1.w;
        asm volatile("" : "+v"(p0), "+v"(p1), "+v"(p2), "+v"(p3), "+v"(q0), "+v"(q1), "+v"(q2), "+v"(q3));
        sts128(Rb + (163 - t) * 16, u32x4{pk(n0, p3), pk(p3, p2), pk(p2, p1), pk(p1, p0)});
        u32x4 v1 = {pk(n1, q3), pk(q3, q2), pk(q2, q1), pk(q1, q0)};
        if (t >= 36) v1 = u32x4{0, 0, 0, 0};
        if (t < 39) sts128(Rb + (t < 36 ? 35 - t : 129 + t) * 16, v1);
        if (t == 0) sts128(Rb + 164 * 16, u32x4{pk(p0, 0.f), 0, 0, 0});
    }
    wg_barrier();
    f32x4 y[8];
    {
#define Q0(i) P0[102 - (i)]
#define Q1(i) P1[132 - (i)]
#define Q2(i) P2[130 - (i)]
#define Q3(i) P3[224 - (i)]
        u32x4 T[2][4], T32, TB[4], UW[4], WA[2];
        f32x4 wd[4];
        u32 raw[7], rx[10];
        auto rdT = [&](int it, int k) {
            u32x4& f0 = T[it & 1][0];
            if (k == 0) { f0.x = Q0(32 * it + 6); f0.y = Q0(32 * it + 4); }
            if (k == 1) { f0.z = Q0(32 * it + 2); f0.w = Q0(32 * it); }
            if (k == 2) { raw[0] = Q1(16 * it); raw[1] = Q1(16 * it + 2); raw[2] = Q1(16 * it + 4); }
            if (k == 3) { raw[3] = Q2(8 * it); raw[4] = Q2(8 * it + 2); }
            if (k == 4) { raw[5] = Q2(8 * it + 64); raw[6] = Q2(8 * it + 66); }
        };
        auto mkT = [&](int it, int aa, int e) {
            T[it & 1][aa][e] = aa == 1 ? (e < 2 ? PERM(raw[2], raw[1], sel[1][2 + e]) : PERM(raw[1], raw[0], sel[1][e])) : PERM(raw[2 * aa], raw[2 * aa - 1], sel[2][e]);
        };
        auto rdX = [&](int k) {
            if (k == 0) { rx[0] = Q0(262); rx[1] = Q0(260); }
            if (k == 1) { rx[2] = Q0(258); rx[3] = Q0(256); }
            if (k == 2) { rx[4] = Q1(128); rx[5] = Q1(130); rx[6] = Q1(132); }
            if (k == 3) { rx[7] = Q2(128); rx[8] = Q2(130); rx[9] = R[-512]; }
        };
        auto fixT = [&](int k, int e) {
            if (k < 2) {
                const u32 lo = k == 0 ? rx[e] : (e < 2 ? PERM(rx[6], rx[5], sel[1][2 + e]) : PERM(rx[5], rx[4], sel[1][e]));
                T[0][1 + k][e] = (lo & msk[e]) | (T[0][1 + k][e] & ~msk[e]);
            } else T32[e] = (PERM(rx[8], rx[7], sel[2][e]) & msk[e]) | (PERM(rx[9], rx[9], 0x03020302u) & mkb[e]);
        };
        LAS char* uw = ubase + 192 + 256 * hf + 1024 * (1 - hf);
        auto rdC = [&](int k) {
            if (k < 4) UW[k] = lds128(uw + 512 * (k >> 1) - 16 * (k & 1));
            else { const int dl = (k - 4) >> 1, e = 2 * (k & 1); TB[dl][e] = Q3(128 + 32 * dl - 2 * e); TB[dl][e + 1] = Q3(128 + 32 * dl - 2 * e - 2); }
        };
#pragma unroll
        for (int k = 0; k < 5; ++k) rdT(3, k);
        SB();
        sfor<104>([&](auto gs) {
            constexpr int it = 3 - gs.v / 26, sl = gs.v % 26, aa = tri_a(sl), bq = tri_b(sl);
            y[aa + bq] = mfma(T[it & 1][aa], U[it & 1][bq], (it == 3 && aa == 0) ? zero4() : y[aa + bq]);
            SB();
            if constexpr (it == 3) {
                if constexpr (sl < 6) { mkT(3, 1 + (sl >> 1), 2 * (sl & 1)); mkT(3, 1 + (sl >> 1), 2 * (sl & 1) + 1); }
                else if constexpr (sl < 14) { if (sl < 11) rdT(2, sl - 6); rdU(2, sl - 6); }
                else mkT(2, 1 + ((sl - 14) >> 2), (sl - 14) & 3);
            } else if constexpr (it > 0) {
                if constexpr (sl < 8) { if (sl < 5) rdT(it - 1, sl); rdU(it - 1, sl); }
                if constexpr (it == 1 && sl >= 5 && sl < 9) rdX(sl - 5);
                if constexpr (it == 2 && sl >= 10 && sl < 22) mkT(1, 1 + ((sl - 10) >> 2), (sl - 10) & 3);
                if constexpr (it == 1 && sl >= 10 && sl < 16) { mkT(0, 1 + ((sl - 10) >> 1), 2 * (sl & 1)); mkT(0, 1 + ((sl - 10) >> 1), 2 * (sl & 1) + 1); }
                if constexpr (it == 1 && sl >= 16 && sl < 24) fixT((sl - 16) >> 2, (sl - 16) & 3);
            } else {
                if constexpr (sl < 12) rdC(sl);
                if constexpr (sl < 4) fixT(2, sl);
                if constexpr (sl >= 10 && sl < 14) wd[sl - 10] = mfma(UW[sl - 10], (sl & 1) ? bE : bO, zero4());
                if constexpr (sl >= 16 && sl < 24) WA[(sl - 16) >> 2][sl & 3] = pk(wd[2 * ((sl - 16) >> 2) + 1][sl & 3], wd[2 * ((sl - 16) >> 2)][sl & 3]);
                if constexpr (sl == 24) TB[0] &= mkd;
            }
            SB();
        });
        if (hf) {
#pragma unroll
            for (int k = 0; k < 4; ++k) y[4 + k] = mfma(WA[0], TB[k], y[4 + k]);
#pragma unroll
            for (int k = 0; k < 3; ++k) y[5 + k] = mfma(WA[1], TB[k], y[5 + k]);
        } else {
#pragma unroll
            for (int k = 0; k < 4; ++k) y[4 + k] = mfma(T32, U[0][k], y[4 + k]);
            y[6] = mfma(WA[0], TB[0], y[6]); y[7] = mfma(WA[0], TB[1], y[7]); y[7] = mfma(WA[1], TB[0], y[7]);
        }
    }
    {
        LAS char* yd = sm + S_TAB + wave * 8192 + lane * 16;
#pragma unroll
        for (int i = 0; i < 8; ++i) sts128(yd + i * 1024, asu(y[i]));
    }
    wg_barrier();
    {
        const float d0 = bflo(dsk), d1 = bfhi(dsk);
        u32x4 o[2];
#pragma unroll
        for (int i = 0; i < 2; ++i) {
            LAS char* ys = sm + S_TAB + (wave + 4 * i) * 1024 + lane * 16;
            const f32x4 y0 = asf(lds128(ys)) + asf(lds128(ys + 8192));
            const f32x4 y1 = asf(lds128(ys + 16384)) + asf(lds128(ys + 24576));
            o[i] = u32x4{pk(y0.x + bflo(uu[i].x) * d0, y1.x + bfhi(uu[i].x) * d1), pk(y0.y + bflo(uu[i].y) * d0, y1.y + bfhi(uu[i].y) * d1),
                         pk(y0.z + bflo(uu[i].z) * d0, y1.z + bfhi(uu[i].z) * d1), pk(y0.w + bflo(uu[i].w) * d0, y1.w + bfhi(uu[i].w) * d1)};
        }
        bst_dev(o[0], rws, WS_YX + pofs);
        bst_dev(o[1], rws, WS_YX + pofs + 4096);
        pubcs(o[0], o[1], WS_YC);
    }
    waitv<0>();
    KEEP(tch);
    {
        u32x4 v[2]; u32 vc[2];
#pragma unroll
        for (int i = 0; i < 2; ++i) { v[i] = bld_dev(rws, cofs, WS_YX + i * 16 * 8192); vc[i] = bld32_dev(rws, ccs, WS_YC + i * 16 * 512); }
        cbar();
        auto ok = [&](int i) { return rowsum(sum4(v[i])) + vc[i] == tag; };
        settle<2>(badmask<2>(ok), ok,
                  [&](auto i, u32 bm) { reld<i.v>(v[i.v], dws, cofs, WS_YX + i.v * 16 * 8192, bm); reld32<i.v>(vc[i.v], dws, ccs, WS_YC + i.v * 16 * 512, bm); },
                  [&](int i) { pin(v[i]); pin(vc[i]); });
        sts128(trr, v[0]); sts128(trr + 4 * 288, v[1]);
        wg_barrier();
        const u32x2 s0 = *(LAS u32x2*)trw, s1 = *(LAS u32x2*)(trw + 8 * 288), s2 = *(LAS u32x2*)(trw + 16 * 288), s3 = *(LAS u32x2*)(trw + 24 * 288);
        const rsrc_t ro = mkrsrc(a.out, 2048 * 1792);
        const u32 oo = trow * 1792 + 128 * ct + (tid & 7) * 16;
        bst_dev(u32x4{s0.x, s1.x, s2.x, s3.x}, ro, oo);
        bst_dev(u32x4{s0.y, s1.y, s2.y, s3.y}, ro, oo + 1792);
    }
}

DI void warm_attn(const Args& a, LAS char* sm, int xcd, int hi) {
    const int lane = threadIdx.x & 63, wave8 = __builtin_amdgcn_readfirstlane(threadIdx.x >> 6);
    const int u = hi * 5 + wave8, idx = (u - 24) * 64 + lane;
    if (wave8 >= 5) return;
    setm0(sm + 163840 - 256);
    if (u < 24) {
        const int rbl = (xcd >> 1) * 4 + u / 6, kc = (2 * (rbl + (xcd & 1) * 4) + 1 + 2 * ((u % 6) >> 1)) % 7;
        touch_lds(mkdesc(a.x, 2048 * 896), (rbl * 128 + (u & 1) * 64 + lane) * 896 + kc * 128);
    } else touch_lds(mkdesc(a.wqkv, 448 * 2688), (idx >> 3) * 2688 + (1 + ((idx >> 2) & 1)) * 896 + ((xcd & 1) * 3 + (idx & 3)) * 128);
}

#if defined(__HIP_DEVICE_COMPILE__)
#define NOPK __attribute__((target("no-packed-fp32-ops")))
#else
#define NOPK
#endif
__global__ NOPK __launch_bounds__(512, 2) void fused_kernel(Args a) {
    __shared__ __attribute__((aligned(16))) char smem[163840];
    LAS char* sm = (LAS char*)smem;
    const int xcd = blockIdx.x & 7, j = blockIdx.x >> 3;
    const bool is_attn = (j & 1) == 0;
    if (__builtin_expect(!is_attn, 1)) warm_attn(a, sm, xcd, j >> 1);
    cbar();
    if (__builtin_expect(is_attn, 1)) attn_role(a, sm, xcd, j >> 1);
    else if (j < 28) ssm_role(a, sm, xcd * 14 + (j >> 1));
    else {
        const int t = (j >> 1 & 1) * 512 + threadIdx.x;
        setm0(sm);
#pragma unroll
        for (int i = 0; i < 2; ++i) touch_lds(mkdesc(a.wqkv, 448 * 2688), ((t + 1024 * i) >> 2) * 2688 + ((xcd & 1) * 3 + (t & 3)) * 128);
#pragma unroll
        for (int i = 0; i < 4; ++i) touch_lds(mkdesc(a.wo, 448 * 896), (t + 1024 * i) * 128);
        waitv<0>();
    }
}

extern "C" void kernel_launch(void* const* d_in, const int* in_sizes, int n_in,
                              void* d_out, int out_size, void* d_ws, size_t ws_size,
                              hipStream_t stream) {
    Args a;
    a.u = (const u16*)d_in[0]; a.logA = (const u16*)d_in[1]; a.Aim = (const u16*)d_in[2];
    a.Cre = (const u16*)d_in[3]; a.Cim = (const u16*)d_in[4]; a.logdt = (const u16*)d_in[5];
    a.Dskip = (const u16*)d_in[6]; a.x = (const u16*)d_in[7]; a.wqkv = (const u16*)d_in[8];
    a.wo = (const u16*)d_in[9]; a.out = (u16*)d_out; a.ws = (char*)d_ws;
    fused_kernel<<<256, 512, 0, stream>>>(a);
}
```

```cpp
#include <hip/hip_runtime.h>
#include <stdint.h>

typedef unsigned short u16;
typedef unsigned int u32;
typedef unsigned long long u64;
typedef __bf16 bf16x8 __attribute__((ext_vector_type(8)));
typedef __bf16 bf16x2 __attribute__((ext_vector_type(2)));
typedef _Float16 h2 __attribute__((ext_vector_type(2)));
typedef _Float16 h8 __attribute__((ext_vector_type(8)));
typedef short s16x4 __attribute__((ext_vector_type(4)));
typedef float f32x2 __attribute__((ext_vector_type(2)));
typedef float f32x4 __attribute__((ext_vector_type(4)));
typedef float f32x16 __attribute__((ext_vector_type(16)));
typedef u32 u32x2 __attribute__((ext_vector_type(2)));
typedef u32 u32x4 __attribute__((ext_vector_type(4)));
typedef __amdgpu_buffer_rsrc_t rsrc_t;
#define LAS __attribute__((address_space(3)))
#define GAS __attribute__((address_space(1)))
#define DI __device__ __forceinline__
#define SB() __builtin_amdgcn_sched_barrier(0)
#define KEEP(x) asm volatile("" :: "v"(x))
#define PERM __builtin_amdgcn_perm

constexpr u32 WS_EPA = 0;
constexpr u32 WS_EPS = 256;
constexpr u32 WS_UC  = 65536;
constexpr u32 WS_YC  = WS_UC + 131072;
constexpr u32 WS_KVP = 524288;
constexpr u32 WS_OX  = WS_KVP + (1u << 20);
constexpr u32 WS_UX  = WS_OX + (2u << 20);
constexpr u32 WS_YX  = WS_UX + (2u << 20);
constexpr u32 WS_END = WS_YX + (2u << 20);
constexpr u32 OOB = 0x80000000u;
constexpr int SPIN_CAP = 1 << 16;

struct Args {
    const u16 *u, *logA, *Aim, *Cre, *Cim, *logdt, *Dskip, *x, *wqkv, *wo;
    u16* out;
    char* ws;
};

DI f32x4 mfma(u32x4 a, u32x4 b, f32x4 c) { return __builtin_amdgcn_mfma_f32_16x16x32_bf16(__builtin_bit_cast(bf16x8, a), __builtin_bit_cast(bf16x8, b), c, 0, 0, 0); }
DI f32x4 mfmah(u32x4 a, u32x4 b, f32x4 c) { return __builtin_amdgcn_mfma_f32_16x16x32_f16(__builtin_bit_cast(h8, a), __builtin_bit_cast(h8, b), c, 0, 0, 0); }
DI f32x16 mfma32(u32x4 a, u32x4 b, f32x16 c) { return __builtin_amdgcn_mfma_f32_32x32x16_bf16(__builtin_bit_cast(bf16x8, a), __builtin_bit_cast(bf16x8, b), c, 0, 0, 0); }
DI u32 pk(float lo, float hi) { f32x2 v = {lo, hi}; return __builtin_bit_cast(u32, __builtin_convertvector(v, bf16x2)); }
DI float bflo(u32 v) { return __uint_as_float(v << 16); }
DI float bfhi(u32 v) { return __uint_as_float(v & 0xffff0000u); }
DI f32x2 unpk(u32 v) { return f32x2{bflo(v), bfhi(v)}; }
DI f32x4 asf(u32x4 v) { return __builtin_bit_cast(f32x4, v); }
DI u32x4 asu(f32x4 v) { return __builtin_bit_cast(u32x4, v); }
DI f32x4 zero4() { return f32x4{0.f, 0.f, 0.f, 0.f}; }
DI f32x16 zero16() { f32x16 z; for (int i = 0; i < 16; ++i) z[i] = 0.f; return z; }
DI u32x4 pk8(const f32x16& a, int s) { return u32x4{pk(a[8 * s], a[8 * s + 1]), pk(a[8 * s + 2], a[8 * s + 3]), pk(a[8 * s + 4], a[8 * s + 5]), pk(a[8 * s + 6], a[8 * s + 7])}; }
DI u32x4 quad(const f32x16& a, int r) { return asu(f32x4{a[4 * r], a[4 * r + 1], a[4 * r + 2], a[4 * r + 3]}); }
template <class T> DI void pin(T& v) { asm volatile("" : "+v"(v) :: "memory"); }
template <int I> struct IC { static constexpr int v = I; };
template <int N, int I = 0, class F> DI void sfor(F f) { if constexpr (I < N) { f(IC<I>{}); sfor<N, I + 1>(f); } }
DI void cbar() { asm volatile("" ::: "memory"); }
DI void raw_barrier() { asm volatile("s_barrier" ::: "memory"); }
DI void wg_barrier() { asm volatile("s_waitcnt lgkmcnt(0)\n\ts_barrier" ::: "memory"); }
template <int N> DI void waitv() { asm volatile("s_waitcnt vmcnt(%0)" :: "i"(N) : "memory"); }
template <int N> DI void waitx(u32x4* x) { asm volatile("s_waitcnt vmcnt(%4)" : "+v"(x[0]), "+v"(x[1]), "+v"(x[2]), "+v"(x[3]) : "i"(N) : "memory"); }

DI u32x4 lds128(LAS char* p) { return *(LAS u32x4*)p; }
DI void sts128(LAS char* p, u32x4 v) { *(LAS u32x4*)p = v; }
DI void sts64(LAS char* p, u32 lo, u32 hi) { u32x2 v = {lo, hi}; *(LAS u32x2*)p = v; }
DI u32x4 trfrag(LAS char* p0, LAS char* p1) {
    u32x2 a = __builtin_bit_cast(u32x2, __builtin_amdgcn_ds_read_tr16_b64_v4i16((LAS s16x4*)p0));
    u32x2 b = __builtin_bit_cast(u32x2, __builtin_amdgcn_ds_read_tr16_b64_v4i16((LAS s16x4*)p1));
    return u32x4{a.x, a.y, b.x, b.y};
}

DI rsrc_t mkrsrc(const void* p, u32 bytes) { return __builtin_amdgcn_make_buffer_rsrc((void*)p, 0, bytes, 0x00020000); }
DI u32x4 mkdesc(const void* p, u32 bytes) { u64 v = (u64)p; return u32x4{(u32)v, (u32)(v >> 32) & 0xffffu, bytes, 0x00020000u}; }
DI u32x4 bld_dev(rsrc_t r, u32 voff, u32 soff) { return __builtin_amdgcn_raw_buffer_load_b128(r, voff, soff, 16); }
DI u32 bld32_dev(rsrc_t r, u32 voff, u32 soff) { return __builtin_amdgcn_raw_buffer_load_b32(r, voff, soff, 16); }
DI void bst_dev(u32x4 v, rsrc_t r, u32 voff) { __builtin_amdgcn_raw_buffer_store_b128(v, r, voff, 0, 16); }
DI void bst32_dev(u32 v, rsrc_t r, u32 voff) { __builtin_amdgcn_raw_buffer_store_b32(v, r, voff, 0, 16); }
DI void st_epoch(char* p, u32 v) { __hip_atomic_store((GAS u32*)p, v, __ATOMIC_RELAXED, __HIP_MEMORY_SCOPE_AGENT); }
DI void setm0(LAS char* dst) { asm volatile("s_mov_b32 m0, %0\n\ts_nop 0" :: "s"((u32)(uintptr_t)dst) : "memory"); }
template <int OFF>
DI void dma(u32x4 desc, u32 voff, u32 soff) { asm volatile("buffer_load_dwordx4 %0, %1, %2 offen offset:%3 lds" :: "v"(voff), "s"(desc), "s"(soff), "i"(OFF) : "memory"); }
DI void ldd(u32x4& v, u32x4 desc, u32 voff, u32 soff) { asm volatile("buffer_load_dwordx4 %0, %1, %2, %3 offen sc1" : "=v"(v) : "v"(voff), "s"(desc), "s"(soff) : "memory"); }
DI u32 ld_epoch(const char* p) { u32 v; asm volatile("global_load_dword %0, %1, %2 sc1" : "=v"(v) : "v"(0u), "s"(p) : "memory"); return v; }
DI u32 touch(u32x4 desc, u32 voff) { u32 v; asm volatile("buffer_load_dword %0, %1, %2, 0 offen" : "=v"(v) : "v"(voff), "s"(desc) : "memory"); return v; }
DI void touch_lds(u32x4 desc, u32 voff) { asm volatile("buffer_load_dword %0, %1, 0 offen lds" :: "v"(voff), "s"(desc) : "memory"); }
template <int BIT> DI void reld(u32x4& v, u32x4 desc, u32 voff, u32 soff, u32 mask) {
    asm volatile("s_bitcmp1_b32 %4, %5\n\ts_cbranch_scc0 1f\n\tbuffer_load_dwordx4 %0, %1, %2, %3 offen sc1\n1:"
                 : "+v"(v) : "v"(voff), "s"(desc), "s"(soff), "s"(mask), "i"(BIT) : "scc", "memory");
}
template <int BIT> DI void reld32(u32& v, u32x4 desc, u32 voff, u32 soff, u32 mask) {
    asm volatile("s_bitcmp1_b32 %4, %5\n\ts_cbranch_scc0 1f\n\tbuffer_load_dword %0, %1, %2, %3 offen sc1\n1:"
                 : "+v"(v) : "v"(voff), "s"(desc), "s"(soff), "s"(mask), "i"(BIT) : "scc", "memory");
}
template <int N, class OK, class LD, class PIN>
DI void settle(u32 bad, OK ok, LD ld, PIN pn) {
#pragma unroll 1
    for (int sp = 0; bad != 0 && sp < SPIN_CAP; ++sp) {
        __builtin_amdgcn_s_sleep(2);
        sfor<N>([&](auto i) { ld(i, bad); });
        waitv<0>();
        sfor<N>([&](auto i) { pn(i.v); });
        sfor<N>([&](auto i) { if ((bad >> i.v & 1) && __all(ok(i.v))) bad &= ~(1u << i.v); });
    }
}
template <int N, class OK> DI u32 badmask(OK ok) { u32 bad = 0; sfor<N>([&](auto i) { if (!__all(ok(i.v))) bad |= 1u << i.v; }); return bad; }

DI u32 sum4(u32x4 v) { return v.x + v.y + v.z + v.w; }
DI u32 rowsum(u32 s) {
    s += __builtin_amdgcn_update_dpp(0, s, 0x128, 0xf, 0xf, false);
    s += __builtin_amdgcn_update_dpp(0, s, 0x124, 0xf, 0xf, false);
    s += __builtin_amdgcn_update_dpp(0, s, 0x122, 0xf, 0xf, false);
    s += __builtin_amdgcn_update_dpp(0, s, 0x121, 0xf, 0xf, false);
    return s;
}
DI u32 sum32(u32 s) { s = rowsum(s); auto t = __builtin_amdgcn_permlane16_swap(s, s, false, false); return t[0] + t[1]; }
DI u32 halfsum(u32 s) { auto r = __builtin_amdgcn_permlane32_swap(s, s, false, false); return r[0] + r[1]; }
DI u32 wavemax(u32 s) {
    s = max(s, (u32)__builtin_amdgcn_update_dpp(0, s, 0x128, 0xf, 0xf, false));
    s = max(s, (u32)__builtin_amdgcn_update_dpp(0, s, 0x124, 0xf, 0xf, false));
    s = max(s, (u32)__builtin_amdgcn_update_dpp(0, s, 0x122, 0xf, 0xf, false));
    s = max(s, (u32)__builtin_amdgcn_update_dpp(0, s, 0x121, 0xf, 0xf, false));
    auto r = __builtin_amdgcn_permlane32_swap(s, s, false, false);
    s = max(r[0], r[1]);
    auto t = __builtin_amdgcn_permlane16_swap(s, s, false, false);
    return max(t[0], t[1]);
}

DI float phi(float t) {
    const float e = __builtin_amdgcn_exp2f(t * 1.44269504089f);
    return __int_as_float(max(__float_as_int(t), 0x358637bd)) + __builtin_amdgcn_fmed3f(e, 0.f, 1.f);
}

constexpr int A_RED = 0, A_WO = 32768, A_WQ = 98304, A_KVB = 155648;
constexpr int PRO = 8;
constexpr int LA = 6;
constexpr int QS0 = 40;
constexpr int GK0 = 14;
constexpr int G2A = 2;
constexpr int issued_before(int gs) {
    constexpr int B0 = 16 - LA;
    int n = PRO + gs;
    int cap = 16 + (gs < B0 ? 0 : 4 * ((gs - B0) / 8) + ((gs - B0) % 8 < 4 ? (gs - B0) % 8 : 4));
    if (gs > B0 + 20) cap = 29;
    n = n < cap ? n : cap;
    if (n < 29) return n;
    const int q = gs - QS0;
    return 29 + (q < 0 ? 0 : q > 7 ? 7 : q);
}
static_assert(QS0 >= 40 - LA, "Wq overwrites ring slot 3");
constexpr int slot_ch(int gs) { return gs < 40 ? gs >> 3 : 5 + ((gs >> 2) & 1); }
constexpr int slot_kk(int gs) { return gs < 40 ? (gs & 7) >> 1 : gs & 3; }
constexpr int slot_sg(int gs) { return gs < 40 ? gs & 1 : gs >= 48; }

DI void attn_role(const Args& a, LAS char* sm, const int xcd, const int j) {
    const int tid = threadIdx.x, lane = tid & 63, wave = __builtin_amdgcn_readfirstlane(tid >> 6);
    const int rw4 = wave & 3, hs = wave >> 2;
    const int hp = (xcd & 1) * 4 + (j & 3), rb = (xcd >> 1) * 4 + (j >> 2);
    const int h = 2 * hp + hs;
    const int c0 = (2 * rb + 2 * hp) % 7;
    const int r32 = lane & 31, hh = lane >> 5, blk = (lane >> 4) & 1, q = (lane >> 2) & 3, p = lane & 3;
    const u32x4 rx = mkdesc(a.x, 2048 * 896), rw = mkdesc(a.wqkv, 448 * 2688), rwo = mkdesc(a.wo, 448 * 896), dws = mkdesc(a.ws, WS_END);
    const rsrc_t rws = mkrsrc(a.ws, WS_END);
    u32 tag = ld_epoch(a.ws + WS_EPA), tch = 0;
    const int widx = (lane & 15) ^ ((lane >> 4) << 2);
    const u32 wv = (8 * wave + (lane >> 4)) * 2688 + (1 + (widx >> 3)) * 896 + hp * 112 + min(widx & 7, 6) * 16;
    const u32 xv0 = (rb * 128 + wave * 16 + (lane >> 3)) * 896 + (((lane & 7) ^ (lane >> 4)) * 16);
    const u32 xv1 = (rb * 128 + wave * 16 + 8 + (lane >> 3)) * 896 + (((lane & 7) ^ (4 + (lane >> 4))) * 16) - 1024;
    const u32 qv = (8 * wave + (lane >> 3)) * 2688 + hp * 112 + min((lane & 7) ^ (blk << 2), 6) * 16;
    auto kchunk = [&](int i) { int kc = i + c0; return kc >= 7 ? kc - 7 : kc; };
    auto ldn = [&](int n) {
        if (n >= 29) { setm0(sm + A_WQ + (n - 29) * 8192 + wave * 1024); dma<0>(rw, qv, kchunk(n - 29) * 64 * 2688); return; }
        if (n == 28) {
            tch = touch(dws, lane < 8 ? WS_KVP + (h * 16 + rb) * 2048 + rw4 * 512 + (lane & 3) * 128
                           : lane < 24 ? WS_OX + ((rb * 4 + rw4) * 16 + h) * 2048 + (lane - 8) * 128 : OOB);
            return;
        }
        const int i = n >> 2, m = n & 3;
        if (m == 0) { setm0(sm + (i & 3) * 32768 + 16384 + wave * 2048); dma<0>(rx, xv0, kchunk(i) * 128); }
        if (m == 1) dma<1024>(rx, xv1, kchunk(i) * 128);
        if (m == 2) { setm0(sm + (i & 3) * 32768 + wave * 2048); dma<0>(rw, wv, kchunk(i) * 64 * 2688); }
        if (m == 3) dma<1024>(rw, wv, kchunk(i) * 64 * 2688 + 4 * 2688 - 1024);
    };
#pragma unroll
    for (int n = 0; n < PRO; ++n) ldn(n);

    const int wb = 56 * hs + 32 * blk + 8 * p;
    LAS char *wsrc[2], *xsrc[4];
#pragma unroll
    for (int sg = 0; sg < 2; ++sg) wsrc[sg] = sm + (32 * hh + q) * 256 + (((2 * sg + (wb >> 6)) ^ q) << 6) + (wb & 63);
#pragma unroll
    for (int kk = 0; kk < 4; ++kk) xsrc[kk] = sm + 16384 + (32 * rw4 + r32) * 128 + (((4 * hh + kk) ^ ((r32 >> 1) & 7)) * 16);

    const bool p3 = j < 14;
    const int r128 = p3 ? xcd * 2 + j / 7 : 0, nb = j % 7;
    f32x16 acc[2] = {zero16(), zero16()};
    u32x4 xf[28], wf[56];
    auto rdw = [&](int gs) { LAS char* s = wsrc[slot_sg(gs)] + (slot_ch(gs) & 3) * 32768 + slot_kk(gs) * 2048; wf[gs] = trfrag(s, s + 1024); };
    auto rdx = [&](int i, int kk) { xf[4 * i + kk] = lds128(xsrc[kk] + (i & 3) * 32768); };
    waitv<PRO - 4>();
    wg_barrier();
#pragma unroll
    for (int sl = 0; sl < LA; ++sl) { if (sl < 4) rdx(0, sl); rdw(sl); }
    u32 pkd[8];
    u32 vm = r32 == 28 ? 0u : ~0u, vo = r32 == 28 ? 0x3f803f80u : 0u;
    pin(vm); pin(vo);
    sfor<56>([&](auto gs) {
        constexpr int issued = issued_before(gs.v);
        constexpr int fu = gs.v + LA, c = fu == 44 ? 6 : (fu <= 40 && (fu & 7) == 0) ? fu >> 3 : 0;
        static_assert(!c || issued >= 4 * c + 4, "chunk not issued yet");
        if constexpr (c != 0) { waitv<issued - (4 * c + 4)>(); raw_barrier(); }
        SB();
        acc[slot_sg(gs.v)] = mfma32(xf[4 * slot_ch(gs.v) + slot_kk(gs.v)], wf[gs.v], acc[slot_sg(gs.v)]);
        SB();
        if constexpr (fu < 40 && fu >= 8 && (fu & 1) == 0) rdx(fu >> 3, (fu & 7) >> 1);
        if constexpr (fu >= 40 && fu < 48) rdx(slot_ch(fu), fu & 3);
        if constexpr (fu < 56) rdw(fu);
        if constexpr (gs.v == 44) raw_barrier();
        if constexpr (gs.v >= 49) { pkd[gs.v - 49] = pk(phi(acc[0][2 * (gs.v - 49)]), phi(acc[0][2 * (gs.v - 49) + 1])); pin(pkd[gs.v - 49]); }
#pragma unroll
        for (int n = issued; n < issued_before(gs.v + 1); ++n) ldn(n);
        SB();
    });
    {
        pkd[7] = pk(phi(acc[0][14]), phi(acc[0][15]));
        pin(pkd[7]);
        f32x16 kv = mfma32(u32x4{pkd[0], pkd[1], pkd[2], pkd[3]}, (pk8(acc[1], 0) & vm) | vo, zero16());
        SB();
        kv = mfma32(u32x4{pkd[4], pkd[5], pkd[6], pkd[7]}, (pk8(acc[1], 1) & vm) | vo, kv);
#pragma unroll
        for (int rq = 0; rq < 4; ++rq) sts128(sm + A_RED + wave * 4096 + rq * 1024 + lane * 16, quad(kv, rq));
    }
    asm volatile("s_waitcnt vmcnt(8)" : "+v"(tag) :: "memory");
    tag += 1;
    wg_barrier();
    u32 hm = hh ? 0u : ~0u, cm = r32 == 31 ? 0u : ~0u;
    pin(hm); pin(cm);
    const bool red = (rw4 >> 1) == hs;
    if (red) {
        LAS char* rs = sm + A_RED + hs * 16384 + (rw4 & 1) * 2048 + lane * 16;
        f32x4 lo = (asf(lds128(rs)) + asf(lds128(rs + 4096))) + (asf(lds128(rs + 8192)) + asf(lds128(rs + 12288)));
        f32x4 hi = (asf(lds128(rs + 1024)) + asf(lds128(rs + 5120))) + (asf(lds128(rs + 9216)) + asf(lds128(rs + 13312)));
        u32x4 v = {pk(lo.x, lo.y), pk(lo.z, lo.w), pk(hi.x, hi.y), pk(hi.z, hi.w) & cm};
        v.w |= (tag - sum32(sum4(v))) & ~cm;
        bst_dev(v, rws, WS_KVP + (h * 16 + rb) * 2048 + (tid & 127) * 16);
    }
    cbar();
    if (red) waitv<1>(); else waitv<0>();
    KEEP(tch);
    raw_barrier();
    const u32 ov = (8 * wave + (lane >> 3)) * 896 + nb * 128 + (((lane & 7) ^ (blk << 2)) * 16);
    u32x4 gk[8];
    const u32 kbase = WS_KVP + (h * 16 + (rw4 >> 1) * 8 + (rb & 7)) * 2048, kvo = (tid & 127) * 16;
    f32x16 aq = zero16();
    {
        LAS char* qsrc = sm + A_WQ + (32 * hh + q) * 128 + (((wb >> 6) ^ (q >> 1)) << 6) + (wb & 63);
        u32x4 wq[28];
        auto rdq = [&](int k) { LAS char* s = qsrc + (k >> 2) * 8192 + (k & 3) * 1024; wq[k] = trfrag(s, s + 512); };
#pragma unroll
        for (int k = 0; k < 4; ++k) rdq(k);
        SB();
#pragma unroll
        for (int k = 0; k < 28; ++k) {
            aq = mfma32(wq[k], xf[k], aq);
            SB();
            if (k + 4 < 28) rdq(k + 4);
            if (k < 7) { setm0(sm + A_WO + k * 8192 + wave * 1024); dma<0>(rwo, ov, k * 64 * 896); }
            if (k >= GK0 && k < GK0 + 8) ldd(gk[k - GK0], dws, kvo, kbase ^ ((k - GK0) * 2048));
            SB();
        }
    }
    u32 pqd[8];
#pragma unroll
    for (int k = 0; k < 8; ++k) {
        pqd[k] = pk(phi(aq[2 * k]), phi(aq[2 * k + 1]));
        if (k >= 6) pqd[k] &= hm;
        pin(pqd[k]);
    }
    {
        waitx<0>(gk); waitx<0>(gk + 4);
        u32 t = 0;
#pragma unroll
        for (int i = 0; i < 8; ++i) t += sum4(gk[i]);
        if (!__all(sum32(t) == 8u * tag)) {
            auto ok = [&](int i) { return sum32(sum4(gk[i])) == tag; };
            settle<8>(badmask<8>(ok), ok, [&](auto i, u32 bm) { reld<i.v>(gk[i.v], dws, kvo, kbase ^ (i.v * 2048), bm); }, [&](int i) { pin(gk[i]); });
        }
    }
    {
        u32x4 v;
#pragma unroll
        for (int c = 0; c < 4; ++c) {
            const float lo = ((bflo(gk[0][c]) + bflo(gk[1][c])) + (bflo(gk[2][c]) + bflo(gk[3][c]))) + ((bflo(gk[4][c]) + bflo(gk[5][c])) + (bflo(gk[6][c]) + bflo(gk[7][c])));
            const float hi = ((bfhi(gk[0][c]) + bfhi(gk[1][c])) + (bfhi(gk[2][c]) + bfhi(gk[3][c]))) + ((bfhi(gk[4][c]) + bfhi(gk[5][c])) + (bfhi(gk[6][c]) + bfhi(gk[7][c])));
            v[c] = pk(lo, hi);
        }
        sts128(sm + A_KVB + tid * 16, v);
    }
    wg_barrier();
    {
        u32x4 kf[4];
#pragma unroll
        for (int i = 0; i < 4; ++i) kf[i] = lds128(sm + A_KVB + hs * 4096 + i * 1024 + lane * 16);
        const u32x4 pq0 = {pqd[0], pqd[1], pqd[2], pqd[3]}, pq1 = {pqd[4], pqd[5], pqd[6], pqd[7]};
        f32x16 oa = mfma32(kf[3], pq1, mfma32(kf[2], pq0, mfma32(kf[1], pq1, mfma32(kf[0], pq0, zero16()))));
        const u32 dn = __float_as_uint(oa[12]);
        auto r = __builtin_amdgcn_permlane32_swap(dn, dn, false, false);
        oa *= __builtin_amdgcn_rcpf(__uint_as_float(r[1]) + 1e-6f);
        const u32x4 f0 = pk8(oa, 0);
        const u32 o = WS_OX + ((rb * 4 + rw4) * 16 + h) * 2048 + lane * 16;
        bst_dev(f0, rws, o);
        SB();
        u32x4 f1 = pk8(oa, 1);
        f1.z &= hm; f1.w &= hm;
        f1.w |= (tag - halfsum(sum4(f0) + sum4(f1))) & ~hm;
        bst_dev(f1, rws, o + 1024);
    }
    cbar();
    if (p3) {
        LAS char* osrc[2];
#pragma unroll
        for (int nt = 0; nt < 2; ++nt) osrc[nt] = sm + A_WO + (224 * hs + 4 * hh + q) * 128 + ((nt ^ (q >> 1)) * 64) + blk * 32 + 8 * p;
        const int padoff = hh ? 0 : 1024;
        u32x4 wf3[8][2][2];
#pragma unroll
        for (int k = 0; k < 32; ++k) {
            const int hl = k >> 2, ks = (k >> 1) & 1, nt = k & 1;
            LAS char* s = osrc[nt] + (hl * 28 + 16 * ks) * 128;
            wf3[hl][ks][nt] = trfrag(s, s + (ks ? padoff : 1024));
        }
        SB();
        waitv<0>();
        u32x4 go[16];
        const u32 ob0 = WS_OX + ((r128 * 4 + rw4) * 16 + 8 * hs) * 2048;
        auto ldo = [&](int i) { ldd(go[i], dws, lane * 16, ob0 + i * 1024); };
#pragma unroll
        for (int i = 0; i < 4 * G2A; ++i) ldo(i);
        f32x16 ac[2];
        sfor<4>([&](auto gi) {
            constexpr int i = 4 * gi.v, nis = gi.v + G2A < 4 ? gi.v + G2A : 4;
            waitx<4 * (nis - gi.v - 1)>(go + i);
            {
                auto r0 = __builtin_amdgcn_permlane32_swap(sum4(go[i]) + sum4(go[i + 1]), sum4(go[i + 2]) + sum4(go[i + 3]), false, false);
                const u64 ne = __ballot(r0[0] + r0[1] != tag);
                if (ne)
                    settle<2>(((u32)ne != 0) | ((ne >> 32) != 0) << 1, [&](int k) { return halfsum(sum4(go[i + 2 * k]) + sum4(go[i + 2 * k + 1])) == tag; },
                              [&](auto k, u32 bm) { reld<k.v>(go[i + 2 * k.v], dws, lane * 16, ob0 + (i + 2 * k.v) * 1024, bm);
                                                    reld<k.v>(go[i + 2 * k.v + 1], dws, lane * 16, ob0 + (i + 2 * k.v + 1) * 1024, bm); },
                              [&](int k) { pin(go[i + 2 * k]); pin(go[i + 2 * k + 1]); });
            }
            go[i + 1].w &= hm; go[i + 3].w &= hm;
            SB();
#pragma unroll
            for (int k = 0; k < 8; ++k) {
                const int hl = 2 * gi.v + (k >> 2), ks = (k >> 1) & 1, nt = k & 1;
                ac[nt] = mfma32(wf3[hl][ks][nt], go[2 * hl + ks], (gi.v == 0 && k < 2) ? zero16() : ac[nt]);
                SB();
                if ((k & 1) == 0 && gi.v + G2A < 4) ldo(4 * (gi.v + G2A) + (k >> 1));
                SB();
            }
        });
        LAS char* xb = sm + wave * 4096 + lane * 16;
        if (hs) {
#pragma unroll
            for (int rq = 0; rq < 4; ++rq) sts128(xb + rq * 1024, quad(ac[0], rq));
            ac[0] = ac[1];
        } else {
#pragma unroll
            for (int rq = 0; rq < 4; ++rq) sts128(xb + rq * 1024, quad(ac[1], rq));
        }
        wg_barrier();
        LAS char* pb = sm + (wave ^ 4) * 4096 + lane * 16;
        LAS char* ob = sm + A_WQ + rw4 * 4608;
#pragma unroll
        for (int rq = 0; rq < 4; ++rq) {
            const f32x4 v = asf(quad(ac[0], rq)) + asf(lds128(pb + rq * 1024));
            sts64(ob + r32 * 144 + hs * 64 + rq * 16 + hh * 8, pk(v.x, v.y), pk(v.z, v.w));
        }
        wg_barrier();
        const rsrc_t ro = mkrsrc(a.out, 2048 * 1792);
        const int orow = 16 * hs + (lane >> 3);
        const u32 oo = (r128 * 128 + rw4 * 32 + orow) * 1792 + 896 + nb * 128 + (lane & 7) * 16;
        const u32x4 v0 = lds128(ob + orow * 144 + (lane & 7) * 16), v1 = lds128(ob + (8 + orow) * 144 + (lane & 7) * 16);
        bst_dev(v0, ro, oo);
        bst_dev(v1, ro, oo + 8 * 1792);
        if (xcd == 0 && j == 0 && tid == 0) st_epoch(a.ws + WS_EPA, tag);
    }
}

constexpr int S_TAB = 0;
constexpr int S_KC = 34816;
constexpr int S_P = 47104;
constexpr int S_UT = 52480;
constexpr int S_TR = 61696;

struct c2 { f32x2 r, i; };
struct hc { h2 r, i; };
DI c2 csq(c2 a) { return c2{a.r * a.r - a.i * a.i, 2.f * a.r * a.i}; }
DI hc hmul(hc a, hc b) { return hc{a.r * b.r - a.i * b.i, a.r * b.i + a.i * b.r}; }
DI hc toh(c2 a) { return hc{__builtin_convertvector(a.r, h2), __builtin_convertvector(a.i, h2)}; }
DI c2 cexp2(f32x2 lr, f32x2 rev, float pw) {
    f32x2 e = lr * pw, f = rev * pw;
    f32x2 m = {__builtin_amdgcn_exp2f(e.x), __builtin_amdgcn_exp2f(e.y)};
    f.x = __builtin_amdgcn_fractf(f.x); f.y = __builtin_amdgcn_fractf(f.y);
    f32x2 c = {__builtin_amdgcn_cosf(f.x), __builtin_amdgcn_cosf(f.y)}, s = {__builtin_amdgcn_sinf(f.x), __builtin_amdgcn_sinf(f.y)};
    return c2{m * c, m * s};
}
constexpr int tri_a(int sl) { int a = 0; while (sl >= 8 - a) { sl -= 8 - a; ++a; } return a; }
constexpr int tri_b(int sl) { int a = 0; while (sl >= 8 - a) { sl -= 8 - a; ++a; } return sl; }

DI void ssm_role(const Args& a, LAS char* sm0, const int sb) {
    const int tid = threadIdx.x & 255, lane = tid & 63, wave8 = __builtin_amdgcn_readfirstlane(threadIdx.x >> 6), wave = wave8 & 3;
    LAS char* sm = sm0 + (wave8 >> 2) * 81920;
    const int ch = wave >> 1, hf = wave & 1, d = 4 * sb + (wave8 >> 1);
    const int g = lane >> 4, r16 = lane & 15;
    const u32 po = (d * 128 + hf * 64 + lane) * 4;
    const u32 ldt = a.logdt[d];
    const u32 pA = *(const u32*)((const char*)a.logA + po), pI = *(const u32*)((const char*)a.Aim + po);
    const u32 pCr = *(const u32*)((const char*)a.Cre + po), pCi = *(const u32*)((const char*)a.Cim + po);
    const u32x2 dsk = ((const u32x2*)a.Dskip)[sb];
    cbar();
    const int G8 = (wave8 + sb) & 7, trow = 256 * G8 + 16 * r16 + 4 * g;
    const rsrc_t ru = mkrsrc(a.u, 2048 * 896);
    u32x2 uu[4];
    auto ldu = [&](int i) { SB(); uu[i] = __builtin_amdgcn_raw_buffer_load_b64(ru, trow * 896 + sb * 8 + i * 896, 0, 0); cbar(); SB(); };
    const int sidx = 2 * sb + (wave8 >> 2);
    const int ct = sidx >> 5, j = sidx & 31;
    u32 tag = ld_epoch(a.ws + WS_EPS + ct * 256);
    ldu(0);
    *(LAS u32*)(sm + S_UT + (tid >> 7) * 4608 + (tid & 127) * 4) = 0;
    const int w = 16 + r16 - 8 * g;
    u32x4 bE, bO, mkb, mkd;
    u32 sel[3][4], msk[4], pb[3];
    {
        u32 y[4];
#pragma unroll
        for (int e = 0; e < 4; ++e) {
            y[e] = (u32)(w + 64 - 2 * e) * 0x10001u - 0x10000u;
            msk[e] = hf ? 0u : (((y[e] >> 6) & 0x10001u) ^ 0x10001u) * 0xffffu;
        }
        pb[0] = w - 7;
#pragma unroll
        for (int s = 1; s < 3; ++s) {
            u32 r[4];
#pragma unroll
            for (int e = (s == 1 ? 2 : 0); e < 4; ++e)
                r[e] = ((y[e] + ((1u << (s - 1)) - 1u) * 0x10001u + ((y[e] >> s) & 0x10001u)) >> s) & 0x00ff00ffu;
            const u32 base = r[3] >> 16;
            pb[s] = base - (64 >> s);
#pragma unroll
            for (int e = (s == 1 ? 2 : 0); e < 4; ++e) sel[s][e] = (((r[e] - base * 0x10001u) ^ 0x10001u) & 0xfffffu) * 0x202u + 0x01000100u;
        }
#pragma unroll
        for (int e = 0; e < 4; ++e) {
            const int z = min(max(16 - w + 2 * e, 0), 20);
            auto two = [&](u32 m) { const u32 b2 = (m >> z) & 3u; return (b2 | (b2 << 15)) & 0x10001u; };
            bE[e] = two(0x1ff0u) * 0x3f80u; bO[e] = two(0x0fe0u) * 0x3f80u; mkb[e] = two(0x1f000u) * 0xffffu;
            mkd[e] = r16 - 4 * g - e >= 0 ? ~0u : 0u;
        }
        asm volatile("" : "+v"(sel[1][2]), "+v"(sel[1][3]), "+v"(sel[2][0]), "+v"(sel[2][1]), "+v"(sel[2][2]), "+v"(sel[2][3]), "+v"(bE), "+v"(bO), "+v"(mkb), "+v"(mkd));
        asm volatile("" : "+v"(msk[0]), "+v"(msk[1]), "+v"(msk[2]), "+v"(msk[3]));
    }
    LAS u32* const R = (LAS u32*)(sm + S_P + ch * 2688) + 655;
    LAS u32 *P0 = R - (128 * hf + (int)pb[0]) - 102, *P1 = R - (256 + 64 * hf + (int)pb[1]) - 132, *P2 = R - (384 + 32 * hf + (int)pb[2]) - 130,
            *P3 = R - (385 + 2 * (r16 - 4 * g)) - 224;
    LAS char* ubase = sm + S_UT + ch * 4608 + 480 + r16 * 32 + g * 16 - 256 * hf - 192;
    asm volatile("" : "+v"(P0), "+v"(P1), "+v"(P2), "+v"(P3), "+v"(ubase));
    LAS char* tab = sm + S_TAB + wave * 8704;
    LAS char* tw = tab + lane * 8;
    LAS char* tr = tab + r16 * 544 + g * 16;
    LAS char* kc = sm + S_KC + wave * 3072;
    SB();
#define EMIT1(row, v) sts64(tw + (row) * 544, __builtin_bit_cast(u32, (v).r), __builtin_bit_cast(u32, (v).i))
#define EMIT2(row, v0, v1) do { SB(); EMIT1(row, v0); EMIT1((row) + 1, v1); SB(); } while (0)
#define KRD() do { SB(); _Pragma("unroll") for (int ks = 0; ks < 8; ++ks) gf[ks] = lds128(tr + ks * 64); cbar(); SB(); } while (0)
#define KM2(k) do { ka = mfmah(gf[2 * (k)], ff[2 * (k)], (k) ? ka : zero4()); ka = mfmah(gf[2 * (k) + 1], ff[2 * (k) + 1], ka); } while (0)
    const float L2E = 1.44269504089f;
    const float edt = __builtin_amdgcn_exp2f(bflo(ldt) * L2E);
    const float dt = bflo(pk(edt, edt));
    f32x2 lr = unpk(pA) * L2E;
    lr = unpk(pk(__builtin_amdgcn_exp2f(lr.x), __builtin_amdgcn_exp2f(lr.y))) * dt * -L2E;
    const f32x2 rev = unpk(pI) * dt * 0.15915494309f;
    hc W[8], F[16], G[17], H, t0, t1;
    u32x4 ff[8], gf[8];
    f32x4 ka;
    c2 e = cexp2(lr, -rev, 8.f);
    F[1] = toh(e); e = csq(e); F[2] = toh(e);
    ldu(1);
    F[0] = hc{h2{1.f16, 1.f16}, h2{0.f16, 0.f16}};
    EMIT2(0, F[0], F[1]);
    e = csq(e); F[4] = toh(e); F[3] = hmul(F[1], F[2]);
    EMIT2(2, F[2], F[3]);
    e = csq(e); F[8] = toh(e); F[5] = hmul(F[4], F[1]);
    EMIT2(4, F[4], F[5]);
    F[6] = hmul(F[4], F[2]); F[7] = hmul(F[4], F[3]); F[9] = hmul(F[8], F[1]);
    EMIT2(6, F[6], F[7]);
    ldu(2);
    EMIT2(8, F[8], F[9]);
    F[10] = hmul(F[8], F[2]); F[11] = hmul(F[8], F[3]);
    e = cexp2(lr, rev, 1.f); W[1] = toh(e);
    EMIT2(10, F[10], F[11]);
    F[12] = hmul(F[8], F[4]); F[13] = hmul(F[8], F[5]);
    e = csq(e); W[2] = toh(e);
    EMIT2(12, F[12], F[13]);
    F[14] = hmul(F[8], F[6]); F[15] = hmul(F[8], F[7]);
    e = csq(e); W[4] = toh(e);
    EMIT2(14, F[14], F[15]);
    ldu(3);
    u32 ce = max(max(pCr & 0x7fffu, (pCr >> 16) & 0x7fffu), max(pCi & 0x7fffu, (pCi >> 16) & 0x7fffu));
    ce = min(max(wavemax(ce) >> 7, 1u), 253u);
    const float cdn = __uint_as_float((254u - ce) << 23), cup = __uint_as_float(ce << 23) * (2.f * dt);
    G[0] = toh(c2{unpk(pCr) * cdn, unpk(pCi) * cdn});
    t1 = hmul(G[0], W[1]);
    SB();
#pragma unroll
    for (int ks = 0; ks < 8; ++ks) ff[ks] = lds128(tr + ks * 64);
    cbar();
    EMIT2(0, G[0], t1);
    W[3] = hmul(W[1], W[2]); t0 = hmul(G[0], W[2]); t1 = hmul(G[0], W[3]);
    EMIT2(2, t0, t1);
    W[5] = hmul(W[4], W[1]); t0 = hmul(G[0], W[4]); t1 = hmul(G[0], W[5]);
    EMIT2(4, t0, t1);
    W[6] = hmul(W[4], W[2]); W[7] = hmul(W[4], W[3]); t0 = hmul(G[0], W[6]); t1 = hmul(G[0], W[7]);
    EMIT2(6, t0, t1);
    e = cexp2(lr, rev, 128.f); H = toh(e);
    G[1] = hmul(G[0], H); t1 = hmul(G[1], W[1]);
    EMIT2(8, G[1], t1);
    t0 = hmul(G[1], W[2]); t1 = hmul(G[1], W[3]);
    e = csq(e); H = toh(e);
    EMIT2(10, t0, t1);
    t0 = hmul(G[1], W[4]); t1 = hmul(G[1], W[5]); G[2] = hmul(G[0], H);
    EMIT2(12, t0, t1);
    t0 = hmul(G[1], W[6]); t1 = hmul(G[1], W[7]); G[3] = hmul(G[1], H);
    EMIT2(14, t0, t1);
    t1 = hmul(G[2], W[2]);
    e = csq(e); H = toh(e);
    KRD();
    EMIT2(0, G[2], t1);
    t0 = hmul(G[2], W[4]); t1 = hmul(G[2], W[6]); G[4] = hmul(G[0], H);
    EMIT2(2, t0, t1);
    t1 = hmul(G[3], W[2]); G[5] = hmul(G[1], H); KM2(0);
    EMIT2(4, G[3], t1);
    t0 = hmul(G[3], W[4]); t1 = hmul(G[3], W[6]); KM2(1);
    EMIT2(6, t0, t1);
    t1 = hmul(G[4], W[4]); G[6] = hmul(G[2], H); KM2(2);
    EMIT2(8, G[4], t1);
    t1 = hmul(G[5], W[4]); G[7] = hmul(G[3], H); KM2(3);
    EMIT2(10, G[5], t1);
    t1 = hmul(G[6], W[4]);
    e = csq(e); H = toh(e);
    EMIT2(12, G[6], t1);
    t1 = hmul(G[7], W[4]); G[8] = hmul(G[0], H);
    sts128(kc + (128 * (g >> 1) + 4 * (g & 1) + 8 * r16) * 4, asu(ka * cup));
    EMIT2(14, G[7], t1);
    G[9] = hmul(G[1], H); G[10] = hmul(G[2], H);
    KRD();
    EMIT2(0, G[8], G[9]);
    G[11] = hmul(G[3], H); G[12] = hmul(G[4], H); G[13] = hmul(G[5], H);
    EMIT2(2, G[10], G[11]);
    G[14] = hmul(G[6], H); KM2(0); KM2(1);
    EMIT2(4, G[12], G[13]);
    G[15] = hmul(G[7], H); G[16] = hmul(G[8], H); KM2(2); KM2(3);
    EMIT2(6, G[14], G[15]);
    EMIT1(8, G[16]);
    KRD();
    {
        LAS char* k0 = kc + (g < 2 ? 256 + 64 * g + 4 * r16 : 384 + 64 * (g - 2) + 2 * r16) * 4;
        const u32x4 kb = asu(ka * cup);
        sts64(k0, kb.x, kb.y);
        sts64(k0 + (g < 2 ? 8 : 128), kb.z, kb.w);
    }
    SB();
    KM2(0); KM2(1); KM2(2); KM2(3);
    {
        LAS char* ud = sm0 + S_UT + 512 + trow * 2;
        sts64(ud, PERM(uu[1].x, uu[0].x, 0x05040100u), PERM(uu[3].x, uu[2].x, 0x05040100u));
        sts64(ud + 4608, PERM(uu[1].x, uu[0].x, 0x07060302u), PERM(uu[3].x, uu[2].x, 0x07060302u));
        sts64(ud + 81920, PERM(uu[1].y, uu[0].y, 0x05040100u), PERM(uu[3].y, uu[2].y, 0x05040100u));
        sts64(ud + 81920 + 4608, PERM(uu[1].y, uu[0].y, 0x07060302u), PERM(uu[3].y, uu[2].y, 0x07060302u));
    }
    {
        LAS float* k0 = (LAS float*)(kc + (512 + 64 * g + r16) * 4);
        k0[0] = ka.x * cup; k0[16] = ka.y * cup; k0[32] = ka.z * cup; k0[48] = ka.w * cup;
    }
    wg_barrier();
    const rsrc_t rws = mkrsrc(a.ws, WS_END);
    const u32x4 dws = mkdesc(a.ws, WS_END);
    u32 tch = touch(dws, lane < 16 ? WS_YX + (2 * sb + (lane >> 3)) * 8192 + G8 * 1024 + (lane & 7) * 128 : lane == 16 ? WS_YC + (sb * 8 + G8) * 128 : OOB);
    u32x4 U[2][8];
    auto rdU = [&](int it, int bq) { U[it & 1][bq] = lds128(ubase + 512 * bq + 192 - 64 * it); };
    {
        LAS char* k0 = sm + S_KC + ch * 6144;
        LAS char* Rb = sm + S_P + ch * 2688;
        const int t = tid & 127, e1 = min(t + 128, 167);
        f32x4 a0 = asf(lds128(k0 + t * 16)), b0 = asf(lds128(k0 + 3072 + t * 16)), a1 = asf(lds128(k0 + e1 * 16)), b1 = asf(lds128(k0 + 3072 + e1 * 16));
        const float n0 = *(LAS float*)(k0 + t * 16 + 16) + *(LAS float*)(k0 + 3072 + t * 16 + 16);
        const float n1 = *(LAS float*)(k0 + e1 * 16 + 16) + *(LAS float*)(k0 + 3072 + e1 * 16 + 16);
#pragma unroll
        for (int bq = 0; bq < 8; ++bq) rdU(3, bq);
        a0 += b0; a1 += b1;
        float p0 = a0.x, p1 = a0.y, p2 = a0.z, p3 = a0.w, q0 = a1.x, q1 = a1.y, q2 = a1.z, q3 = a1.w;
        asm volatile("" : "+v"(p0), "+v"(p1), "+v"(p2), "+v"(p3), "+v"(q0), "+v"(q1), "+v"(q2), "+v"(q3));
        sts128(Rb + (163 - t) * 16, u32x4{pk(n0, p3), pk(p3, p2), pk(p2, p1), pk(p1, p0)});
        u32x4 v1 = {pk(n1, q3), pk(q3, q2), pk(q2, q1), pk(q1, q0)};
        if (t >= 36) v1 = u32x4{0, 0, 0, 0};
        if (t < 39) sts128(Rb + (t < 36 ? 35 - t : 129 + t) * 16, v1);
        if (t == 0) sts128(Rb + 164 * 16, u32x4{pk(p0, 0.f), 0, 0, 0});
    }
    wg_barrier();
    f32x4 y[8];
    {
#define Q0(i) P0[102 - (i)]
#define Q1(i) P1[132 - (i)]
#define Q2(i) P2[130 - (i)]
#define Q3(i) P3[224 - (i)]
        u32x4 T[2][4], T32, TB[4], UW[4], WA[2];
        f32x4 wd[4];
        u32 raw[7], rx[10];
        auto rdT = [&](int it, int k) {
            u32x4& f0 = T[it & 1][0];
            if (k == 0) { f0.x = Q0(32 * it + 6); f0.y = Q0(32 * it + 4); }
            if (k == 1) { f0.z = Q0(32 * it + 2); f0.w = Q0(32 * it); }
            if (k == 2) { raw[0] = Q1(16 * it); raw[1] = Q1(16 * it + 2); raw[2] = Q1(16 * it + 4); }
            if (k == 3) { raw[3] = Q2(8 * it); raw[4] = Q2(8 * it + 2); }
            if (k == 4) { raw[5] = Q2(8 * it + 64); raw[6] = Q2(8 * it + 66); }
        };
        auto mkT = [&](int it, int aa, int e) {
            T[it & 1][aa][e] = aa == 1 ? (e < 2 ? PERM(raw[2], raw[1], sel[1][2 + e]) : PERM(raw[1], raw[0], sel[1][e])) : PERM(raw[2 * aa], raw[2 * aa - 1], sel[2][e]);
        };
        auto rdX = [&](int k) {
            if (k == 0) { rx[0] = Q0(262); rx[1] = Q0(260); }
            if (k == 1) { rx[2] = Q0(258); rx[3] = Q0(256); }
            if (k == 2) { rx[4] = Q1(128); rx[5] = Q1(130); rx[6] = Q1(132); }
            if (k == 3) { rx[7] = Q2(128); rx[8] = Q2(130); rx[9] = R[-512]; }
        };
        auto fixT = [&](int k, int e) {
            if (k < 2) {
                const u32 lo = k == 0 ? rx[e] : (e < 2 ? PERM(rx[6], rx[5], sel[1][2 + e]) : PERM(rx[5], rx[4], sel[1][e]));
                T[0][1 + k][e] = (lo & msk[e]) | (T[0][1 + k][e] & ~msk[e]);
            } else T32[e] = (PERM(rx[8], rx[7], sel[2][e]) & msk[e]) | (PERM(rx[9], rx[9], 0x03020302u) & mkb[e]);
        };
        LAS char* uw = ubase + 192 + 256 * hf + 1024 * (1 - hf);
        auto rdC = [&](int k) {
            if (k < 4) UW[k] = lds128(uw + 512 * (k >> 1) - 16 * (k & 1));
            else { const int dl = (k - 4) >> 1, e = 2 * (k & 1); TB[dl][e] = Q3(128 + 32 * dl - 2 * e); TB[dl][e + 1] = Q3(128 + 32 * dl - 2 * e - 2); }
        };
#pragma unroll
        for (int k = 0; k < 5; ++k) rdT(3, k);
        SB();
        sfor<104>([&](auto gs) {
            constexpr int it = 3 - gs.v / 26, sl = gs.v % 26, aa = tri_a(sl), bq = tri_b(sl);
            y[aa + bq] = mfma(T[it & 1][aa], U[it & 1][bq], (it == 3 && aa == 0) ? zero4() : y[aa + bq]);
            SB();
            if constexpr (it == 3) {
                if constexpr (sl < 6) { mkT(3, 1 + (sl >> 1), 2 * (sl & 1)); mkT(3, 1 + (sl >> 1), 2 * (sl & 1) + 1); }
                else if constexpr (sl < 14) { if (sl < 11) rdT(2, sl - 6); rdU(2, sl - 6); }
                else mkT(2, 1 + ((sl - 14) >> 2), (sl - 14) & 3);
            } else if constexpr (it > 0) {
                if constexpr (sl < 8) { if (sl < 5) rdT(it - 1, sl); rdU(it - 1, sl); }
                if constexpr (it == 1 && sl >= 5 && sl < 9) rdX(sl - 5);
                if constexpr (it == 2 && sl >= 10 && sl < 22) mkT(1, 1 + ((sl - 10) >> 2), (sl - 10) & 3);
                if constexpr (it == 1 && sl >= 10 && sl < 16) { mkT(0, 1 + ((sl - 10) >> 1), 2 * (sl & 1)); mkT(0, 1 + ((sl - 10) >> 1), 2 * (sl & 1) + 1); }
                if constexpr (it == 1 && sl >= 16 && sl < 24) fixT((sl - 16) >> 2, (sl - 16) & 3);
            } else {
                if constexpr (sl < 12) rdC(sl);
                if constexpr (sl < 4) fixT(2, sl);
                if constexpr (sl >= 10 && sl < 14) wd[sl - 10] = mfma(UW[sl - 10], (sl & 1) ? bE : bO, zero4());
                if constexpr (sl >= 16 && sl < 24) WA[(sl - 16) >> 2][sl & 3] = pk(wd[2 * ((sl - 16) >> 2) + 1][sl & 3], wd[2 * ((sl - 16) >> 2)][sl & 3]);
                if constexpr (sl == 24) TB[0] &= mkd;
            }
            SB();
        });
        if (hf) {
#pragma unroll
            for (int k = 0; k < 4; ++k) y[4 + k] = mfma(WA[0], TB[k], y[4 + k]);
#pragma unroll
            for (int k = 0; k < 3; ++k) y[5 + k] = mfma(WA[1], TB[k], y[5 + k]);
        } else {
#pragma unroll
            for (int k = 0; k < 4; ++k) y[4 + k] = mfma(T32, U[0][k], y[4 + k]);
            y[6] = mfma(WA[0], TB[0], y[6]); y[7] = mfma(WA[0], TB[1], y[7]); y[7] = mfma(WA[1], TB[0], y[7]);
        }
    }
    {
        LAS char* yd = sm + S_TAB + wave * 8192 + lane * 16;
#pragma unroll
        for (int i = 0; i < 8; ++i) sts128(yd + i * 1024, asu(y[i]));
    }
    asm volatile("s_waitcnt vmcnt(0)" : "+v"(tag), "+v"(tch) :: "memory");
    tag += 1;
    wg_barrier();
    {
        LAS char* ys = sm0 + S_TAB + G8 * 1024 + lane * 16;
        u32x4 o[2];
#pragma unroll
        for (int c = 0; c < 2; ++c) {
            const f32x4 y0 = asf(lds128(ys + c * 81920)) + asf(lds128(ys + c * 81920 + 8192));
            const f32x4 y1 = asf(lds128(ys + c * 81920 + 16384)) + asf(lds128(ys + c * 81920 + 24576));
            const float d0 = bflo(dsk[c]), d1 = bfhi(dsk[c]);
#pragma unroll
            for (int i = 0; i < 4; ++i) o[c][i] = pk(y0[i] + bflo(uu[i][c]) * d0, y1[i] + bfhi(uu[i][c]) * d1);
            bst_dev(o[c], rws, WS_YX + (2 * sb + c) * 8192 + G8 * 1024 + lane * 16);
        }
        const u32 s0 = rowsum(sum4(o[0])), s1 = rowsum(sum4(o[1]));
        bst32_dev(tag - (r16 ? s1 : s0), rws, r16 < 2 ? WS_YC + (sb * 8 + G8) * 128 + g * 8 + r16 * 4 : OOB);
    }
    {
        const u32 cofs = (ct * 32 + (tid >> 4)) * 8192 + j * 256 + (tid & 15) * 16;
        const u32 ccs = ((ct * 16 + (tid >> 5)) * 8 + (j >> 2)) * 128 + (j & 3) * 8 + ((tid >> 4) & 1) * 4;
        LAS char* trw = sm + S_TR + (tid & 7) * 288 + (tid >> 3) * 8;
        LAS char* trr = sm + S_TR + (((tid >> 4) & 3) * 8 + (tid >> 6)) * 288 + (tid & 15) * 16;
        const u32 oo = (256 * (j >> 2) + 16 * (tid >> 4) + 4 * (j & 3) + 2 * ((tid >> 3) & 1)) * 1792 + 128 * ct + (tid & 7) * 16;
        waitv<0>();
        u32x4 v[2]; u32 vc[2];
#pragma unroll
        for (int i = 0; i < 2; ++i) { v[i] = bld_dev(rws, cofs, WS_YX + i * 16 * 8192); vc[i] = bld32_dev(rws, ccs, WS_YC + i * 8192); }
        cbar();
        auto ok = [&](int i) { return rowsum(sum4(v[i])) + vc[i] == tag; };
        settle<2>(badmask<2>(ok), ok,
                  [&](auto i, u32 bm) { reld<i.v>(v[i.v], dws, cofs, WS_YX + i.v * 16 * 8192, bm); reld32<i.v>(vc[i.v], dws, ccs, WS_YC + i.v * 8192, bm); },
                  [&](int i) { pin(v[i]); pin(vc[i]); });
        sts128(trr, v[0]); sts128(trr + 4 * 288, v[1]);
        wg_barrier();
        const u32x2 s0 = *(LAS u32x2*)trw, s1 = *(LAS u32x2*)(trw + 8 * 288), s2 = *(LAS u32x2*)(trw + 16 * 288), s3 = *(LAS u32x2*)(trw + 24 * 288);
        const rsrc_t ro = mkrsrc(a.out, 2048 * 1792);
        bst_dev(u32x4{s0.x, s1.x, s2.x, s3.x}, ro, oo);
        bst_dev(u32x4{s0.y, s1.y, s2.y, s3.y}, ro, oo + 1792);
        if (j == 0 && tid == 0) st_epoch(a.ws + WS_EPS + ct * 256, tag);
    }
}

DI void warm_attn(const Args& a, LAS char* sm, int xcd, int hi) {
    const int lane = threadIdx.x & 63, wave8 = __builtin_amdgcn_readfirstlane(threadIdx.x >> 6);
    const int u = hi * 5 + wave8, idx = (u - 24) * 64 + lane;
    if (wave8 >= 5) return;
    setm0(sm + 163840 - 256);
    if (u < 24) {
        const int rbl = (xcd >> 1) * 4 + u / 6, kc = (2 * (rbl + (xcd & 1) * 4) + 1 + 2 * ((u % 6) >> 1)) % 7;
        touch_lds(mkdesc(a.x, 2048 * 896), (rbl * 128 + (u & 1) * 64 + lane) * 896 + kc * 128);
    } else touch_lds(mkdesc(a.wqkv, 448 * 2688), (idx >> 3) * 2688 + (1 + ((idx >> 2) & 1)) * 896 + ((xcd & 1) * 3 + (idx & 3)) * 128);
}

#if defined(__HIP_DEVICE_COMPILE__)
#define NOPK __attribute__((target("no-packed-fp32-ops")))
#else
#define NOPK
#endif
__global__ NOPK __launch_bounds__(512, 2) void fused_kernel(Args a) {
    __shared__ __attribute__((aligned(16))) char smem[163840];
    LAS char* sm = (LAS char*)smem;
    const int xcd = blockIdx.x & 7, j = blockIdx.x >> 3;
    const bool is_attn = (j & 1) == 0;
    if (__builtin_expect(!is_attn, 1)) warm_attn(a, sm, xcd, j >> 1);
    cbar();
    if (__builtin_expect(is_attn, 1)) attn_role(a, sm, xcd, j >> 1);
    else if (j < 28) ssm_role(a, sm, xcd * 14 + (j >> 1));
    else {
        const int t = (j >> 1 & 1) * 512 + threadIdx.x;
        setm0(sm);
#pragma unroll
        for (int i = 0; i < 2; ++i) touch_lds(mkdesc(a.wqkv, 448 * 2688), ((t + 1024 * i) >> 2) * 2688 + ((xcd & 1) * 3 + (t & 3)) * 128);
#pragma unroll
        for (int i = 0; i < 4; ++i) touch_lds(mkdesc(a.wo, 448 * 896), (t + 1024 * i) * 128);
        waitv<0>();
    }
}

extern "C" void kernel_launch(void* const* d_in, const int* in_sizes, int n_in,
                              void* d_out, int out_size, void* d_ws, size_t ws_size,
                              hipStream_t stream) {
    Args a;
    a.u = (const u16*)d_in[0]; a.logA = (const u16*)d_in[1]; a.Aim = (const u16*)d_in[2];
    a.Cre = (const u16*)d_in[3]; a.Cim = (const u16*)d_in[4]; a.logdt = (const u16*)d_in[5];
    a.Dskip = (const u16*)d_in[6]; a.x = (const u16*)d_in[7]; a.wqkv = (const u16*)d_in[8];
    a.wo = (const u16*)d_in[9]; a.out = (u16*)d_out; a.ws = (char*)d_ws;
    fused_kernel<<<256, 512, 0, stream>>>(a);
}
```

```cpp
#include <hip/hip_runtime.h>
#include <stdint.h>

typedef unsigned short u16;
typedef unsigned int u32;
typedef unsigned long long u64;
typedef __bf16 bf16x8 __attribute__((ext_vector_type(8)));
typedef __bf16 bf16x2 __attribute__((ext_vector_type(2)));
typedef _Float16 h2 __attribute__((ext_vector_type(2)));
typedef _Float16 h8 __attribute__((ext_vector_type(8)));
typedef short s16x4 __attribute__((ext_vector_type(4)));
typedef float f32x2 __attribute__((ext_vector_type(2)));
typedef float f32x4 __attribute__((ext_vector_type(4)));
typedef float f32x16 __attribute__((ext_vector_type(16)));
typedef u32 u32x2 __attribute__((ext_vector_type(2)));
typedef u32 u32x4 __attribute__((ext_vector_type(4)));
typedef __amdgpu_buffer_rsrc_t rsrc_t;
#define LAS __attribute__((address_space(3)))
#define GAS __attribute__((address_space(1)))
#define DI __device__ __forceinline__
#define SB() __builtin_amdgcn_sched_barrier(0)
#define KEEP(x) asm volatile("" :: "v"(x))
#define PERM __builtin_amdgcn_perm

constexpr u32 WS_EPA = 0;
constexpr u32 WS_EPS = 256;
constexpr u32 WS_YC  = 65536;
constexpr u32 WS_KVP = 524288;
constexpr u32 WS_OX  = WS_KVP + (1u << 19);
constexpr u32 WS_YX  = WS_OX + (2u << 20);
constexpr u32 WS_END = WS_YX + (2u << 20);
constexpr u32 OOB = 0x80000000u;
constexpr int SPIN_CAP = 1 << 16;

struct Args {
    const u16 *u, *logA, *Aim, *Cre, *Cim, *logdt, *Dskip, *x, *wqkv, *wo;
    u16* out;
    char* ws;
};

DI f32x4 mfma(u32x4 a, u32x4 b, f32x4 c) { return __builtin_amdgcn_mfma_f32_16x16x32_bf16(__builtin_bit_cast(bf16x8, a), __builtin_bit_cast(bf16x8, b), c, 0, 0, 0); }
DI f32x4 mfmah(u32x4 a, u32x4 b, f32x4 c) { return __builtin_amdgcn_mfma_f32_16x16x32_f16(__builtin_bit_cast(h8, a), __builtin_bit_cast(h8, b), c, 0, 0, 0); }
DI f32x16 mfma32(u32x4 a, u32x4 b, f32x16 c) { return __builtin_amdgcn_mfma_f32_32x32x16_bf16(__builtin_bit_cast(bf16x8, a), __builtin_bit_cast(bf16x8, b), c, 0, 0, 0); }
DI u32 pk(float lo, float hi) { f32x2 v = {lo, hi}; return __builtin_bit_cast(u32, __builtin_convertvector(v, bf16x2)); }
DI float bflo(u32 v) { return __uint_as_float(v << 16); }
DI float bfhi(u32 v) { return __uint_as_float(v & 0xffff0000u); }
DI f32x2 unpk(u32 v) { return f32x2{bflo(v), bfhi(v)}; }
DI f32x4 asf(u32x4 v) { return __builtin_bit_cast(f32x4, v); }
DI u32x4 asu(f32x4 v) { return __builtin_bit_cast(u32x4, v); }
DI f32x4 zero4() { return f32x4{0.f, 0.f, 0.f, 0.f}; }
DI f32x16 zero16() { f32x16 z; for (int i = 0; i < 16; ++i) z[i] = 0.f; return z; }
DI u32x4 pk8(const f32x16& a, int s) { return u32x4{pk(a[8 * s], a[8 * s + 1]), pk(a[8 * s + 2], a[8 * s + 3]), pk(a[8 * s + 4], a[8 * s + 5]), pk(a[8 * s + 6], a[8 * s + 7])}; }
DI u32x4 quad(const f32x16& a, int r) { return asu(f32x4{a[4 * r], a[4 * r + 1], a[4 * r + 2], a[4 * r + 3]}); }
template <class T> DI void pin(T& v) { asm volatile("" : "+v"(v) :: "memory"); }
template <int I> struct IC { static constexpr int v = I; };
template <int N, int I = 0, class F> DI void sfor(F f) { if constexpr (I < N) { f(IC<I>{}); sfor<N, I + 1>(f); } }
DI void cbar() { asm volatile("" ::: "memory"); }
DI void raw_barrier() { asm volatile("s_barrier" ::: "memory"); }
DI void wg_barrier() { asm volatile("s_waitcnt lgkmcnt(0)\n\ts_barrier" ::: "memory"); }
template <int N> DI void waitv() { asm volatile("s_waitcnt vmcnt(%0)" :: "i"(N) : "memory"); }
template <int N> DI void waitx(u32x4* x) { asm volatile("s_waitcnt vmcnt(%4)" : "+v"(x[0]), "+v"(x[1]), "+v"(x[2]), "+v"(x[3]) : "i"(N) : "memory"); }

DI u32x4 lds128(LAS char* p) { return *(LAS u32x4*)p; }
DI void sts128(LAS char* p, u32x4 v) { *(LAS u32x4*)p = v; }
DI void sts64(LAS char* p, u32 lo, u32 hi) { u32x2 v = {lo, hi}; *(LAS u32x2*)p = v; }
DI u32x4 trfrag(LAS char* p0, LAS char* p1) {
    u32x2 a = __builtin_bit_cast(u32x2, __builtin_amdgcn_ds_read_tr16_b64_v4i16((LAS s16x4*)p0));
    u32x2 b = __builtin_bit_cast(u32x2, __builtin_amdgcn_ds_read_tr16_b64_v4i16((LAS s16x4*)p1));
    return u32x4{a.x, a.y, b.x, b.y};
}

DI rsrc_t mkrsrc(const void* p, u32 bytes) { return __builtin_amdgcn_make_buffer_rsrc((void*)p, 0, bytes, 0x00020000); }
DI u32x4 mkdesc(const void* p, u32 bytes) { u64 v = (u64)p; return u32x4{(u32)v, (u32)(v >> 32) & 0xffffu, bytes, 0x00020000u}; }
DI u32x4 bld_dev(rsrc_t r, u32 voff, u32 soff) { return __builtin_amdgcn_raw_buffer_load_b128(r, voff, soff, 16); }
DI u32 bld32_dev(rsrc_t r, u32 voff, u32 soff) { return __builtin_amdgcn_raw_buffer_load_b32(r, voff, soff, 16); }
DI void bst_dev(u32x4 v, rsrc_t r, u32 voff) { __builtin_amdgcn_raw_buffer_store_b128(v, r, voff, 0, 16); }
DI void bst32_dev(u32 v, rsrc_t r, u32 voff) { __builtin_amdgcn_raw_buffer_store_b32(v, r, voff, 0, 16); }
DI void st_epoch(char* p, u32 v) { __hip_atomic_store((GAS u32*)p, v, __ATOMIC_RELAXED, __HIP_MEMORY_SCOPE_AGENT); }
DI void setm0(LAS char* dst) { asm volatile("s_mov_b32 m0, %0\n\ts_nop 0" :: "s"((u32)(uintptr_t)dst) : "memory"); }
template <int OFF>
DI void dma(u32x4 desc, u32 voff, u32 soff) { asm volatile("buffer_load_dwordx4 %0, %1, %2 offen offset:%3 lds" :: "v"(voff), "s"(desc), "s"(soff), "i"(OFF) : "memory"); }
DI void ldd(u32x4& v, u32x4 desc, u32 voff, u32 soff) { asm volatile("buffer_load_dwordx4 %0, %1, %2, %3 offen sc1" : "=v"(v) : "v"(voff), "s"(desc), "s"(soff) : "memory"); }
DI u32 ld_epoch(const char* p) { u32 v; asm volatile("global_load_dword %0, %1, %2 sc1" : "=v"(v) : "v"(0u), "s"(p) : "memory"); return v; }
DI u32 touch(u32x4 desc, u32 voff) { u32 v; asm volatile("buffer_load_dword %0, %1, %2, 0 offen" : "=v"(v) : "v"(voff), "s"(desc) : "memory"); return v; }
DI void touch_lds(u32x4 desc, u32 voff) { asm volatile("buffer_load_dword %0, %1, 0 offen lds" :: "v"(voff), "s"(desc) : "memory"); }
template <int BIT> DI void reld(u32x4& v, u32x4 desc, u32 voff, u32 soff, u32 mask) {
    asm volatile("s_bitcmp1_b32 %4, %5\n\ts_cbranch_scc0 1f\n\tbuffer_load_dwordx4 %0, %1, %2, %3 offen sc1\n1:"
                 : "+v"(v) : "v"(voff), "s"(desc), "s"(soff), "s"(mask), "i"(BIT) : "scc", "memory");
}
template <int BIT> DI void reld32(u32& v, u32x4 desc, u32 voff, u32 soff, u32 mask) {
    asm volatile("s_bitcmp1_b32 %4, %5\n\ts_cbranch_scc0 1f\n\tbuffer_load_dword %0, %1, %2, %3 offen sc1\n1:"
                 : "+v"(v) : "v"(voff), "s"(desc), "s"(soff), "s"(mask), "i"(BIT) : "scc", "memory");
}
template <int N, class OK, class LD, class PIN>
DI void settle(u32 bad, OK ok, LD ld, PIN pn) {
#pragma unroll 1
    for (int sp = 0; bad != 0 && sp < SPIN_CAP; ++sp) {
        __builtin_amdgcn_s_sleep(2);
        sfor<N>([&](auto i) { ld(i, bad); });
        waitv<0>();
        sfor<N>([&](auto i) { pn(i.v); });
        sfor<N>([&](auto i) { if ((bad >> i.v & 1) && __all(ok(i.v))) bad &= ~(1u << i.v); });
    }
}
template <int N, class OK> DI u32 badmask(OK ok) { u32 bad = 0; sfor<N>([&](auto i) { if (!__all(ok(i.v))) bad |= 1u << i.v; }); return bad; }

DI u32 sum4(u32x4 v) { return v.x + v.y + v.z + v.w; }
DI u32 rowsum(u32 s) {
    s += __builtin_amdgcn_update_dpp(0, s, 0x128, 0xf, 0xf, false);
    s += __builtin_amdgcn_update_dpp(0, s, 0x124, 0xf, 0xf, false);
    s += __builtin_amdgcn_update_dpp(0, s, 0x122, 0xf, 0xf, false);
    s += __builtin_amdgcn_update_dpp(0, s, 0x121, 0xf, 0xf, false);
    return s;
}
DI u32 sum32(u32 s) { s = rowsum(s); auto t = __builtin_amdgcn_permlane16_swap(s, s, false, false); return t[0] + t[1]; }
DI u32 halfsum(u32 s) { auto r = __builtin_amdgcn_permlane32_swap(s, s, false, false); return r[0] + r[1]; }
DI u32 wavemax(u32 s) {
    s = max(s, (u32)__builtin_amdgcn_update_dpp(0, s, 0x128, 0xf, 0xf, false));
    s = max(s, (u32)__builtin_amdgcn_update_dpp(0, s, 0x124, 0xf, 0xf, false));
    s = max(s, (u32)__builtin_amdgcn_update_dpp(0, s, 0x122, 0xf, 0xf, false));
    s = max(s, (u32)__builtin_amdgcn_update_dpp(0, s, 0x121, 0xf, 0xf, false));
    auto r = __builtin_amdgcn_permlane32_swap(s, s, false, false);
    s = max(r[0], r[1]);
    auto t = __builtin_amdgcn_permlane16_swap(s, s, false, false);
    return max(t[0], t[1]);
}

DI float phi(float t) {
    const float e = __builtin_amdgcn_exp2f(t * 1.44269504089f);
    return __int_as_float(max(__float_as_int(t), 0x358637bd)) + __builtin_amdgcn_fmed3f(e, 0.f, 1.f);
}

constexpr int A_RED = 0, A_WO = 32768, A_WQ = 98304, A_KVB = 155648;
constexpr int PRO = 8;
constexpr int LA = 6;
constexpr int QS0 = 40;
constexpr int GK0 = 14;
constexpr int G2A = 2;
constexpr int issued_before(int gs) {
    constexpr int B0 = 16 - LA;
    int n = PRO + gs;
    int cap = 16 + (gs < B0 ? 0 : 4 * ((gs - B0) / 8) + ((gs - B0) % 8 < 4 ? (gs - B0) % 8 : 4));
    if (gs > B0 + 20) cap = 29;
    n = n < cap ? n : cap;
    if (n < 29) return n;
    const int q = gs - QS0;
    return 29 + (q < 0 ? 0 : q > 7 ? 7 : q);
}
static_assert(QS0 >= 40 - LA, "Wq overwrites ring slot 3");
constexpr int slot_ch(int gs) { return gs < 40 ? gs >> 3 : 5 + ((gs >> 2) & 1); }
constexpr int slot_kk(int gs) { return gs < 40 ? (gs & 7) >> 1 : gs & 3; }
constexpr int slot_sg(int gs) { return gs < 40 ? gs & 1 : gs >= 48; }

DI void attn_role(const Args& a, LAS char* sm, const int xcd, const int j) {
    const int tid = threadIdx.x, lane = tid & 63, wave = __builtin_amdgcn_readfirstlane(tid >> 6);
    const int rw4 = wave & 3, hs = wave >> 2;
    const int hp = (xcd & 1) * 4 + (j & 3), rb = (xcd >> 1) * 4 + (j >> 2);
    const int h = 2 * hp + hs;
    const int c0 = (2 * rb + 2 * hp) % 7;
    const int r32 = lane & 31, hh = lane >> 5, blk = (lane >> 4) & 1, q = (lane >> 2) & 3, p = lane & 3;
    const u32x4 rx = mkdesc(a.x, 2048 * 896), rw = mkdesc(a.wqkv, 448 * 2688), rwo = mkdesc(a.wo, 448 * 896), dws = mkdesc(a.ws, WS_END);
    const rsrc_t rws = mkrsrc(a.ws, WS_END);
    u32 tag = ld_epoch(a.ws + WS_EPA), tch = 0;
    const int widx = (lane & 15) ^ ((lane >> 4) << 2);
    const u32 wv = (8 * wave + (lane >> 4)) * 2688 + (1 + (widx >> 3)) * 896 + hp * 112 + min(widx & 7, 6) * 16;
    const u32 xv0 = (rb * 128 + wave * 16 + (lane >> 3)) * 896 + (((lane & 7) ^ (lane >> 4)) * 16);
    const u32 xv1 = (rb * 128 + wave * 16 + 8 + (lane >> 3)) * 896 + (((lane & 7) ^ (4 + (lane >> 4))) * 16) - 1024;
    const u32 qv = (8 * wave + (lane >> 3)) * 2688 + hp * 112 + min((lane & 7) ^ (blk << 2), 6) * 16;
    auto kchunk = [&](int i) { int kc = i + c0; return kc >= 7 ? kc - 7 : kc; };
    auto ldn = [&](int n) {
        if (n >= 29) { setm0(sm + A_WQ + (n - 29) * 8192 + wave * 1024); dma<0>(rw, qv, kchunk(n - 29) * 64 * 2688); return; }
        if (n == 28) {
            tch = touch(dws, lane < 8 ? WS_KVP + (h * 16 + rb) * 2048 + rw4 * 512 + (lane & 3) * 128
                           : lane < 24 ? WS_OX + ((rb * 4 + rw4) * 16 + h) * 2048 + (lane - 8) * 128 : OOB);
            return;
        }
        const int i = n >> 2, m = n & 3;
        if (m == 0) { setm0(sm + (i & 3) * 32768 + 16384 + wave * 2048); dma<0>(rx, xv0, kchunk(i) * 128); }
        if (m == 1) dma<1024>(rx, xv1, kchunk(i) * 128);
        if (m == 2) { setm0(sm + (i & 3) * 32768 + wave * 2048); dma<0>(rw, wv, kchunk(i) * 64 * 2688); }
        if (m == 3) dma<1024>(rw, wv, kchunk(i) * 64 * 2688 + 4 * 2688 - 1024);
    };
#pragma unroll
    for (int n = 0; n < PRO; ++n) ldn(n);

    const int wb = 56 * hs + 32 * blk + 8 * p;
    LAS char *wsrc[2], *xsrc[4];
#pragma unroll
    for (int sg = 0; sg < 2; ++sg) wsrc[sg] = sm + (32 * hh + q) * 256 + (((2 * sg + (wb >> 6)) ^ q) << 6) + (wb & 63);
#pragma unroll
    for (int kk = 0; kk < 4; ++kk) xsrc[kk] = sm + 16384 + (32 * rw4 + r32) * 128 + (((4 * hh + kk) ^ ((r32 >> 1) & 7)) * 16);

    const bool p3 = j < 14;
    const int r128 = p3 ? xcd * 2 + j / 7 : 0, nb = j % 7;
    f32x16 acc[2] = {zero16(), zero16()};
    u32x4 xf[28], wf[56];
    auto rdw = [&](int gs) { LAS char* s = wsrc[slot_sg(gs)] + (slot_ch(gs) & 3) * 32768 + slot_kk(gs) * 2048; wf[gs] = trfrag(s, s + 1024); };
    auto rdx = [&](int i, int kk) { xf[4 * i + kk] = lds128(xsrc[kk] + (i & 3) * 32768); };
    waitv<PRO - 4>();
    wg_barrier();
#pragma unroll
    for (int sl = 0; sl < LA; ++sl) { if (sl < 4) rdx(0, sl); rdw(sl); }
    u32 pkd[8];
    u32 vm = r32 == 28 ? 0u : ~0u, vo = r32 == 28 ? 0x3f803f80u : 0u;
    pin(vm); pin(vo);
    sfor<56>([&](auto gs) {
        constexpr int issued = issued_before(gs.v);
        constexpr int fu = gs.v + LA, c = fu == 44 ? 6 : (fu <= 40 && (fu & 7) == 0) ? fu >> 3 : 0;
        static_assert(!c || issued >= 4 * c + 4, "chunk not issued yet");
        if constexpr (c != 0) { waitv<issued - (4 * c + 4)>(); raw_barrier(); }
        SB();
        acc[slot_sg(gs.v)] = mfma32(xf[4 * slot_ch(gs.v) + slot_kk(gs.v)], wf[gs.v], acc[slot_sg(gs.v)]);
        SB();
        if constexpr (fu < 40 && fu >= 8 && (fu & 1) == 0) rdx(fu >> 3, (fu & 7) >> 1);
        if constexpr (fu >= 40 && fu < 48) rdx(slot_ch(fu), fu & 3);
        if constexpr (fu < 56) rdw(fu);
        if constexpr (gs.v == 44) raw_barrier();
        if constexpr (gs.v >= 49) { pkd[gs.v - 49] = pk(phi(acc[0][2 * (gs.v - 49)]), phi(acc[0][2 * (gs.v - 49) + 1])); pin(pkd[gs.v - 49]); }
#pragma unroll
        for (int n = issued; n < issued_before(gs.v + 1); ++n) ldn(n);
        SB();
    });
    {
        pkd[7] = pk(phi(acc[0][14]), phi(acc[0][15]));
        pin(pkd[7]);
        f32x16 kv = mfma32(u32x4{pkd[0], pkd[1], pkd[2], pkd[3]}, (pk8(acc[1], 0) & vm) | vo, zero16());
        SB();
        kv = mfma32(u32x4{pkd[4], pkd[5], pkd[6], pkd[7]}, (pk8(acc[1], 1) & vm) | vo, kv);
#pragma unroll
        for (int rq = 0; rq < 4; ++rq) sts128(sm + A_RED + wave * 4096 + rq * 1024 + lane * 16, quad(kv, rq));
    }
    asm volatile("s_waitcnt vmcnt(8)" : "+v"(tag) :: "memory");
    tag += 1;
    wg_barrier();
    u32 hm = hh ? 0u : ~0u, cm = r32 == 31 ? 0u : ~0u;
    pin(hm); pin(cm);
    const bool red = (rw4 >> 1) == hs;
    if (red) {
        LAS char* rs = sm + A_RED + hs * 16384 + (rw4 & 1) * 2048 + lane * 16;
        f32x4 lo = (asf(lds128(rs)) + asf(lds128(rs + 4096))) + (asf(lds128(rs + 8192)) + asf(lds128(rs + 12288)));
        f32x4 hi = (asf(lds128(rs + 1024)) + asf(lds128(rs + 5120))) + (asf(lds128(rs + 9216)) + asf(lds128(rs + 13312)));
        u32x4 v = {pk(lo.x, lo.y), pk(lo.z, lo.w), pk(hi.x, hi.y), pk(hi.z, hi.w) & cm};
        v.w |= (tag - sum32(sum4(v))) & ~cm;
        bst_dev(v, rws, WS_KVP + (h * 16 + rb) * 2048 + (tid & 127) * 16);
    }
    cbar();
    if (red) waitv<1>(); else waitv<0>();
    KEEP(tch);
    raw_barrier();
    const u32 ov = (8 * wave + (lane >> 3)) * 896 + nb * 128 + (((lane & 7) ^ (blk << 2)) * 16);
    u32x4 gk[8];
    const u32 kbase = WS_KVP + (h * 16 + (rw4 >> 1) * 8 + (rb & 7)) * 2048, kvo = (tid & 127) * 16;
    f32x16 aq = zero16();
    {
        LAS char* qsrc = sm + A_WQ + (32 * hh + q) * 128 + (((wb >> 6) ^ (q >> 1)) << 6) + (wb & 63);
        u32x4 wq[28];
        auto rdq = [&](int k) { LAS char* s = qsrc + (k >> 2) * 8192 + (k & 3) * 1024; wq[k] = trfrag(s, s + 512); };
#pragma unroll
        for (int k = 0; k < 4; ++k) rdq(k);
        SB();
#pragma unroll
        for (int k = 0; k < 28; ++k) {
            aq = mfma32(wq[k], xf[k], aq);
            SB();
            if (k + 4 < 28) rdq(k + 4);
            if (k < 7) { setm0(sm + A_WO + k * 8192 + wave * 1024); dma<0>(rwo, ov, k * 64 * 896); }
            if (k >= GK0 && k < GK0 + 8) ldd(gk[k - GK0], dws, kvo, kbase ^ ((k - GK0) * 2048));
            SB();
        }
    }
    u32 pqd[8];
#pragma unroll
    for (int k = 0; k < 8; ++k) {
        pqd[k] = pk(phi(aq[2 * k]), phi(aq[2 * k + 1]));
        if (k >= 6) pqd[k] &= hm;
        pin(pqd[k]);
    }
    {
        waitx<0>(gk); waitx<0>(gk + 4);
        u32 t = 0;
#pragma unroll
        for (int i = 0; i < 8; ++i) t += sum4(gk[i]);
        if (!__all(sum32(t) == 8u * tag)) {
            auto ok = [&](int i) { return sum32(sum4(gk[i])) == tag; };
            settle<8>(badmask<8>(ok), ok, [&](auto i, u32 bm) { reld<i.v>(gk[i.v], dws, kvo, kbase ^ (i.v * 2048), bm); }, [&](int i) { pin(gk[i]); });
        }
    }
    {
        u32x4 v;
#pragma unroll
        for (int c = 0; c < 4; ++c) {
            const float lo = ((bflo(gk[0][c]) + bflo(gk[1][c])) + (bflo(gk[2][c]) + bflo(gk[3][c]))) + ((bflo(gk[4][c]) + bflo(gk[5][c])) + (bflo(gk[6][c]) + bflo(gk[7][c])));
            const float hi = ((bfhi(gk[0][c]) + bfhi(gk[1][c])) + (bfhi(gk[2][c]) + bfhi(gk[3][c]))) + ((bfhi(gk[4][c]) + bfhi(gk[5][c])) + (bfhi(gk[6][c]) + bfhi(gk[7][c])));
            v[c] = pk(lo, hi);
        }
        sts128(sm + A_KVB + tid * 16, v);
    }
    wg_barrier();
    {
        u32x4 kf[4];
#pragma unroll
        for (int i = 0; i < 4; ++i) kf[i] = lds128(sm + A_KVB + hs * 4096 + i * 1024 + lane * 16);
        const u32x4 pq0 = {pqd[0], pqd[1], pqd[2], pqd[3]}, pq1 = {pqd[4], pqd[5], pqd[6], pqd[7]};
        f32x16 oa = mfma32(kf[3], pq1, mfma32(kf[2], pq0, mfma32(kf[1], pq1, mfma32(kf[0], pq0, zero16()))));
        const u32 dn = __float_as_uint(oa[12]);
        auto r = __builtin_amdgcn_permlane32_swap(dn, dn, false, false);
        oa *= __builtin_amdgcn_rcpf(__uint_as_float(r[1]) + 1e-6f);
        const u32x4 f0 = pk8(oa, 0);
        const u32 o = WS_OX + ((rb * 4 + rw4) * 16 + h) * 2048 + lane * 16;
        bst_dev(f0, rws, o);
        SB();
        u32x4 f1 = pk8(oa, 1);
        f1.z &= hm; f1.w &= hm;
        f1.w |= (tag - halfsum(sum4(f0) + sum4(f1))) & ~hm;
        bst_dev(f1, rws, o + 1024);
    }
    cbar();
    if (p3) {
        LAS char* osrc[2];
#pragma unroll
        for (int nt = 0; nt < 2; ++nt) osrc[nt] = sm + A_WO + (224 * hs + 4 * hh + q) * 128 + ((nt ^ (q >> 1)) * 64) + blk * 32 + 8 * p;
        const int padoff = hh ? 0 : 1024;
        u32x4 wf3[8][2][2];
#pragma unroll
        for (int k = 0; k < 32; ++k) {
            const int hl = k >> 2, ks = (k >> 1) & 1, nt = k & 1;
            LAS char* s = osrc[nt] + (hl * 28 + 16 * ks) * 128;
            wf3[hl][ks][nt] = trfrag(s, s + (ks ? padoff : 1024));
        }
        SB();
        waitv<0>();
        u32x4 go[16];
        const u32 ob0 = WS_OX + ((r128 * 4 + rw4) * 16 + 8 * hs) * 2048;
        auto ldo = [&](int i) { ldd(go[i], dws, lane * 16, ob0 + i * 1024); };
#pragma unroll
        for (int i = 0; i < 4 * G2A; ++i) ldo(i);
        f32x16 ac[2];
        sfor<4>([&](auto gi) {
            constexpr int i = 4 * gi.v, nis = gi.v + G2A < 4 ? gi.v + G2A : 4;
            waitx<4 * (nis - gi.v - 1)>(go + i);
            {
                auto r0 = __builtin_amdgcn_permlane32_swap(sum4(go[i]) + sum4(go[i + 1]), sum4(go[i + 2]) + sum4(go[i + 3]), false, false);
                const u64 ne = __ballot(r0[0] + r0[1] != tag);
                if (ne)
                    settle<2>(((u32)ne != 0) | ((ne >> 32) != 0) << 1, [&](int k) { return halfsum(sum4(go[i + 2 * k]) + sum4(go[i + 2 * k + 1])) == tag; },
                              [&](auto k, u32 bm) { reld<k.v>(go[i + 2 * k.v], dws, lane * 16, ob0 + (i + 2 * k.v) * 1024, bm);
                                                    reld<k.v>(go[i + 2 * k.v + 1], dws, lane * 16, ob0 + (i + 2 * k.v + 1) * 1024, bm); },
                              [&](int k) { pin(go[i + 2 * k]); pin(go[i + 2 * k + 1]); });
            }
            go[i + 1].w &= hm; go[i + 3].w &= hm;
            SB();
#pragma unroll
            for (int k = 0; k < 8; ++k) {
                const int hl = 2 * gi.v + (k >> 2), ks = (k >> 1) & 1, nt = k & 1;
                ac[nt] = mfma32(wf3[hl][ks][nt], go[2 * hl + ks], (gi.v == 0 && k < 2) ? zero16() : ac[nt]);
                SB();
                if ((k & 1) == 0 && gi.v + G2A < 4) ldo(4 * (gi.v + G2A) + (k >> 1));
                SB();
            }
        });
        LAS char* xb = sm + wave * 4096 + lane * 16;
        if (hs) {
#pragma unroll
            for (int rq = 0; rq < 4; ++rq) sts128(xb + rq * 1024, quad(ac[0], rq));
            ac[0] = ac[1];
        } else {
#pragma unroll
            for (int rq = 0; rq < 4; ++rq) sts128(xb + rq * 1024, quad(ac[1], rq));
        }
        wg_barrier();
        LAS char* pb = sm + (wave ^ 4) * 4096 + lane * 16;
        LAS char* ob = sm + A_WQ + rw4 * 4608;
#pragma unroll
        for (int rq = 0; rq < 4; ++rq) {
            const f32x4 v = asf(quad(ac[0], rq)) + asf(lds128(pb + rq * 1024));
            sts64(ob + r32 * 144 + hs * 64 + rq * 16 + hh * 8, pk(v.x, v.y), pk(v.z, v.w));
        }
        wg_barrier();
        const rsrc_t ro = mkrsrc(a.out, 2048 * 1792);
        const int orow = 16 * hs + (lane >> 3);
        const u32 oo = (r128 * 128 + rw4 * 32 + orow) * 1792 + 896 + nb * 128 + (lane & 7) * 16;
        const u32x4 v0 = lds128(ob + orow * 144 + (lane & 7) * 16), v1 = lds128(ob + (8 + orow) * 144 + (lane & 7) * 16);
        bst_dev(v0, ro, oo);
        bst_dev(v1, ro, oo + 8 * 1792);
        if (xcd == 0 && j == 0 && tid == 0) st_epoch(a.ws + WS_EPA, tag);
    }
}

constexpr int S_TAB = 0;
constexpr int S_KC = 34816;
constexpr int S_P = 47104;
constexpr int S_UT = 52480;
constexpr int S_TR = 61696;

struct c2 { f32x2 r, i; };
struct hc { h2 r, i; };
DI c2 csq(c2 a) { return c2{a.r * a.r - a.i * a.i, 2.f * a.r * a.i}; }
DI hc hmul(hc a, hc b) { return hc{a.r * b.r - a.i * b.i, a.r * b.i + a.i * b.r}; }
DI hc toh(c2 a) { return hc{__builtin_convertvector(a.r, h2), __builtin_convertvector(a.i, h2)}; }
DI c2 cexp2(f32x2 lr, f32x2 rev, float pw) {
    f32x2 e = lr * pw, f = rev * pw;
    f32x2 m = {__builtin_amdgcn_exp2f(e.x), __builtin_amdgcn_exp2f(e.y)};
    f.x = __builtin_amdgcn_fractf(f.x); f.y = __builtin_amdgcn_fractf(f.y);
    f32x2 c = {__builtin_amdgcn_cosf(f.x), __builtin_amdgcn_cosf(f.y)}, s = {__builtin_amdgcn_sinf(f.x), __builtin_amdgcn_sinf(f.y)};
    return c2{m * c, m * s};
}
constexpr int tri_a(int sl) { int a = 0; while (sl >= 8 - a) { sl -= 8 - a; ++a; } return a; }
constexpr int tri_b(int sl) { int a = 0; while (sl >= 8 - a) { sl -= 8 - a; ++a; } return sl; }

DI void ssm_role(const Args& a, LAS char* sm0, const int sb) {
    const int tid = threadIdx.x & 255, lane = tid & 63, wave8 = __builtin_amdgcn_readfirstlane(threadIdx.x >> 6), wave = wave8 & 3;
    LAS char* sm = sm0 + (wave8 >> 2) * 81920;
    const int ch = wave >> 1, hf = wave & 1, d = 4 * sb + (wave8 >> 1);
    const int g = lane >> 4, r16 = lane & 15;
    const u32 po = (d * 128 + hf * 64 + lane) * 4;
    const u32 ldt = a.logdt[d];
    const u32 pA = *(const u32*)((const char*)a.logA + po), pI = *(const u32*)((const char*)a.Aim + po);
    const u32 pCr = *(const u32*)((const char*)a.Cre + po), pCi = *(const u32*)((const char*)a.Cim + po);
    const u32x2 dsk = ((const u32x2*)a.Dskip)[sb];
    cbar();
    const int G8 = (wave8 + sb) & 7, trow = 256 * G8 + 16 * r16 + 4 * g;
    const rsrc_t ru = mkrsrc(a.u, 2048 * 896);
    u32x2 uu[4];
    auto ldu = [&](int i) { SB(); uu[i] = __builtin_amdgcn_raw_buffer_load_b64(ru, trow * 896 + sb * 8 + i * 896, 0, 0); cbar(); SB(); };
    const int sidx = 2 * sb + (wave8 >> 2);
    const int ct = sidx >> 5, j = sidx & 31;
    u32 tag = ld_epoch(a.ws + WS_EPS + ct * 256);
    ldu(0);
    *(LAS u32*)(sm + S_UT + (tid >> 7) * 4608 + (tid & 127) * 4) = 0;
    const int w = 16 + r16 - 8 * g;
    u32x4 bE, bO, mkb, mkd;
    u32 sel[3][4], msk[4], pb[3];
    {
        u32 y[4];
#pragma unroll
        for (int e = 0; e < 4; ++e) {
            y[e] = (u32)(w + 64 - 2 * e) * 0x10001u - 0x10000u;
            msk[e] = hf ? 0u : (((y[e] >> 6) & 0x10001u) ^ 0x10001u) * 0xffffu;
        }
        pb[0] = w - 7;
#pragma unroll
        for (int s = 1; s < 3; ++s) {
            u32 r[4];
#pragma unroll
            for (int e = (s == 1 ? 2 : 0); e < 4; ++e)
                r[e] = ((y[e] + ((1u << (s - 1)) - 1u) * 0x10001u + ((y[e] >> s) & 0x10001u)) >> s) & 0x00ff00ffu;
            const u32 base = r[3] >> 16;
            pb[s] = base - (64 >> s);
#pragma unroll
            for (int e = (s == 1 ? 2 : 0); e < 4; ++e) sel[s][e] = (((r[e] - base * 0x10001u) ^ 0x10001u) & 0xfffffu) * 0x202u + 0x01000100u;
        }
#pragma unroll
        for (int e = 0; e < 4; ++e) {
            const int z = min(max(16 - w + 2 * e, 0), 20);
            auto two = [&](u32 m) { const u32 b2 = (m >> z) & 3u; return (b2 | (b2 << 15)) & 0x10001u; };
            bE[e] = two(0x1ff0u) * 0x3f80u; bO[e] = two(0x0fe0u) * 0x3f80u; mkb[e] = two(0x1f000u) * 0xffffu;
            mkd[e] = r16 - 4 * g - e >= 0 ? ~0u : 0u;
        }
        asm volatile("" : "+v"(sel[1][2]), "+v"(sel[1][3]), "+v"(sel[2][0]), "+v"(sel[2][1]), "+v"(sel[2][2]), "+v"(sel[2][3]), "+v"(bE), "+v"(bO), "+v"(mkb), "+v"(mkd));
        asm volatile("" : "+v"(msk[0]), "+v"(msk[1]), "+v"(msk[2]), "+v"(msk[3]));
    }
    LAS u32* const R = (LAS u32*)(sm + S_P + ch * 2688) + 655;
    LAS u32 *P0 = R - (128 * hf + (int)pb[0]) - 102, *P1 = R - (256 + 64 * hf + (int)pb[1]) - 132, *P2 = R - (384 + 32 * hf + (int)pb[2]) - 130,
            *P3 = R - (385 + 2 * (r16 - 4 * g)) - 224;
    LAS char* ubase = sm + S_UT + ch * 4608 + 480 + r16 * 32 + g * 16 - 256 * hf - 192;
    asm volatile("" : "+v"(P0), "+v"(P1), "+v"(P2), "+v"(P3), "+v"(ubase));
    LAS char* tab = sm + S_TAB + wave * 8704;
    LAS char* tw = tab + lane * 8;
    LAS char* tr = tab + r16 * 544 + g * 16;
    LAS char* kc = sm + S_KC + wave * 3072;
    SB();
#define EMIT1(row, v) sts64(tw + (row) * 544, __builtin_bit_cast(u32, (v).r), __builtin_bit_cast(u32, (v).i))
#define EMIT2(row, v0, v1) do { SB(); EMIT1(row, v0); EMIT1((row) + 1, v1); SB(); } while (0)
#define KRD() do { SB(); _Pragma("unroll") for (int ks = 0; ks < 8; ++ks) gf[ks] = lds128(tr + ks * 64); cbar(); SB(); } while (0)
#define KM2(k) do { ka = mfmah(gf[2 * (k)], ff[2 * (k)], (k) ? ka : zero4()); ka = mfmah(gf[2 * (k) + 1], ff[2 * (k) + 1], ka); } while (0)
    const float L2E = 1.44269504089f;
    const float edt = __builtin_amdgcn_exp2f(bflo(ldt) * L2E);
    const float dt = bflo(pk(edt, edt));
    f32x2 lr = unpk(pA) * L2E;
    lr = unpk(pk(__builtin_amdgcn_exp2f(lr.x), __builtin_amdgcn_exp2f(lr.y))) * dt * -L2E;
    const f32x2 rev = unpk(pI) * dt * 0.15915494309f;
    hc W[8], F[16], G[17], H, t0, t1;
    u32x4 ff[8], gf[8];
    f32x4 ka;
    c2 e = cexp2(lr, -rev, 8.f);
    F[1] = toh(e); e = csq(e); F[2] = toh(e);
    ldu(1);
    F[0] = hc{h2{1.f16, 1.f16}, h2{0.f16, 0.f16}};
    EMIT2(0, F[0], F[1]);
    e = csq(e); F[4] = toh(e); F[3] = hmul(F[1], F[2]);
    EMIT2(2, F[2], F[3]);
    e = csq(e); F[8] = toh(e); F[5] = hmul(F[4], F[1]);
    EMIT2(4, F[4], F[5]);
    F[6] = hmul(F[4], F[2]); F[7] = hmul(F[4], F[3]); F[9] = hmul(F[8], F[1]);
    EMIT2(6, F[6], F[7]);
    ldu(2);
    EMIT2(8, F[8], F[9]);
    F[10] = hmul(F[8], F[2]); F[11] = hmul(F[8], F[3]);
    e = cexp2(lr, rev, 1.f); W[1] = toh(e);
    EMIT2(10, F[10], F[11]);
    F[12] = hmul(F[8], F[4]); F[13] = hmul(F[8], F[5]);
    e = csq(e); W[2] = toh(e);
    EMIT2(12, F[12], F[13]);
    F[14] = hmul(F[8], F[6]); F[15] = hmul(F[8], F[7]);
    e = csq(e); W[4] = toh(e);
    EMIT2(14, F[14], F[15]);
    ldu(3);
    u32 ce = max(max(pCr & 0x7fffu, (pCr >> 16) & 0x7fffu), max(pCi & 0x7fffu, (pCi >> 16) & 0x7fffu));
    ce = min(max(wavemax(ce) >> 7, 1u), 253u);
    const float cdn = __uint_as_float((254u - ce) << 23), cup = __uint_as_float(ce << 23) * (2.f * dt);
    G[0] = toh(c2{unpk(pCr) * cdn, unpk(pCi) * cdn});
    t1 = hmul(G[0], W[1]);
    SB();
#pragma unroll
    for (int ks = 0; ks < 8; ++ks) ff[ks] = lds128(tr + ks * 64);
    cbar();
    EMIT2(0, G[0], t1);
    W[3] = hmul(W[1], W[2]); t0 = hmul(G[0], W[2]); t1 = hmul(G[0], W[3]);
    EMIT2(2, t0, t1);
    W[5] = hmul(W[4], W[1]); t0 = hmul(G[0], W[4]); t1 = hmul(G[0], W[5]);
    EMIT2(4, t0, t1);
    W[6] = hmul(W[4], W[2]); W[7] = hmul(W[4], W[3]); t0 = hmul(G[0], W[6]); t1 = hmul(G[0], W[7]);
    EMIT2(6, t0, t1);
    e = cexp2(lr, rev, 128.f); H = toh(e);
    G[1] = hmul(G[0], H); t1 = hmul(G[1], W[1]);
    EMIT2(8, G[1], t1);
    t0 = hmul(G[1], W[2]); t1 = hmul(G[1], W[3]);
    e = csq(e); H = toh(e);
    EMIT2(10, t0, t1);
    t0 = hmul(G[1], W[4]); t1 = hmul(G[1], W[5]); G[2] = hmul(G[0], H);
    EMIT2(12, t0, t1);
    t0 = hmul(G[1], W[6]); t1 = hmul(G[1], W[7]); G[3] = hmul(G[1], H);
    EMIT2(14, t0, t1);
    t1 = hmul(G[2], W[2]);
    e = csq(e); H = toh(e);
    KRD();
    EMIT2(0, G[2], t1);
    t0 = hmul(G[2], W[4]); t1 = hmul(G[2], W[6]); G[4] = hmul(G[0], H);
    EMIT2(2, t0, t1);
    t1 = hmul(G[3], W[2]); G[5] = hmul(G[1], H); KM2(0);
    EMIT2(4, G[3], t1);
    t0 = hmul(G[3], W[4]); t1 = hmul(G[3], W[6]); KM2(1);
    EMIT2(6, t0, t1);
    t1 = hmul(G[4], W[4]); G[6] = hmul(G[2], H); KM2(2);
    EMIT2(8, G[4], t1);
    t1 = hmul(G[5], W[4]); G[7] = hmul(G[3], H); KM2(3);
    EMIT2(10, G[5], t1);
    t1 = hmul(G[6], W[4]);
    e = csq(e); H = toh(e);
    EMIT2(12, G[6], t1);
    t1 = hmul(G[7], W[4]); G[8] = hmul(G[0], H);
    sts128(kc + (128 * (g >> 1) + 4 * (g & 1) + 8 * r16) * 4, asu(ka * cup));
    EMIT2(14, G[7], t1);
    G[9] = hmul(G[1], H); G[10] = hmul(G[2], H);
    KRD();
    EMIT2(0, G[8], G[9]);
    G[11] = hmul(G[3], H); G[12] = hmul(G[4], H); G[13] = hmul(G[5], H);
    EMIT2(2, G[10], G[11]);
    G[14] = hmul(G[6], H); KM2(0); KM2(1);
    EMIT2(4, G[12], G[13]);
    G[15] = hmul(G[7], H); G[16] = hmul(G[8], H); KM2(2); KM2(3);
    EMIT2(6, G[14], G[15]);
    EMIT1(8, G[16]);
    KRD();
    {
        LAS char* k0 = kc + (g < 2 ? 256 + 64 * g + 4 * r16 : 384 + 64 * (g - 2) + 2 * r16) * 4;
        const u32x4 kb = asu(ka * cup);
        sts64(k0, kb.x, kb.y);
        sts64(k0 + (g < 2 ? 8 : 128), kb.z, kb.w);
    }
    SB();
    KM2(0); KM2(1); KM2(2); KM2(3);
    {
        LAS char* ud = sm0 + S_UT + 512 + trow * 2;
        sts64(ud, PERM(uu[1].x, uu[0].x, 0x05040100u), PERM(uu[3].x, uu[2].x, 0x05040100u));
        sts64(ud + 4608, PERM(uu[1].x, uu[0].x, 0x07060302u), PERM(uu[3].x, uu[2].x, 0x07060302u));
        sts64(ud + 81920, PERM(uu[1].y, uu[0].y, 0x05040100u), PERM(uu[3].y, uu[2].y, 0x05040100u));
        sts64(ud + 81920 + 4608, PERM(uu[1].y, uu[0].y, 0x07060302u), PERM(uu[3].y, uu[2].y, 0x07060302u));
    }
    {
        LAS float* k0 = (LAS float*)(kc + (512 + 64 * g + r16) * 4);
        k0[0] = ka.x * cup; k0[16] = ka.y * cup; k0[32] = ka.z * cup; k0[48] = ka.w * cup;
    }
    wg_barrier();
    const rsrc_t rws = mkrsrc(a.ws, WS_END);
    const u32x4 dws = mkdesc(a.ws, WS_END);
    u32 tch = touch(dws, lane < 16 ? WS_YX + (2 * sb + (lane >> 3)) * 8192 + G8 * 1024 + (lane & 7) * 128 : lane == 16 ? WS_YC + (sb * 8 + G8) * 128 : OOB);
    u32x4 U[2][8];
    auto rdU = [&](int it, int bq) { U[it & 1][bq] = lds128(ubase + 512 * bq + 192 - 64 * it); };
    {
        LAS char* k0 = sm + S_KC + ch * 6144;
        LAS char* Rb = sm + S_P + ch * 2688;
        const int t = tid & 127, e1 = min(t + 128, 167);
        f32x4 a0 = asf(lds128(k0 + t * 16)), b0 = asf(lds128(k0 + 3072 + t * 16)), a1 = asf(lds128(k0 + e1 * 16)), b1 = asf(lds128(k0 + 3072 + e1 * 16));
        const float n0 = *(LAS float*)(k0 + t * 16 + 16) + *(LAS float*)(k0 + 3072 + t * 16 + 16);
        const float n1 = *(LAS float*)(k0 + e1 * 16 + 16) + *(LAS float*)(k0 + 3072 + e1 * 16 + 16);
#pragma unroll
        for (int bq = 0; bq < 8; ++bq) rdU(3, bq);
        a0 += b0; a1 += b1;
        float p0 = a0.x, p1 = a0.y, p2 = a0.z, p3 = a0.w, q0 = a1.x, q1 = a1.y, q2 = a1.z, q3 = a1.w;
        asm volatile("" : "+v"(p0), "+v"(p1), "+v"(p2), "+v"(p3), "+v"(q0), "+v"(q1), "+v"(q2), "+v"(q3));
        sts128(Rb + (163 - t) * 16, u32x4{pk(n0, p3), pk(p3, p2), pk(p2, p1), pk(p1, p0)});
        u32x4 v1 = {pk(n1, q3), pk(q3, q2), pk(q2, q1), pk(q1, q0)};
        if (t >= 36) v1 = u32x4{0, 0, 0, 0};
        if (t < 39) sts128(Rb + (t < 36 ? 35 - t : 129 + t) * 16, v1);
        if (t == 0) sts128(Rb + 164 * 16, u32x4{pk(p0, 0.f), 0, 0, 0});
    }
    wg_barrier();
    f32x4 y[8];
    {
#define Q0(i) P0[102 - (i)]
#define Q1(i) P1[132 - (i)]
#define Q2(i) P2[130 - (i)]
#define Q3(i) P3[224 - (i)]
        u32x4 T[2][4], T32, TB[4], UW[4], WA[2];
        f32x4 wd[4];
        u32 raw[7], rx[10];
        auto rdT = [&](int it, int k) {
            u32x4& f0 = T[it & 1][0];
            if (k == 0) { f0.x = Q0(32 * it + 6); f0.y = Q0(32 * it + 4); }
            if (k == 1) { f0.z = Q0(32 * it + 2); f0.w = Q0(32 * it); }
            if (k == 2) { raw[0] = Q1(16 * it); raw[1] = Q1(16 * it + 2); raw[2] = Q1(16 * it + 4); }
            if (k == 3) { raw[3] = Q2(8 * it); raw[4] = Q2(8 * it + 2); }
            if (k == 4) { raw[5] = Q2(8 * it + 64); raw[6] = Q2(8 * it + 66); }
        };
        auto mkT = [&](int it, int aa, int e) {
            T[it & 1][aa][e] = aa == 1 ? (e < 2 ? PERM(raw[2], raw[1], sel[1][2 + e]) : PERM(raw[1], raw[0], sel[1][e])) : PERM(raw[2 * aa], raw[2 * aa - 1], sel[2][e]);
        };
        auto rdX = [&](int k) {
            if (k == 0) { rx[0] = Q0(262); rx[1] = Q0(260); }
            if (k == 1) { rx[2] = Q0(258); rx[3] = Q0(256); }
            if (k == 2) { rx[4] = Q1(128); rx[5] = Q1(130); rx[6] = Q1(132); }
            if (k == 3) { rx[7] = Q2(128); rx[8] = Q2(130); rx[9] = R[-512]; }
        };
        auto fixT = [&](int k, int e) {
            if (k < 2) {
                const u32 lo = k == 0 ? rx[e] : (e < 2 ? PERM(rx[6], rx[5], sel[1][2 + e]) : PERM(rx[5], rx[4], sel[1][e]));
                T[0][1 + k][e] = (lo & msk[e]) | (T[0][1 + k][e] & ~msk[e]);
            } else T32[e] = (PERM(rx[8], rx[7], sel[2][e]) & msk[e]) | (PERM(rx[9], rx[9], 0x03020302u) & mkb[e]);
        };
        LAS char* uw = ubase + 192 + 256 * hf + 1024 * (1 - hf);
        auto rdC = [&](int k) {
            if (k < 4) UW[k] = lds128(uw + 512 * (k >> 1) - 16 * (k & 1));
            else { const int dl = (k - 4) >> 1, e = 2 * (k & 1); TB[dl][e] = Q3(128 + 32 * dl - 2 * e); TB[dl][e + 1] = Q3(128 + 32 * dl - 2 * e - 2); }
        };
#pragma unroll
        for (int k = 0; k < 5; ++k) rdT(3, k);
        SB();
        sfor<104>([&](auto gs) {
            constexpr int it = 3 - gs.v / 26, sl = gs.v % 26, aa = tri_a(sl), bq = tri_b(sl);
            y[aa + bq] = mfma(T[it & 1][aa], U[it & 1][bq], (it == 3 && aa == 0) ? zero4() : y[aa + bq]);
            SB();
            if constexpr (it == 3) {
                if constexpr (sl < 6) { mkT(3, 1 + (sl >> 1), 2 * (sl & 1)); mkT(3, 1 + (sl >> 1), 2 * (sl & 1) + 1); }
                else if constexpr (sl < 14) { if (sl < 11) rdT(2, sl - 6); rdU(2, sl - 6); }
                else mkT(2, 1 + ((sl - 14) >> 2), (sl - 14) & 3);
            } else if constexpr (it > 0) {
                if constexpr (sl < 8) { if (sl < 5) rdT(it - 1, sl); rdU(it - 1, sl); }
                if constexpr (it == 1 && sl >= 5 && sl < 9) rdX(sl - 5);
                if constexpr (it == 2 && sl >= 10 && sl < 22) mkT(1, 1 + ((sl - 10) >> 2), (sl - 10) & 3);
                if constexpr (it == 1 && sl >= 10 && sl < 16) { mkT(0, 1 + ((sl - 10) >> 1), 2 * (sl & 1)); mkT(0, 1 + ((sl - 10) >> 1), 2 * (sl & 1) + 1); }
                if constexpr (it == 1 && sl >= 16 && sl < 24) fixT((sl - 16) >> 2, (sl - 16) & 3);
            } else {
                if constexpr (sl < 12) rdC(sl);
                if constexpr (sl < 4) fixT(2, sl);
                if constexpr (sl >= 10 && sl < 14) wd[sl - 10] = mfma(UW[sl - 10], (sl & 1) ? bE : bO, zero4());
                if constexpr (sl >= 16 && sl < 24) WA[(sl - 16) >> 2][sl & 3] = pk(wd[2 * ((sl - 16) >> 2) + 1][sl & 3], wd[2 * ((sl - 16) >> 2)][sl & 3]);
                if constexpr (sl == 24) TB[0] &= mkd;
            }
            SB();
        });
        if (hf) {
#pragma unroll
            for (int k = 0; k < 4; ++k) y[4 + k] = mfma(WA[0], TB[k], y[4 + k]);
#pragma unroll
            for (int k = 0; k < 3; ++k) y[5 + k] = mfma(WA[1], TB[k], y[5 + k]);
        } else {
#pragma unroll
            for (int k = 0; k < 4; ++k) y[4 + k] = mfma(T32, U[0][k], y[4 + k]);
            y[6] = mfma(WA[0], TB[0], y[6]); y[7] = mfma(WA[0], TB[1], y[7]); y[7] = mfma(WA[1], TB[0], y[7]);
        }
    }
    {
        LAS char* yd = sm + S_TAB + wave * 8192 + lane * 16;
#pragma unroll
        for (int i = 0; i < 8; ++i) sts128(yd + i * 1024, asu(y[i]));
    }
    asm volatile("s_waitcnt vmcnt(0)" : "+v"(tag), "+v"(tch) :: "memory");
    tag += 1;
    wg_barrier();
    {
        LAS char* ys = sm0 + S_TAB + G8 * 1024 + lane * 16;
        u32x4 o[2];
#pragma unroll
        for (int c = 0; c < 2; ++c) {
            const f32x4 y0 = asf(lds128(ys + c * 81920)) + asf(lds128(ys + c * 81920 + 8192));
            const f32x4 y1 = asf(lds128(ys + c * 81920 + 16384)) + asf(lds128(ys + c * 81920 + 24576));
            const float d0 = bflo(dsk[c]), d1 = bfhi(dsk[c]);
#pragma unroll
            for (int i = 0; i < 4; ++i) o[c][i] = pk(y0[i] + bflo(uu[i][c]) * d0, y1[i] + bfhi(uu[i][c]) * d1);
            bst_dev(o[c], rws, WS_YX + (2 * sb + c) * 8192 + G8 * 1024 + lane * 16);
        }
        const u32 s0 = rowsum(sum4(o[0])), s1 = rowsum(sum4(o[1]));
        bst32_dev(tag - (r16 ? s1 : s0), rws, r16 < 2 ? WS_YC + (sb * 8 + G8) * 128 + g * 8 + r16 * 4 : OOB);
    }
    {
        const u32 cofs = (ct * 32 + (tid >> 4)) * 8192 + j * 256 + (tid & 15) * 16;
        const u32 ccs = ((ct * 16 + (tid >> 5)) * 8 + (j >> 2)) * 128 + (j & 3) * 8 + ((tid >> 4) & 1) * 4;
        LAS char* trw = sm + S_TR + (tid & 7) * 288 + (tid >> 3) * 8;
        LAS char* trr = sm + S_TR + (((tid >> 4) & 3) * 8 + (tid >> 6)) * 288 + (tid & 15) * 16;
        const u32 oo = (256 * (j >> 2) + 16 * (tid >> 4) + 4 * (j & 3) + 2 * ((tid >> 3) & 1)) * 1792 + 128 * ct + (tid & 7) * 16;
        waitv<0>();
        u32x4 v[2]; u32 vc[2];
#pragma unroll
        for (int i = 0; i < 2; ++i) { v[i] = bld_dev(rws, cofs, WS_YX + i * 16 * 8192); vc[i] = bld32_dev(rws, ccs, WS_YC + i * 8192); }
        cbar();
        auto ok = [&](int i) { return rowsum(sum4(v[i])) + vc[i] == tag; };
        settle<2>(badmask<2>(ok), ok,
                  [&](auto i, u32 bm) { reld<i.v>(v[i.v], dws, cofs, WS_YX + i.v * 16 * 8192, bm); reld32<i.v>(vc[i.v], dws, ccs, WS_YC + i.v * 8192, bm); },
                  [&](int i) { pin(v[i]); pin(vc[i]); });
        sts128(trr, v[0]); sts128(trr + 4 * 288, v[1]);
        wg_barrier();
        const u32x2 s0 = *(LAS u32x2*)trw, s1 = *(LAS u32x2*)(trw + 8 * 288), s2 = *(LAS u32x2*)(trw + 16 * 288), s3 = *(LAS u32x2*)(trw + 24 * 288);
        const rsrc_t ro = mkrsrc(a.out, 2048 * 1792);
        bst_dev(u32x4{s0.x, s1.x, s2.x, s3.x}, ro, oo);
        bst_dev(u32x4{s0.y, s1.y, s2.y, s3.y}, ro, oo + 1792);
        if (j == 0 && tid == 0) st_epoch(a.ws + WS_EPS + ct * 256, tag);
    }
}

DI void warm_attn(const Args& a, LAS char* sm, int xcd, int hi) {
    const int lane = threadIdx.x & 63, wave8 = __builtin_amdgcn_readfirstlane(threadIdx.x >> 6);
    const int u = hi * 5 + wave8, idx = (u - 24) * 64 + lane;
    if (wave8 >= 5) return;
    setm0(sm + 163840 - 256);
    if (u < 24) {
        const int rbl = (xcd >> 1) * 4 + u / 6, kc = (2 * (rbl + (xcd & 1) * 4) + 1 + 2 * ((u % 6) >> 1)) % 7;
        touch_lds(mkdesc(a.x, 2048 * 896), (rbl * 128 + (u & 1) * 64 + lane) * 896 + kc * 128);
    } else touch_lds(mkdesc(a.wqkv, 448 * 2688), (idx >> 3) * 2688 + (1 + ((idx >> 2) & 1)) * 896 + ((xcd & 1) * 3 + (idx & 3)) * 128);
}

#if defined(__HIP_DEVICE_COMPILE__)
#define NOPK __attribute__((target("no-packed-fp32-ops")))
#else
#define NOPK
#endif
__global__ NOPK __launch_bounds__(512, 2) void fused_kernel(Args a) {
    __shared__ __attribute__((aligned(16))) char smem[163840];
    LAS char* sm = (LAS char*)smem;
    const int xcd = blockIdx.x & 7, j = blockIdx.x >> 3;
    const bool is_attn = (j & 1) == 0;
    if (__builtin_expect(!is_attn, 1)) warm_attn(a, sm, xcd, j >> 1);
    cbar();
    if (__builtin_expect(is_attn, 1)) attn_role(a, sm, xcd, j >> 1);
    else if (j < 28) ssm_role(a, sm, xcd * 14 + (j >> 1));
    else {
        const int t = (j >> 1 & 1) * 512 + threadIdx.x;
        setm0(sm);
#pragma unroll
        for (int i = 0; i < 2; ++i) touch_lds(mkdesc(a.wqkv, 448 * 2688), ((t + 1024 * i) >> 2) * 2688 + ((xcd & 1) * 3 + (t & 3)) * 128);
#pragma unroll
        for (int i = 0; i < 4; ++i) touch_lds(mkdesc(a.wo, 448 * 896), (t + 1024 * i) * 128);
        waitv<0>();
    }
}

extern "C" void kernel_launch(void* const* d_in, const int* in_sizes, int n_in,
                              void* d_out, int out_size, void* d_ws, size_t ws_size,
                              hipStream_t stream) {
    Args a;
    a.u = (const u16*)d_in[0]; a.logA = (const u16*)d_in[1]; a.Aim = (const u16*)d_in[2];
    a.Cre = (const u16*)d_in[3]; a.Cim = (const u16*)d_in[4]; a.logdt = (const u16*)d_in[5];
    a.Dskip = (const u16*)d_in[6]; a.x = (const u16*)d_in[7]; a.wqkv = (const u16*)d_in[8];
    a.wo = (const u16*)d_in[9]; a.out = (u16*)d_out; a.ws = (char*)d_ws;
    fused_kernel<<<256, 512, 0, stream>>>(a);
}
```

```cpp
#include <hip/hip_runtime.h>
#include <stdint.h>

typedef unsigned short u16;
typedef unsigned int u32;
typedef unsigned long long u64;
typedef __bf16 bf16x8 __attribute__((ext_vector_type(8)));
typedef __bf16 bf16x2 __attribute__((ext_vector_type(2)));
typedef _Float16 h2 __attribute__((ext_vector_type(2)));
typedef _Float16 h8 __attribute__((ext_vector_type(8)));
typedef short s16x4 __attribute__((ext_vector_type(4)));
typedef float f32x2 __attribute__((ext_vector_type(2)));
typedef float f32x4 __attribute__((ext_vector_type(4)));
typedef float f32x16 __attribute__((ext_vector_type(16)));
typedef u32 u32x2 __attribute__((ext_vector_type(2)));
typedef u32 u32x4 __attribute__((ext_vector_type(4)));
typedef __amdgpu_buffer_rsrc_t rsrc_t;
#define LAS __attribute__((address_space(3)))
#define GAS __attribute__((address_space(1)))
#define DI __device__ __forceinline__
#define SB() __builtin_amdgcn_sched_barrier(0)
#define KEEP(x) asm volatile("" :: "v"(x))
#define PERM __builtin_amdgcn_perm

constexpr u32 WS_EPA = 0;
constexpr u32 WS_EPS = 256;
constexpr u32 WS_YC  = 65536;
constexpr u32 WS_KVP = 524288;
constexpr u32 WS_OX  = WS_KVP + (1u << 19);
constexpr u32 WS_YX  = WS_OX + (2u << 20);
constexpr u32 WS_END = WS_YX + (2u << 20);
constexpr u32 OOB = 0x80000000u;
constexpr int SPIN_CAP = 1 << 16;

struct Args {
    const u16 *u, *logA, *Aim, *Cre, *Cim, *logdt, *Dskip, *x, *wqkv, *wo;
    u16* out;
    char* ws;
};

DI f32x4 mfma(u32x4 a, u32x4 b, f32x4 c) { return __builtin_amdgcn_mfma_f32_16x16x32_bf16(__builtin_bit_cast(bf16x8, a), __builtin_bit_cast(bf16x8, b), c, 0, 0, 0); }
DI f32x4 mfmah(u32x4 a, u32x4 b, f32x4 c) { return __builtin_amdgcn_mfma_f32_16x16x32_f16(__builtin_bit_cast(h8, a), __builtin_bit_cast(h8, b), c, 0, 0, 0); }
DI f32x16 mfma32(u32x4 a, u32x4 b, f32x16 c) { return __builtin_amdgcn_mfma_f32_32x32x16_bf16(__builtin_bit_cast(bf16x8, a), __builtin_bit_cast(bf16x8, b), c, 0, 0, 0); }
DI u32 pk(float lo, float hi) { f32x2 v = {lo, hi}; return __builtin_bit_cast(u32, __builtin_convertvector(v, bf16x2)); }
DI float bflo(u32 v) { return __uint_as_float(v << 16); }
DI float bfhi(u32 v) { return __uint_as_float(v & 0xffff0000u); }
DI f32x2 unpk(u32 v) { return f32x2{bflo(v), bfhi(v)}; }
DI f32x4 asf(u32x4 v) { return __builtin_bit_cast(f32x4, v); }
DI u32x4 asu(f32x4 v) { return __builtin_bit_cast(u32x4, v); }
DI f32x4 zero4() { return f32x4{0.f, 0.f, 0.f, 0.f}; }
DI f32x16 zero16() { f32x16 z; for (int i = 0; i < 16; ++i) z[i] = 0.f; return z; }
DI u32x4 pk8(const f32x16& a, int s) { return u32x4{pk(a[8 * s], a[8 * s + 1]), pk(a[8 * s + 2], a[8 * s + 3]), pk(a[8 * s + 4], a[8 * s + 5]), pk(a[8 * s + 6], a[8 * s + 7])}; }
DI u32x4 quad(const f32x16& a, int r) { return asu(f32x4{a[4 * r], a[4 * r + 1], a[4 * r + 2], a[4 * r + 3]}); }
template <class T> DI void pin(T& v) { asm volatile("" : "+v"(v) :: "memory"); }
template <int I> struct IC { static constexpr int v = I; };
template <int N, int I = 0, class F> DI void sfor(F f) { if constexpr (I < N) { f(IC<I>{}); sfor<N, I + 1>(f); } }
DI void cbar() { asm volatile("" ::: "memory"); }
DI void raw_barrier() { asm volatile("s_barrier" ::: "memory"); }
DI void wg_barrier() { asm volatile("s_waitcnt lgkmcnt(0)\n\ts_barrier" ::: "memory"); }
template <int N> DI void waitv() { asm volatile("s_waitcnt vmcnt(%0)" :: "i"(N) : "memory"); }
template <int N> DI void waitx(u32x4* x) { asm volatile("s_waitcnt vmcnt(%4)" : "+v"(x[0]), "+v"(x[1]), "+v"(x[2]), "+v"(x[3]) : "i"(N) : "memory"); }

DI u32x4 lds128(LAS char* p) { return *(LAS u32x4*)p; }
DI void sts128(LAS char* p, u32x4 v) { *(LAS u32x4*)p = v; }
DI void sts64(LAS char* p, u32 lo, u32 hi) { u32x2 v = {lo, hi}; *(LAS u32x2*)p = v; }
DI u32x4 trfrag(LAS char* p0, LAS char* p1) {
    u32x2 a = __builtin_bit_cast(u32x2, __builtin_amdgcn_ds_read_tr16_b64_v4i16((LAS s16x4*)p0));
    u32x2 b = __builtin_bit_cast(u32x2, __builtin_amdgcn_ds_read_tr16_b64_v4i16((LAS s16x4*)p1));
    return u32x4{a.x, a.y, b.x, b.y};
}

DI rsrc_t mkrsrc(const void* p, u32 bytes) { return __builtin_amdgcn_make_buffer_rsrc((void*)p, 0, bytes, 0x00020000); }
DI u32x4 mkdesc(const void* p, u32 bytes) { u64 v = (u64)p; return u32x4{(u32)v, (u32)(v >> 32) & 0xffffu, bytes, 0x00020000u}; }
DI u32x4 bld_dev(rsrc_t r, u32 voff, u32 soff) { return __builtin_amdgcn_raw_buffer_load_b128(r, voff, soff, 16); }
DI u32 bld32_dev(rsrc_t r, u32 voff, u32 soff) { return __builtin_amdgcn_raw_buffer_load_b32(r, voff, soff, 16); }
DI void bst_dev(u32x4 v, rsrc_t r, u32 voff) { __builtin_amdgcn_raw_buffer_store_b128(v, r, voff, 0, 16); }
DI void bst32_dev(u32 v, rsrc_t r, u32 voff) { __builtin_amdgcn_raw_buffer_store_b32(v, r, voff, 0, 16); }
DI void st_epoch(char* p, u32 v) { __hip_atomic_store((GAS u32*)p, v, __ATOMIC_RELAXED, __HIP_MEMORY_SCOPE_AGENT); }
DI void setm0(LAS char* dst) { asm volatile("s_mov_b32 m0, %0\n\ts_nop 0" :: "s"((u32)(uintptr_t)dst) : "memory"); }
template <int OFF>
DI void dma(u32x4 desc, u32 voff, u32 soff) { asm volatile("buffer_load_dwordx4 %0, %1, %2 offen offset:%3 lds" :: "v"(voff), "s"(desc), "s"(soff), "i"(OFF) : "memory"); }
DI void ldd(u32x4& v, u32x4 desc, u32 voff, u32 soff) { asm volatile("buffer_load_dwordx4 %0, %1, %2, %3 offen sc1" : "=v"(v) : "v"(voff), "s"(desc), "s"(soff) : "memory"); }
DI u32 ld_epoch(const char* p) { u32 v; asm volatile("global_load_dword %0, %1, %2 sc1" : "=v"(v) : "v"(0u), "s"(p) : "memory"); return v; }
DI u32 touch(u32x4 desc, u32 voff) { u32 v; asm volatile("buffer_load_dword %0, %1, %2, 0 offen" : "=v"(v) : "v"(voff), "s"(desc) : "memory"); return v; }
DI void touch_lds_dev(u32x4 desc, u32 voff) { asm volatile("buffer_load_dword %0, %1, 0 offen sc1 lds" :: "v"(voff), "s"(desc) : "memory"); }
DI void touch_lds(u32x4 desc, u32 voff) { asm volatile("buffer_load_dword %0, %1, 0 offen lds" :: "v"(voff), "s"(desc) : "memory"); }
template <int BIT> DI void reld(u32x4& v, u32x4 desc, u32 voff, u32 soff, u32 mask) {
    asm volatile("s_bitcmp1_b32 %4, %5\n\ts_cbranch_scc0 1f\n\tbuffer_load_dwordx4 %0, %1, %2, %3 offen sc1\n1:"
                 : "+v"(v) : "v"(voff), "s"(desc), "s"(soff), "s"(mask), "i"(BIT) : "scc", "memory");
}
template <int BIT> DI void reld32(u32& v, u32x4 desc, u32 voff, u32 soff, u32 mask) {
    asm volatile("s_bitcmp1_b32 %4, %5\n\ts_cbranch_scc0 1f\n\tbuffer_load_dword %0, %1, %2, %3 offen sc1\n1:"
                 : "+v"(v) : "v"(voff), "s"(desc), "s"(soff), "s"(mask), "i"(BIT) : "scc", "memory");
}
template <int N, class OK, class LD, class PIN>
DI void settle(u32 bad, OK ok, LD ld, PIN pn) {
#pragma unroll 1
    for (int sp = 0; bad != 0 && sp < SPIN_CAP; ++sp) {
        __builtin_amdgcn_s_sleep(2);
        sfor<N>([&](auto i) { ld(i, bad); });
        waitv<0>();
        sfor<N>([&](auto i) { pn(i.v); });
        sfor<N>([&](auto i) { if ((bad >> i.v & 1) && __all(ok(i.v))) bad &= ~(1u << i.v); });
    }
}
template <int N, class OK> DI u32 badmask(OK ok) { u32 bad = 0; sfor<N>([&](auto i) { if (!__all(ok(i.v))) bad |= 1u << i.v; }); return bad; }

DI u32 sum4(u32x4 v) { return v.x + v.y + v.z + v.w; }
DI u32 rowsum(u32 s) {
    s += __builtin_amdgcn_update_dpp(0, s, 0x128, 0xf, 0xf, false);
    s += __builtin_amdgcn_update_dpp(0, s, 0x124, 0xf, 0xf, false);
    s += __builtin_amdgcn_update_dpp(0, s, 0x122, 0xf, 0xf, false);
    s += __builtin_amdgcn_update_dpp(0, s, 0x121, 0xf, 0xf, false);
    return s;
}
DI u32 sum32(u32 s) { s = rowsum(s); auto t = __builtin_amdgcn_permlane16_swap(s, s, false, false); return t[0] + t[1]; }
DI u32 halfsum(u32 s) { auto r = __builtin_amdgcn_permlane32_swap(s, s, false, false); return r[0] + r[1]; }
DI u32 wavemax(u32 s) {
    s = max(s, (u32)__builtin_amdgcn_update_dpp(0, s, 0x128, 0xf, 0xf, false));
    s = max(s, (u32)__builtin_amdgcn_update_dpp(0, s, 0x124, 0xf, 0xf, false));
    s = max(s, (u32)__builtin_amdgcn_update_dpp(0, s, 0x122, 0xf, 0xf, false));
    s = max(s, (u32)__builtin_amdgcn_update_dpp(0, s, 0x121, 0xf, 0xf, false));
    auto r = __builtin_amdgcn_permlane32_swap(s, s, false, false);
    s = max(r[0], r[1]);
    auto t = __builtin_amdgcn_permlane16_swap(s, s, false, false);
    return max(t[0], t[1]);
}

DI float phi(float t) {
    const float e = __builtin_amdgcn_exp2f(t * 1.44269504089f);
    return __int_as_float(max(__float_as_int(t), 0x358637bd)) + __builtin_amdgcn_fmed3f(e, 0.f, 1.f);
}

constexpr int A_RED = 0, A_WO = 32768, A_WQ = 98304, A_KVB = 155648;
constexpr int PRO = 8;
constexpr int LA = 6;
constexpr int QS0 = 40;
constexpr int GK0 = 14;
constexpr int G2A = 2;
constexpr int issued_before(int gs) {
    constexpr int B0 = 16 - LA;
    int n = PRO + gs;
    int cap = 16 + (gs < B0 ? 0 : 4 * ((gs - B0) / 8) + ((gs - B0) % 8 < 4 ? (gs - B0) % 8 : 4));
    if (gs > B0 + 20) cap = 29;
    n = n < cap ? n : cap;
    if (n < 29) return n;
    const int q = gs - QS0;
    return 29 + (q < 0 ? 0 : q > 7 ? 7 : q);
}
static_assert(QS0 >= 40 - LA, "Wq overwrites ring slot 3");
constexpr int slot_ch(int gs) { return gs < 40 ? gs >> 3 : 5 + ((gs >> 2) & 1); }
constexpr int slot_kk(int gs) { return gs < 40 ? (gs & 7) >> 1 : gs & 3; }
constexpr int slot_sg(int gs) { return gs < 40 ? gs & 1 : gs >= 48; }

DI void attn_role(const Args& a, LAS char* sm, const int xcd, const int j) {
    const int tid = threadIdx.x, lane = tid & 63, wave = __builtin_amdgcn_readfirstlane(tid >> 6);
    const int rw4 = wave & 3, hs = wave >> 2;
    const int hp = (xcd & 1) * 4 + (j & 3), rb = (xcd >> 1) * 4 + (j >> 2);
    const int h = 2 * hp + hs;
    const int c0 = (2 * rb + 2 * hp) % 7;
    const int r32 = lane & 31, hh = lane >> 5, blk = (lane >> 4) & 1, q = (lane >> 2) & 3, p = lane & 3;
    const u32x4 rx = mkdesc(a.x, 2048 * 896), rw = mkdesc(a.wqkv, 448 * 2688), rwo = mkdesc(a.wo, 448 * 896), dws = mkdesc(a.ws, WS_END);
    const rsrc_t rws = mkrsrc(a.ws, WS_END);
    u32 tag = ld_epoch(a.ws + WS_EPA), tch = 0;
    const int widx = (lane & 15) ^ ((lane >> 4) << 2);
    const u32 wv = (8 * wave + (lane >> 4)) * 2688 + (1 + (widx >> 3)) * 896 + hp * 112 + min(widx & 7, 6) * 16;
    const u32 xv0 = (rb * 128 + wave * 16 + (lane >> 3)) * 896 + (((lane & 7) ^ (lane >> 4)) * 16);
    const u32 xv1 = (rb * 128 + wave * 16 + 8 + (lane >> 3)) * 896 + (((lane & 7) ^ (4 + (lane >> 4))) * 16) - 1024;
    const u32 qv = (8 * wave + (lane >> 3)) * 2688 + hp * 112 + min((lane & 7) ^ (blk << 2), 6) * 16;
    auto kchunk = [&](int i) { int kc = i + c0; return kc >= 7 ? kc - 7 : kc; };
    auto ldn = [&](int n) {
        if (n >= 29) { setm0(sm + A_WQ + (n - 29) * 8192 + wave * 1024); dma<0>(rw, qv, kchunk(n - 29) * 64 * 2688); return; }
        if (n == 28) {
            tch = touch(dws, OOB);
            return;
        }
        const int i = n >> 2, m = n & 3;
        if (m == 0) { setm0(sm + (i & 3) * 32768 + 16384 + wave * 2048); dma<0>(rx, xv0, kchunk(i) * 128); }
        if (m == 1) dma<1024>(rx, xv1, kchunk(i) * 128);
        if (m == 2) { setm0(sm + (i & 3) * 32768 + wave * 2048); dma<0>(rw, wv, kchunk(i) * 64 * 2688); }
        if (m == 3) dma<1024>(rw, wv, kchunk(i) * 64 * 2688 + 4 * 2688 - 1024);
    };
#pragma unroll
    for (int n = 0; n < PRO; ++n) ldn(n);

    const int wb = 56 * hs + 32 * blk + 8 * p;
    LAS char *wsrc[2], *xsrc[4];
#pragma unroll
    for (int sg = 0; sg < 2; ++sg) wsrc[sg] = sm + (32 * hh + q) * 256 + (((2 * sg + (wb >> 6)) ^ q) << 6) + (wb & 63);
#pragma unroll
    for (int kk = 0; kk < 4; ++kk) xsrc[kk] = sm + 16384 + (32 * rw4 + r32) * 128 + (((4 * hh + kk) ^ ((r32 >> 1) & 7)) * 16);

    const bool p3 = j < 14;
    const int r128 = p3 ? xcd * 2 + j / 7 : 0, nb = j % 7;
    f32x16 acc[2] = {zero16(), zero16()};
    u32x4 xf[28], wf[56];
    auto rdw = [&](int gs) { LAS char* s = wsrc[slot_sg(gs)] + (slot_ch(gs) & 3) * 32768 + slot_kk(gs) * 2048; wf[gs] = trfrag(s, s + 1024); };
    auto rdx = [&](int i, int kk) { xf[4 * i + kk] = lds128(xsrc[kk] + (i & 3) * 32768); };
    waitv<PRO - 4>();
    wg_barrier();
#pragma unroll
    for (int sl = 0; sl < LA; ++sl) { if (sl < 4) rdx(0, sl); rdw(sl); }
    u32 pkd[8];
    u32 vm = r32 == 28 ? 0u : ~0u, vo = r32 == 28 ? 0x3f803f80u : 0u;
    pin(vm); pin(vo);
    sfor<56>([&](auto gs) {
        constexpr int issued = issued_before(gs.v);
        constexpr int fu = gs.v + LA, c = fu == 44 ? 6 : (fu <= 40 && (fu & 7) == 0) ? fu >> 3 : 0;
        static_assert(!c || issued >= 4 * c + 4, "chunk not issued yet");
        if constexpr (c != 0) { waitv<issued - (4 * c + 4)>(); raw_barrier(); }
        SB();
        acc[slot_sg(gs.v)] = mfma32(xf[4 * slot_ch(gs.v) + slot_kk(gs.v)], wf[gs.v], acc[slot_sg(gs.v)]);
        SB();
        if constexpr (fu < 40 && fu >= 8 && (fu & 1) == 0) rdx(fu >> 3, (fu & 7) >> 1);
        if constexpr (fu >= 40 && fu < 48) rdx(slot_ch(fu), fu & 3);
        if constexpr (fu < 56) rdw(fu);
        if constexpr (gs.v == 44) raw_barrier();
        if constexpr (gs.v >= 49) { pkd[gs.v - 49] = pk(phi(acc[0][2 * (gs.v - 49)]), phi(acc[0][2 * (gs.v - 49) + 1])); pin(pkd[gs.v - 49]); }
#pragma unroll
        for (int n = issued; n < issued_before(gs.v + 1); ++n) ldn(n);
        SB();
    });
    {
        pkd[7] = pk(phi(acc[0][14]), phi(acc[0][15]));
        pin(pkd[7]);
        f32x16 kv = mfma32(u32x4{pkd[0], pkd[1], pkd[2], pkd[3]}, (pk8(acc[1], 0) & vm) | vo, zero16());
        SB();
        kv = mfma32(u32x4{pkd[4], pkd[5], pkd[6], pkd[7]}, (pk8(acc[1], 1) & vm) | vo, kv);
#pragma unroll
        for (int rq = 0; rq < 4; ++rq) sts128(sm + A_RED + wave * 4096 + rq * 1024 + lane * 16, quad(kv, rq));
    }
    asm volatile("s_waitcnt vmcnt(8)" : "+v"(tag) :: "memory");
    tag += 1;
    wg_barrier();
    u32 hm = hh ? 0u : ~0u, cm = r32 == 31 ? 0u : ~0u;
    pin(hm); pin(cm);
    const bool red = (rw4 >> 1) == hs;
    if (red) {
        LAS char* rs = sm + A_RED + hs * 16384 + (rw4 & 1) * 2048 + lane * 16;
        f32x4 lo = (asf(lds128(rs)) + asf(lds128(rs + 4096))) + (asf(lds128(rs + 8192)) + asf(lds128(rs + 12288)));
        f32x4 hi = (asf(lds128(rs + 1024)) + asf(lds128(rs + 5120))) + (asf(lds128(rs + 9216)) + asf(lds128(rs + 13312)));
        u32x4 v = {pk(lo.x, lo.y), pk(lo.z, lo.w), pk(hi.x, hi.y), pk(hi.z, hi.w) & cm};
        v.w |= (tag - sum32(sum4(v))) & ~cm;
        bst_dev(v, rws, WS_KVP + (h * 16 + rb) * 2048 + (tid & 127) * 16);
    }
    cbar();
    if (red) waitv<1>(); else waitv<0>();
    KEEP(tch);
    raw_barrier();
    const u32 ov = (8 * wave + (lane >> 3)) * 896 + nb * 128 + (((lane & 7) ^ (blk << 2)) * 16);
    u32x4 gk[8];
    const u32 kbase = WS_KVP + (h * 16 + (rw4 >> 1) * 8 + (rb & 7)) * 2048, kvo = (tid & 127) * 16;
    f32x16 aq = zero16();
    {
        LAS char* qsrc = sm + A_WQ + (32 * hh + q) * 128 + (((wb >> 6) ^ (q >> 1)) << 6) + (wb & 63);
        u32x4 wq[28];
        auto rdq = [&](int k) { LAS char* s = qsrc + (k >> 2) * 8192 + (k & 3) * 1024; wq[k] = trfrag(s, s + 512); };
#pragma unroll
        for (int k = 0; k < 4; ++k) rdq(k);
        SB();
#pragma unroll
        for (int k = 0; k < 28; ++k) {
            aq = mfma32(wq[k], xf[k], aq);
            SB();
            if (k + 4 < 28) rdq(k + 4);
            if (k < 7) { setm0(sm + A_WO + k * 8192 + wave * 1024); dma<0>(rwo, ov, k * 64 * 896); }
            if (k >= GK0 && k < GK0 + 8) ldd(gk[k - GK0], dws, kvo, kbase ^ ((k - GK0) * 2048));
            SB();
        }
    }
    u32 pqd[8];
#pragma unroll
    for (int k = 0; k < 8; ++k) {
        pqd[k] = pk(phi(aq[2 * k]), phi(aq[2 * k + 1]));
        if (k >= 6) pqd[k] &= hm;
        pin(pqd[k]);
    }
    {
        waitx<0>(gk); waitx<0>(gk + 4);
        u32 t = 0;
#pragma unroll
        for (int i = 0; i < 8; ++i) t += sum4(gk[i]);
        if (!__all(sum32(t) == 8u * tag)) {
            auto ok = [&](int i) { return sum32(sum4(gk[i])) == tag; };
            settle<8>(badmask<8>(ok), ok, [&](auto i, u32 bm) { reld<i.v>(gk[i.v], dws, kvo, kbase ^ (i.v * 2048), bm); }, [&](int i) { pin(gk[i]); });
        }
    }
    {
        u32x4 v;
#pragma unroll
        for (int c = 0; c < 4; ++c) {
            const float lo = ((bflo(gk[0][c]) + bflo(gk[1][c])) + (bflo(gk[2][c]) + bflo(gk[3][c]))) + ((bflo(gk[4][c]) + bflo(gk[5][c])) + (bflo(gk[6][c]) + bflo(gk[7][c])));
            const float hi = ((bfhi(gk[0][c]) + bfhi(gk[1][c])) + (bfhi(gk[2][c]) + bfhi(gk[3][c]))) + ((bfhi(gk[4][c]) + bfhi(gk[5][c])) + (bfhi(gk[6][c]) + bfhi(gk[7][c])));
            v[c] = pk(lo, hi);
        }
        sts128(sm + A_KVB + tid * 16, v);
    }
    wg_barrier();
    {
        u32x4 kf[4];
#pragma unroll
        for (int i = 0; i < 4; ++i) kf[i] = lds128(sm + A_KVB + hs * 4096 + i * 1024 + lane * 16);
        const u32x4 pq0 = {pqd[0], pqd[1], pqd[2], pqd[3]}, pq1 = {pqd[4], pqd[5], pqd[6], pqd[7]};
        f32x16 oa = mfma32(kf[3], pq1, mfma32(kf[2], pq0, mfma32(kf[1], pq1, mfma32(kf[0], pq0, zero16()))));
        const u32 dn = __float_as_uint(oa[12]);
        auto r = __builtin_amdgcn_permlane32_swap(dn, dn, false, false);
        oa *= __builtin_amdgcn_rcpf(__uint_as_float(r[1]) + 1e-6f);
        const u32x4 f0 = pk8(oa, 0);
        const u32 o = WS_OX + ((rb * 4 + rw4) * 16 + h) * 2048 + lane * 16;
        bst_dev(f0, rws, o);
        SB();
        u32x4 f1 = pk8(oa, 1);
        f1.z &= hm; f1.w &= hm;
        f1.w |= (tag - halfsum(sum4(f0) + sum4(f1))) & ~hm;
        bst_dev(f1, rws, o + 1024);
    }
    cbar();
    if (p3) {
        LAS char* osrc[2];
#pragma unroll
        for (int nt = 0; nt < 2; ++nt) osrc[nt] = sm + A_WO + (224 * hs + 4 * hh + q) * 128 + ((nt ^ (q >> 1)) * 64) + blk * 32 + 8 * p;
        const int padoff = hh ? 0 : 1024;
        u32x4 wf3[8][2][2];
#pragma unroll
        for (int k = 0; k < 32; ++k) {
            const int hl = k >> 2, ks = (k >> 1) & 1, nt = k & 1;
            LAS char* s = osrc[nt] + (hl * 28 + 16 * ks) * 128;
            wf3[hl][ks][nt] = trfrag(s, s + (ks ? padoff : 1024));
        }
        SB();
        waitv<0>();
        u32x4 go[16];
        const u32 ob0 = WS_OX + ((r128 * 4 + rw4) * 16 + 8 * hs) * 2048;
        auto ldo = [&](int i) { ldd(go[i], dws, lane * 16, ob0 + i * 1024); };
#pragma unroll
        for (int i = 0; i < 4 * G2A; ++i) ldo(i);
        f32x16 ac[2];
        sfor<4>([&](auto gi) {
            constexpr int i = 4 * gi.v, nis = gi.v + G2A < 4 ? gi.v + G2A : 4;
            waitx<4 * (nis - gi.v - 1)>(go + i);
            {
                auto r0 = __builtin_amdgcn_permlane32_swap(sum4(go[i]) + sum4(go[i + 1]), sum4(go[i + 2]) + sum4(go[i + 3]), false, false);
                const u64 ne = __ballot(r0[0] + r0[1] != tag);
                if (ne)
                    settle<2>(((u32)ne != 0) | ((ne >> 32) != 0) << 1, [&](int k) { return halfsum(sum4(go[i + 2 * k]) + sum4(go[i + 2 * k + 1])) == tag; },
                              [&](auto k, u32 bm) { reld<k.v>(go[i + 2 * k.v], dws, lane * 16, ob0 + (i + 2 * k.v) * 1024, bm);
                                                    reld<k.v>(go[i + 2 * k.v + 1], dws, lane * 16, ob0 + (i + 2 * k.v + 1) * 1024, bm); },
                              [&](int k) { pin(go[i + 2 * k]); pin(go[i + 2 * k + 1]); });
            }
            go[i + 1].w &= hm; go[i + 3].w &= hm;
            SB();
#pragma unroll
            for (int k = 0; k < 8; ++k) {
                const int hl = 2 * gi.v + (k >> 2), ks = (k >> 1) & 1, nt = k & 1;
                ac[nt] = mfma32(wf3[hl][ks][nt], go[2 * hl + ks], (gi.v == 0 && k < 2) ? zero16() : ac[nt]);
                SB();
                if ((k & 1) == 0 && gi.v + G2A < 4) ldo(4 * (gi.v + G2A) + (k >> 1));
                SB();
            }
        });
        LAS char* xb = sm + wave * 4096 + lane * 16;
        if (hs) {
#pragma unroll
            for (int rq = 0; rq < 4; ++rq) sts128(xb + rq * 1024, quad(ac[0], rq));
            ac[0] = ac[1];
        } else {
#pragma unroll
            for (int rq = 0; rq < 4; ++rq) sts128(xb + rq * 1024, quad(ac[1], rq));
        }
        wg_barrier();
        LAS char* pb = sm + (wave ^ 4) * 4096 + lane * 16;
        LAS char* ob = sm + A_WQ + rw4 * 4608;
#pragma unroll
        for (int rq = 0; rq < 4; ++rq) {
            const f32x4 v = asf(quad(ac[0], rq)) + asf(lds128(pb + rq * 1024));
            sts64(ob + r32 * 144 + hs * 64 + rq * 16 + hh * 8, pk(v.x, v.y), pk(v.z, v.w));
        }
        wg_barrier();
        const rsrc_t ro = mkrsrc(a.out, 2048 * 1792);
        const int orow = 16 * hs + (lane >> 3);
        const u32 oo = (r128 * 128 + rw4 * 32 + orow) * 1792 + 896 + nb * 128 + (lane & 7) * 16;
        const u32x4 v0 = lds128(ob + orow * 144 + (lane & 7) * 16), v1 = lds128(ob + (8 + orow) * 144 + (lane & 7) * 16);
        bst_dev(v0, ro, oo);
        bst_dev(v1, ro, oo + 8 * 1792);
        if (xcd == 0 && j == 0 && tid == 0) st_epoch(a.ws + WS_EPA, tag);
    }
}

constexpr int S_TAB = 0;
constexpr int S_KC = 34816;
constexpr int S_P = 47104;
constexpr int S_UT = 52480;
constexpr int S_TR = 61696;

struct c2 { f32x2 r, i; };
struct hc { h2 r, i; };
DI c2 csq(c2 a) { return c2{a.r * a.r - a.i * a.i, 2.f * a.r * a.i}; }
DI hc hmul(hc a, hc b) { return hc{a.r * b.r - a.i * b.i, a.r * b.i + a.i * b.r}; }
DI hc toh(c2 a) { return hc{__builtin_convertvector(a.r, h2), __builtin_convertvector(a.i, h2)}; }
DI c2 cexp2(f32x2 lr, f32x2 rev, float pw) {
    f32x2 e = lr * pw, f = rev * pw;
    f32x2 m = {__builtin_amdgcn_exp2f(e.x), __builtin_amdgcn_exp2f(e.y)};
    f.x = __builtin_amdgcn_fractf(f.x); f.y = __builtin_amdgcn_fractf(f.y);
    f32x2 c = {__builtin_amdgcn_cosf(f.x), __builtin_amdgcn_cosf(f.y)}, s = {__builtin_amdgcn_sinf(f.x), __builtin_amdgcn_sinf(f.y)};
    return c2{m * c, m * s};
}
constexpr int tri_a(int sl) { int a = 0; while (sl >= 8 - a) { sl -= 8 - a; ++a; } return a; }
constexpr int tri_b(int sl) { int a = 0; while (sl >= 8 - a) { sl -= 8 - a; ++a; } return sl; }

DI void ssm_role(const Args& a, LAS char* sm0, const int sb) {
    const int tid = threadIdx.x & 255, lane = tid & 63, wave8 = __builtin_amdgcn_readfirstlane(threadIdx.x >> 6), wave = wave8 & 3;
    LAS char* sm = sm0 + (wave8 >> 2) * 81920;
    const int ch = wave >> 1, hf = wave & 1, d = 4 * sb + (wave8 >> 1);
    const int g = lane >> 4, r16 = lane & 15;
    const u32 po = (d * 128 + hf * 64 + lane) * 4;
    const u32 ldt = a.logdt[d];
    const u32 pA = *(const u32*)((const char*)a.logA + po), pI = *(const u32*)((const char*)a.Aim + po);
    const u32 pCr = *(const u32*)((const char*)a.Cre + po), pCi = *(const u32*)((const char*)a.Cim + po);
    const u32x2 dsk = ((const u32x2*)a.Dskip)[sb];
    cbar();
    const int G8 = (wave8 + sb) & 7, trow = 256 * G8 + 16 * r16 + 4 * g;
    const rsrc_t ru = mkrsrc(a.u, 2048 * 896);
    u32x2 uu[4];
    auto ldu = [&](int i) { SB(); uu[i] = __builtin_amdgcn_raw_buffer_load_b64(ru, trow * 896 + sb * 8 + i * 896, 0, 0); cbar(); SB(); };
    const int sidx = 2 * sb + (wave8 >> 2);
    const int ct = sidx >> 5, j = sidx & 31;
    u32 tag = ld_epoch(a.ws + WS_EPS + ct * 256);
    *(LAS u32*)(sm + S_UT + (tid >> 7) * 4608 + (tid & 127) * 4) = 0;
    const int w = 16 + r16 - 8 * g;
    u32x4 bE, bO, mkb, mkd;
    u32 sel[3][4], msk[4], pb[3];
    {
        u32 y[4];
#pragma unroll
        for (int e = 0; e < 4; ++e) {
            y[e] = (u32)(w + 64 - 2 * e) * 0x10001u - 0x10000u;
            msk[e] = hf ? 0u : (((y[e] >> 6) & 0x10001u) ^ 0x10001u) * 0xffffu;
        }
        pb[0] = w - 7;
#pragma unroll
        for (int s = 1; s < 3; ++s) {
            u32 r[4];
#pragma unroll
            for (int e = (s == 1 ? 2 : 0); e < 4; ++e)
                r[e] = ((y[e] + ((1u << (s - 1)) - 1u) * 0x10001u + ((y[e] >> s) & 0x10001u)) >> s) & 0x00ff00ffu;
            const u32 base = r[3] >> 16;
            pb[s] = base - (64 >> s);
#pragma unroll
            for (int e = (s == 1 ? 2 : 0); e < 4; ++e) sel[s][e] = (((r[e] - base * 0x10001u) ^ 0x10001u) & 0xfffffu) * 0x202u + 0x01000100u;
        }
#pragma unroll
        for (int e = 0; e < 4; ++e) {
            const int z = min(max(16 - w + 2 * e, 0), 20);
            auto two = [&](u32 m) { const u32 b2 = (m >> z) & 3u; return (b2 | (b2 << 15)) & 0x10001u; };
            bE[e] = two(0x1ff0u) * 0x3f80u; bO[e] = two(0x0fe0u) * 0x3f80u; mkb[e] = two(0x1f000u) * 0xffffu;
            mkd[e] = r16 - 4 * g - e >= 0 ? ~0u : 0u;
        }
        asm volatile("" : "+v"(sel[1][2]), "+v"(sel[1][3]), "+v"(sel[2][0]), "+v"(sel[2][1]), "+v"(sel[2][2]), "+v"(sel[2][3]), "+v"(bE), "+v"(bO), "+v"(mkb), "+v"(mkd));
        asm volatile("" : "+v"(msk[0]), "+v"(msk[1]), "+v"(msk[2]), "+v"(msk[3]));
    }
    LAS u32* const R = (LAS u32*)(sm + S_P + ch * 2688) + 655;
    LAS u32 *P0 = R - (128 * hf + (int)pb[0]) - 102, *P1 = R - (256 + 64 * hf + (int)pb[1]) - 132, *P2 = R - (384 + 32 * hf + (int)pb[2]) - 130,
            *P3 = R - (385 + 2 * (r16 - 4 * g)) - 224;
    LAS char* ubase = sm + S_UT + ch * 4608 + 480 + r16 * 32 + g * 16 - 256 * hf - 192;
    asm volatile("" : "+v"(P0), "+v"(P1), "+v"(P2), "+v"(P3), "+v"(ubase));
    LAS char* tab = sm + S_TAB + wave * 8704;
    LAS char* tw = tab + lane * 8;
    LAS char* tr = tab + r16 * 544 + g * 16;
    LAS char* kc = sm + S_KC + wave * 3072;
    SB();
#define EMIT1(row, v) sts64(tw + (row) * 544, __builtin_bit_cast(u32, (v).r), __builtin_bit_cast(u32, (v).i))
#define EMIT2(row, v0, v1) do { SB(); EMIT1(row, v0); EMIT1((row) + 1, v1); SB(); } while (0)
#define KRD() do { SB(); _Pragma("unroll") for (int ks = 0; ks < 8; ++ks) gf[ks] = lds128(tr + ks * 64); cbar(); SB(); } while (0)
#define KM2(k) do { ka = mfmah(gf[2 * (k)], ff[2 * (k)], (k) ? ka : zero4()); ka = mfmah(gf[2 * (k) + 1], ff[2 * (k) + 1], ka); } while (0)
    const float L2E = 1.44269504089f;
    const float edt = __builtin_amdgcn_exp2f(bflo(ldt) * L2E);
    const float dt = bflo(pk(edt, edt));
    f32x2 lr = unpk(pA) * L2E;
    lr = unpk(pk(__builtin_amdgcn_exp2f(lr.x), __builtin_amdgcn_exp2f(lr.y))) * dt * -L2E;
    const f32x2 rev = unpk(pI) * dt * 0.15915494309f;
    hc W[8], F[16], G[17], H, t0, t1;
    u32x4 ff[8], gf[8];
    f32x4 ka;
    c2 e = cexp2(lr, -rev, 8.f);
    F[1] = toh(e); e = csq(e); F[2] = toh(e);
    F[0] = hc{h2{1.f16, 1.f16}, h2{0.f16, 0.f16}};
    EMIT2(0, F[0], F[1]);
    e = csq(e); F[4] = toh(e); F[3] = hmul(F[1], F[2]);
    EMIT2(2, F[2], F[3]);
    e = csq(e); F[8] = toh(e); F[5] = hmul(F[4], F[1]);
    EMIT2(4, F[4], F[5]);
    F[6] = hmul(F[4], F[2]); F[7] = hmul(F[4], F[3]); F[9] = hmul(F[8], F[1]);
    EMIT2(6, F[6], F[7]);
    {
        const int xcd = sb / 14, un = ((sb % 14) * 3 + wave8) * 64 + lane, v = un - 512, h0 = (xcd & 1) * 8, r0 = (xcd >> 1) * 4;
        setm0(sm0 + 163840 - 256);
        touch_lds_dev(mkdesc(a.ws, WS_END), wave8 >= 3 || un >= 2560 ? OOB
                      : un < 512 ? WS_KVP + ((h0 + (un >> 6)) * 16 + r0) * 2048 + (un & 63) * 128
                                 : WS_OX + ((r0 * 4 + (v >> 7)) * 16 + h0) * 2048 + (v & 127) * 128);
    }
    EMIT2(8, F[8], F[9]);
    F[10] = hmul(F[8], F[2]); F[11] = hmul(F[8], F[3]);
    e = cexp2(lr, rev, 1.f); W[1] = toh(e);
    EMIT2(10, F[10], F[11]);
    F[12] = hmul(F[8], F[4]); F[13] = hmul(F[8], F[5]);
    e = csq(e); W[2] = toh(e);
    EMIT2(12, F[12], F[13]);
    F[14] = hmul(F[8], F[6]); F[15] = hmul(F[8], F[7]);
    e = csq(e); W[4] = toh(e);
    EMIT2(14, F[14], F[15]);
    u32 ce = max(max(pCr & 0x7fffu, (pCr >> 16) & 0x7fffu), max(pCi & 0x7fffu, (pCi >> 16) & 0x7fffu));
    ce = min(max(wavemax(ce) >> 7, 1u), 253u);
    const float cdn = __uint_as_float((254u - ce) << 23), cup = __uint_as_float(ce << 23) * (2.f * dt);
    G[0] = toh(c2{unpk(pCr) * cdn, unpk(pCi) * cdn});
    t1 = hmul(G[0], W[1]);
    SB();
#pragma unroll
    for (int ks = 0; ks < 8; ++ks) ff[ks] = lds128(tr + ks * 64);
    cbar();
    ldu(0);
    EMIT2(0, G[0], t1);
    W[3] = hmul(W[1], W[2]); t0 = hmul(G[0], W[2]); t1 = hmul(G[0], W[3]);
    EMIT2(2, t0, t1);
    W[5] = hmul(W[4], W[1]); t0 = hmul(G[0], W[4]); t1 = hmul(G[0], W[5]);
    EMIT2(4, t0, t1);
    W[6] = hmul(W[4], W[2]); W[7] = hmul(W[4], W[3]); t0 = hmul(G[0], W[6]); t1 = hmul(G[0], W[7]);
    ldu(1);
    EMIT2(6, t0, t1);
    e = cexp2(lr, rev, 128.f); H = toh(e);
    G[1] = hmul(G[0], H); t1 = hmul(G[1], W[1]);
    EMIT2(8, G[1], t1);
    t0 = hmul(G[1], W[2]); t1 = hmul(G[1], W[3]);
    e = csq(e); H = toh(e);
    EMIT2(10, t0, t1);
    t0 = hmul(G[1], W[4]); t1 = hmul(G[1], W[5]); G[2] = hmul(G[0], H);
    ldu(2);
    EMIT2(12, t0, t1);
    t0 = hmul(G[1], W[6]); t1 = hmul(G[1], W[7]); G[3] = hmul(G[1], H);
    EMIT2(14, t0, t1);
    t1 = hmul(G[2], W[2]);
    e = csq(e); H = toh(e);
    KRD();
    EMIT2(0, G[2], t1);
    t0 = hmul(G[2], W[4]); t1 = hmul(G[2], W[6]); G[4] = hmul(G[0], H);
    ldu(3);
    EMIT2(2, t0, t1);
    t1 = hmul(G[3], W[2]); G[5] = hmul(G[1], H); KM2(0);
    EMIT2(4, G[3], t1);
    t0 = hmul(G[3], W[4]); t1 = hmul(G[3], W[6]); KM2(1);
    EMIT2(6, t0, t1);
    t1 = hmul(G[4], W[4]); G[6] = hmul(G[2], H); KM2(2);
    EMIT2(8, G[4], t1);
    t1 = hmul(G[5], W[4]); G[7] = hmul(G[3], H); KM2(3);
    EMIT2(10, G[5], t1);
    t1 = hmul(G[6], W[4]);
    e = csq(e); H = toh(e);
    EMIT2(12, G[6], t1);
    t1 = hmul(G[7], W[4]); G[8] = hmul(G[0], H);
    sts128(kc + (128 * (g >> 1) + 4 * (g & 1) + 8 * r16) * 4, asu(ka * cup));
    EMIT2(14, G[7], t1);
    G[9] = hmul(G[1], H); G[10] = hmul(G[2], H);
    KRD();
    EMIT2(0, G[8], G[9]);
    G[11] = hmul(G[3], H); G[12] = hmul(G[4], H); G[13] = hmul(G[5], H);
    EMIT2(2, G[10], G[11]);
    G[14] = hmul(G[6], H); KM2(0); KM2(1);
    EMIT2(4, G[12], G[13]);
    G[15] = hmul(G[7], H); G[16] = hmul(G[8], H); KM2(2); KM2(3);
    EMIT2(6, G[14], G[15]);
    EMIT1(8, G[16]);
    KRD();
    {
        LAS char* k0 = kc + (g < 2 ? 256 + 64 * g + 4 * r16 : 384 + 64 * (g - 2) + 2 * r16) * 4;
        const u32x4 kb = asu(ka * cup);
        sts64(k0, kb.x, kb.y);
        sts64(k0 + (g < 2 ? 8 : 128), kb.z, kb.w);
    }
    SB();
    KM2(0); KM2(1); KM2(2); KM2(3);
    {
        LAS char* ud = sm0 + S_UT + 512 + trow * 2;
        sts64(ud, PERM(uu[1].x, uu[0].x, 0x05040100u), PERM(uu[3].x, uu[2].x, 0x05040100u));
        sts64(ud + 4608, PERM(uu[1].x, uu[0].x, 0x07060302u), PERM(uu[3].x, uu[2].x, 0x07060302u));
        sts64(ud + 81920, PERM(uu[1].y, uu[0].y, 0x05040100u), PERM(uu[3].y, uu[2].y, 0x05040100u));
        sts64(ud + 81920 + 4608, PERM(uu[1].y, uu[0].y, 0x07060302u), PERM(uu[3].y, uu[2].y, 0x07060302u));
    }
    {
        LAS float* k0 = (LAS float*)(kc + (512 + 64 * g + r16) * 4);
        k0[0] = ka.x * cup; k0[16] = ka.y * cup; k0[32] = ka.z * cup; k0[48] = ka.w * cup;
    }
    wg_barrier();
    const rsrc_t rws = mkrsrc(a.ws, WS_END);
    const u32x4 dws = mkdesc(a.ws, WS_END);
    u32 tch = touch(dws, lane < 16 ? WS_YX + (2 * sb + (lane >> 3)) * 8192 + G8 * 1024 + (lane & 7) * 128 : lane == 16 ? WS_YC + (sb * 8 + G8) * 128 : OOB);
    u32x4 U[2][8];
    auto rdU = [&](int it, int bq) { U[it & 1][bq] = lds128(ubase + 512 * bq + 192 - 64 * it); };
    {
        LAS char* k0 = sm + S_KC + ch * 6144;
        LAS char* Rb = sm + S_P + ch * 2688;
        const int t = tid & 127, e1 = min(t + 128, 167);
        f32x4 a0 = asf(lds128(k0 + t * 16)), b0 = asf(lds128(k0 + 3072 + t * 16)), a1 = asf(lds128(k0 + e1 * 16)), b1 = asf(lds128(k0 + 3072 + e1 * 16));
        const float n0 = *(LAS float*)(k0 + t * 16 + 16) + *(LAS float*)(k0 + 3072 + t * 16 + 16);
        const float n1 = *(LAS float*)(k0 + e1 * 16 + 16) + *(LAS float*)(k0 + 3072 + e1 * 16 + 16);
#pragma unroll
        for (int bq = 0; bq < 8; ++bq) rdU(3, bq);
        a0 += b0; a1 += b1;
        float p0 = a0.x, p1 = a0.y, p2 = a0.z, p3 = a0.w, q0 = a1.x, q1 = a1.y, q2 = a1.z, q3 = a1.w;
        asm volatile("" : "+v"(p0), "+v"(p1), "+v"(p2), "+v"(p3), "+v"(q0), "+v"(q1), "+v"(q2), "+v"(q3));
        sts128(Rb + (163 - t) * 16, u32x4{pk(n0, p3), pk(p3, p2), pk(p2, p1), pk(p1, p0)});
        u32x4 v1 = {pk(n1, q3), pk(q3, q2), pk(q2, q1), pk(q1, q0)};
        if (t >= 36) v1 = u32x4{0, 0, 0, 0};
        if (t < 39) sts128(Rb + (t < 36 ? 35 - t : 129 + t) * 16, v1);
        if (t == 0) sts128(Rb + 164 * 16, u32x4{pk(p0, 0.f), 0, 0, 0});
    }
    wg_barrier();
    f32x4 y[8];
    {
#define Q0(i) P0[102 - (i)]
#define Q1(i) P1[132 - (i)]
#define Q2(i) P2[130 - (i)]
#define Q3(i) P3[224 - (i)]
        u32x4 T[2][4], T32, TB[4], UW[4], WA[2];
        f32x4 wd[4];
        u32 raw[7], rx[10];
        auto rdT = [&](int it, int k) {
            u32x4& f0 = T[it & 1][0];
            if (k == 0) { f0.x = Q0(32 * it + 6); f0.y = Q0(32 * it + 4); }
            if (k == 1) { f0.z = Q0(32 * it + 2); f0.w = Q0(32 * it); }
            if (k == 2) { raw[0] = Q1(16 * it); raw[1] = Q1(16 * it + 2); raw[2] = Q1(16 * it + 4); }
            if (k == 3) { raw[3] = Q2(8 * it); raw[4] = Q2(8 * it + 2); }
            if (k == 4) { raw[5] = Q2(8 * it + 64); raw[6] = Q2(8 * it + 66); }
        };
        auto mkT = [&](int it, int aa, int e) {
            T[it & 1][aa][e] = aa == 1 ? (e < 2 ? PERM(raw[2], raw[1], sel[1][2 + e]) : PERM(raw[1], raw[0], sel[1][e])) : PERM(raw[2 * aa], raw[2 * aa - 1], sel[2][e]);
        };
        auto rdX = [&](int k) {
            if (k == 0) { rx[0] = Q0(262); rx[1] = Q0(260); }
            if (k == 1) { rx[2] = Q0(258); rx[3] = Q0(256); }
            if (k == 2) { rx[4] = Q1(128); rx[5] = Q1(130); rx[6] = Q1(132); }
            if (k == 3) { rx[7] = Q2(128); rx[8] = Q2(130); rx[9] = R[-512]; }
        };
        auto fixT = [&](int k, int e) {
            if (k < 2) {
                const u32 lo = k == 0 ? rx[e] : (e < 2 ? PERM(rx[6], rx[5], sel[1][2 + e]) : PERM(rx[5], rx[4], sel[1][e]));
                T[0][1 + k][e] = (lo & msk[e]) | (T[0][1 + k][e] & ~msk[e]);
            } else T32[e] = (PERM(rx[8], rx[7], sel[2][e]) & msk[e]) | (PERM(rx[9], rx[9], 0x03020302u) & mkb[e]);
        };
        LAS char* uw = ubase + 192 + 256 * hf + 1024 * (1 - hf);
        auto rdC = [&](int k) {
            if (k < 4) UW[k] = lds128(uw + 512 * (k >> 1) - 16 * (k & 1));
            else { const int dl = (k - 4) >> 1, e = 2 * (k & 1); TB[dl][e] = Q3(128 + 32 * dl - 2 * e); TB[dl][e + 1] = Q3(128 + 32 * dl - 2 * e - 2); }
        };
#pragma unroll
        for (int k = 0; k < 5; ++k) rdT(3, k);
        SB();
        sfor<104>([&](auto gs) {
            constexpr int it = 3 - gs.v / 26, sl = gs.v % 26, aa = tri_a(sl), bq = tri_b(sl);
            y[aa + bq] = mfma(T[it & 1][aa], U[it & 1][bq], (it == 3 && aa == 0) ? zero4() : y[aa + bq]);
            SB();
            if constexpr (it == 3) {
                if constexpr (sl < 6) { mkT(3, 1 + (sl >> 1), 2 * (sl & 1)); mkT(3, 1 + (sl >> 1), 2 * (sl & 1) + 1); }
                else if constexpr (sl < 14) { if (sl < 11) rdT(2, sl - 6); rdU(2, sl - 6); }
                else mkT(2, 1 + ((sl - 14) >> 2), (sl - 14) & 3);
            } else if constexpr (it > 0) {
                if constexpr (sl < 8) { if (sl < 5) rdT(it - 1, sl); rdU(it - 1, sl); }
                if constexpr (it == 1 && sl >= 5 && sl < 9) rdX(sl - 5);
                if constexpr (it == 2 && sl >= 10 && sl < 22) mkT(1, 1 + ((sl - 10) >> 2), (sl - 10) & 3);
                if constexpr (it == 1 && sl >= 10 && sl < 16) { mkT(0, 1 + ((sl - 10) >> 1), 2 * (sl & 1)); mkT(0, 1 + ((sl - 10) >> 1), 2 * (sl & 1) + 1); }
                if constexpr (it == 1 && sl >= 16 && sl < 24) fixT((sl - 16) >> 2, (sl - 16) & 3);
            } else {
                if constexpr (sl < 12) rdC(sl);
                if constexpr (sl < 4) fixT(2, sl);
                if constexpr (sl >= 10 && sl < 14) wd[sl - 10] = mfma(UW[sl - 10], (sl & 1) ? bE : bO, zero4());
                if constexpr (sl >= 16 && sl < 24) WA[(sl - 16) >> 2][sl & 3] = pk(wd[2 * ((sl - 16) >> 2) + 1][sl & 3], wd[2 * ((sl - 16) >> 2)][sl & 3]);
                if constexpr (sl == 24) TB[0] &= mkd;
            }
            SB();
        });
        if (hf) {
#pragma unroll
            for (int k = 0; k < 4; ++k) y[4 + k] = mfma(WA[0], TB[k], y[4 + k]);
#pragma unroll
            for (int k = 0; k < 3; ++k) y[5 + k] = mfma(WA[1], TB[k], y[5 + k]);
        } else {
#pragma unroll
            for (int k = 0; k < 4; ++k) y[4 + k] = mfma(T32, U[0][k], y[4 + k]);
            y[6] = mfma(WA[0], TB[0], y[6]); y[7] = mfma(WA[0], TB[1], y[7]); y[7] = mfma(WA[1], TB[0], y[7]);
        }
    }
    {
        LAS char* yd = sm + S_TAB + wave * 8192 + lane * 16;
#pragma unroll
        for (int i = 0; i < 8; ++i) sts128(yd + i * 1024, asu(y[i]));
    }
    asm volatile("s_waitcnt vmcnt(0)" : "+v"(tag), "+v"(tch) :: "memory");
    tag += 1;
    wg_barrier();
    {
        LAS char* ys = sm0 + S_TAB + G8 * 1024 + lane * 16;
        u32x4 o[2];
#pragma unroll
        for (int c = 0; c < 2; ++c) {
            const f32x4 y0 = asf(lds128(ys + c * 81920)) + asf(lds128(ys + c * 81920 + 8192));
            const f32x4 y1 = asf(lds128(ys + c * 81920 + 16384)) + asf(lds128(ys + c * 81920 + 24576));
            const float d0 = bflo(dsk[c]), d1 = bfhi(dsk[c]);
#pragma unroll
            for (int i = 0; i < 4; ++i) o[c][i] = pk(y0[i] + bflo(uu[i][c]) * d0, y1[i] + bfhi(uu[i][c]) * d1);
            bst_dev(o[c], rws, WS_YX + (2 * sb + c) * 8192 + G8 * 1024 + lane * 16);
        }
        const u32 s0 = rowsum(sum4(o[0])), s1 = rowsum(sum4(o[1]));
        bst32_dev(tag - (r16 ? s1 : s0), rws, r16 < 2 ? WS_YC + (sb * 8 + G8) * 128 + g * 8 + r16 * 4 : OOB);
    }
    {
        const u32 cofs = (ct * 32 + (tid >> 4)) * 8192 + j * 256 + (tid & 15) * 16;
        const u32 ccs = ((ct * 16 + (tid >> 5)) * 8 + (j >> 2)) * 128 + (j & 3) * 8 + ((tid >> 4) & 1) * 4;
        LAS char* trw = sm + S_TR + (tid & 7) * 288 + (tid >> 3) * 8;
        LAS char* trr = sm + S_TR + (((tid >> 4) & 3) * 8 + (tid >> 6)) * 288 + (tid & 15) * 16;
        const u32 oo = (256 * (j >> 2) + 16 * (tid >> 4) + 4 * (j & 3) + 2 * ((tid >> 3) & 1)) * 1792 + 128 * ct + (tid & 7) * 16;
        waitv<0>();
        u32x4 v[2]; u32 vc[2];
#pragma unroll
        for (int i = 0; i < 2; ++i) { v[i] = bld_dev(rws, cofs, WS_YX + i * 16 * 8192); vc[i] = bld32_dev(rws, ccs, WS_YC + i * 8192); }
        cbar();
        auto ok = [&](int i) { return rowsum(sum4(v[i])) + vc[i] == tag; };
        settle<2>(badmask<2>(ok), ok,
                  [&](auto i, u32 bm) { reld<i.v>(v[i.v], dws, cofs, WS_YX + i.v * 16 * 8192, bm); reld32<i.v>(vc[i.v], dws, ccs, WS_YC + i.v * 8192, bm); },
                  [&](int i) { pin(v[i]); pin(vc[i]); });
        sts128(trr, v[0]); sts128(trr + 4 * 288, v[1]);
        wg_barrier();
        const u32x2 s0 = *(LAS u32x2*)trw, s1 = *(LAS u32x2*)(trw + 8 * 288), s2 = *(LAS u32x2*)(trw + 16 * 288), s3 = *(LAS u32x2*)(trw + 24 * 288);
        const rsrc_t ro = mkrsrc(a.out, 2048 * 1792);
        bst_dev(u32x4{s0.x, s1.x, s2.x, s3.x}, ro, oo);
        bst_dev(u32x4{s0.y, s1.y, s2.y, s3.y}, ro, oo + 1792);
        if (j == 0 && tid == 0) st_epoch(a.ws + WS_EPS + ct * 256, tag);
    }
}

DI void warm_attn(const Args& a, LAS char* sm, int xcd, int hi) {
    const int lane = threadIdx.x & 63, wave8 = __builtin_amdgcn_readfirstlane(threadIdx.x >> 6);
    const int u = hi * 5 + wave8, idx = (u - 24) * 64 + lane;
    if (wave8 >= 5) return;
    setm0(sm + 163840 - 256);
    if (u < 24) {
        const int rbl = (xcd >> 1) * 4 + u / 6, kc = (2 * (rbl + (xcd & 1) * 4) + 1 + 2 * ((u % 6) >> 1)) % 7;
        touch_lds(mkdesc(a.x, 2048 * 896), (rbl * 128 + (u & 1) * 64 + lane) * 896 + kc * 128);
    } else touch_lds(mkdesc(a.wqkv, 448 * 2688), (idx >> 3) * 2688 + (1 + ((idx >> 2) & 1)) * 896 + ((xcd & 1) * 3 + (idx & 3)) * 128);
}

#if defined(__HIP_DEVICE_COMPILE__)
#define NOPK __attribute__((target("no-packed-fp32-ops")))
#else
#define NOPK
#endif
__global__ NOPK __launch_bounds__(512, 2) void fused_kernel(Args a) {
    __shared__ __attribute__((aligned(16))) char smem[163840];
    LAS char* sm = (LAS char*)smem;
    const int xcd = blockIdx.x & 7, j = blockIdx.x >> 3;
    const bool is_attn = (j & 1) == 0;
    if (__builtin_expect(!is_attn, 1)) warm_attn(a, sm, xcd, j >> 1);
    cbar();
    if (__builtin_expect(is_attn, 1)) attn_role(a, sm, xcd, j >> 1);
    else if (j < 28) ssm_role(a, sm, xcd * 14 + (j >> 1));
    else {
        const int t = (j >> 1 & 1) * 512 + threadIdx.x;
        setm0(sm);
#pragma unroll
        for (int i = 0; i < 2; ++i) touch_lds(mkdesc(a.wqkv, 448 * 2688), ((t + 1024 * i) >> 2) * 2688 + ((xcd & 1) * 3 + (t & 3)) * 128);
#pragma unroll
        for (int i = 0; i < 4; ++i) touch_lds(mkdesc(a.wo, 448 * 896), (t + 1024 * i) * 128);
        waitv<0>();
    }
}

extern "C" void kernel_launch(void* const* d_in, const int* in_sizes, int n_in,
                              void* d_out, int out_size, void* d_ws, size_t ws_size,
                              hipStream_t stream) {
    Args a;
    a.u = (const u16*)d_in[0]; a.logA = (const u16*)d_in[1]; a.Aim = (const u16*)d_in[2];
    a.Cre = (const u16*)d_in[3]; a.Cim = (const u16*)d_in[4]; a.logdt = (const u16*)d_in[5];
    a.Dskip = (const u16*)d_in[6]; a.x = (const u16*)d_in[7]; a.wqkv = (const u16*)d_in[8];
    a.wo = (const u16*)d_in[9]; a.out = (u16*)d_out; a.ws = (char*)d_ws;
    fused_kernel<<<256, 512, 0, stream>>>(a);
}
```

```cpp
#include <hip/hip_runtime.h>
#include <stdint.h>

typedef unsigned short u16;
typedef unsigned int u32;
typedef unsigned long long u64;
typedef __bf16 bf16x8 __attribute__((ext_vector_type(8)));
typedef __bf16 bf16x2 __attribute__((ext_vector_type(2)));
typedef _Float16 h2 __attribute__((ext_vector_type(2)));
typedef _Float16 h8 __attribute__((ext_vector_type(8)));
typedef short s16x4 __attribute__((ext_vector_type(4)));
typedef float f32x2 __attribute__((ext_vector_type(2)));
typedef float f32x4 __attribute__((ext_vector_type(4)));
typedef float f32x16 __attribute__((ext_vector_type(16)));
typedef u32 u32x2 __attribute__((ext_vector_type(2)));
typedef u32 u32x4 __attribute__((ext_vector_type(4)));
typedef __amdgpu_buffer_rsrc_t rsrc_t;
#define LAS __attribute__((address_space(3)))
#define GAS __attribute__((address_space(1)))
#define DI __device__ __forceinline__
#define SB() __builtin_amdgcn_sched_barrier(0)
#define KEEP(x) asm volatile("" :: "v"(x))
#define PERM __builtin_amdgcn_perm

#define STAMP(i) do { if ((i) == 0) stamp_rt = (u32)__builtin_amdgcn_s_memrealtime(); } while (0)
#define STAMP_DECL u32 stamp_rt = 0, dbgv_
#define DBG(i) dbgv_
#define STAMP_FLUSH(slot) do { if ((slot) == 18 && threadIdx.x < 64) { u32 end_ = stamp_rt + 4000u; while ((int)((u32)__builtin_amdgcn_s_memrealtime() - end_) < 0) __builtin_amdgcn_s_sleep(1); } } while (0)
constexpr u32 WS_DBG = 16u << 20;

constexpr u32 WS_EPA = 0;
constexpr u32 WS_EPS = 256;
constexpr u32 WS_YC  = 65536;
constexpr u32 WS_KVP = 524288;
constexpr u32 WS_OX  = WS_KVP + (1u << 19);
constexpr u32 WS_YX  = WS_OX + (2u << 20);
constexpr u32 WS_END = WS_YX + (2u << 20);
constexpr u32 OOB = 0x80000000u;
constexpr int SPIN_CAP = 1 << 16;

struct Args {
    const u16 *u, *logA, *Aim, *Cre, *Cim, *logdt, *Dskip, *x, *wqkv, *wo;
    u16* out;
    char* ws;
};

DI f32x4 mfma(u32x4 a, u32x4 b, f32x4 c) { return __builtin_amdgcn_mfma_f32_16x16x32_bf16(__builtin_bit_cast(bf16x8, a), __builtin_bit_cast(bf16x8, b), c, 0, 0, 0); }
DI f32x4 mfmah(u32x4 a, u32x4 b, f32x4 c) { return __builtin_amdgcn_mfma_f32_16x16x32_f16(__builtin_bit_cast(h8, a), __builtin_bit_cast(h8, b), c, 0, 0, 0); }
DI f32x16 mfma32(u32x4 a, u32x4 b, f32x16 c) { return __builtin_amdgcn_mfma_f32_32x32x16_bf16(__builtin_bit_cast(bf16x8, a), __builtin_bit_cast(bf16x8, b), c, 0, 0, 0); }
DI u32 pk(float lo, float hi) { f32x2 v = {lo, hi}; return __builtin_bit_cast(u32, __builtin_convertvector(v, bf16x2)); }
DI float bflo(u32 v) { return __uint_as_float(v << 16); }
DI float bfhi(u32 v) { return __uint_as_float(v & 0xffff0000u); }
DI f32x2 unpk(u32 v) { return f32x2{bflo(v), bfhi(v)}; }
DI f32x4 asf(u32x4 v) { return __builtin_bit_cast(f32x4, v); }
DI u32x4 asu(f32x4 v) { return __builtin_bit_cast(u32x4, v); }
DI f32x4 zero4() { return f32x4{0.f, 0.f, 0.f, 0.f}; }
DI f32x16 zero16() { f32x16 z; for (int i = 0; i < 16; ++i) z[i] = 0.f; return z; }
DI u32x4 pk8(const f32x16& a, int s) { return u32x4{pk(a[8 * s], a[8 * s + 1]), pk(a[8 * s + 2], a[8 * s + 3]), pk(a[8 * s + 4], a[8 * s + 5]), pk(a[8 * s + 6], a[8 * s + 7])}; }
DI u32x4 quad(const f32x16& a, int r) { return asu(f32x4{a[4 * r], a[4 * r + 1], a[4 * r + 2], a[4 * r + 3]}); }
template <class T> DI void pin(T& v) { asm volatile("" : "+v"(v) :: "memory"); }
template <int I> struct IC { static constexpr int v = I; };
template <int N, int I = 0, class F> DI void sfor(F f) { if constexpr (I < N) { f(IC<I>{}); sfor<N, I + 1>(f); } }
DI void cbar() { asm volatile("" ::: "memory"); }
DI void raw_barrier() { asm volatile("s_barrier" ::: "memory"); }
DI void wg_barrier() { asm volatile("s_waitcnt lgkmcnt(0)\n\ts_barrier" ::: "memory"); }
template <int N> DI void waitv() { asm volatile("s_waitcnt vmcnt(%0)" :: "i"(N) : "memory"); }
template <int N> DI void waitx(u32x4* x) { asm volatile("s_waitcnt vmcnt(%4)" : "+v"(x[0]), "+v"(x[1]), "+v"(x[2]), "+v"(x[3]) : "i"(N) : "memory"); }

DI u32x4 lds128(LAS char* p) { return *(LAS u32x4*)p; }
DI void sts128(LAS char* p, u32x4 v) { *(LAS u32x4*)p = v; }
DI void sts64(LAS char* p, u32 lo, u32 hi) { u32x2 v = {lo, hi}; *(LAS u32x2*)p = v; }
DI u32x4 trfrag(LAS char* p0, LAS char* p1) {
    u32x2 a = __builtin_bit_cast(u32x2, __builtin_amdgcn_ds_read_tr16_b64_v4i16((LAS s16x4*)p0));
    u32x2 b = __builtin_bit_cast(u32x2, __builtin_amdgcn_ds_read_tr16_b64_v4i16((LAS s16x4*)p1));
    return u32x4{a.x, a.y, b.x, b.y};
}

DI rsrc_t mkrsrc(const void* p, u32 bytes) { return __builtin_amdgcn_make_buffer_rsrc((void*)p, 0, bytes, 0x00020000); }
DI u32x4 mkdesc(const void* p, u32 bytes) { u64 v = (u64)p; return u32x4{(u32)v, (u32)(v >> 32) & 0xffffu, bytes, 0x00020000u}; }
DI u32x4 bld_dev(rsrc_t r, u32 voff, u32 soff) { return __builtin_amdgcn_raw_buffer_load_b128(r, voff, soff, 16); }
DI u32 bld32_dev(rsrc_t r, u32 voff, u32 soff) { return __builtin_amdgcn_raw_buffer_load_b32(r, voff, soff, 16); }
DI void bst_dev(u32x4 v, rsrc_t r, u32 voff) { __builtin_amdgcn_raw_buffer_store_b128(v, r, voff, 0, 16); }
DI void bst32_dev(u32 v, rsrc_t r, u32 voff) { __builtin_amdgcn_raw_buffer_store_b32(v, r, voff, 0, 16); }
DI void st_epoch(char* p, u32 v) { __hip_atomic_store((GAS u32*)p, v, __ATOMIC_RELAXED, __HIP_MEMORY_SCOPE_AGENT); }
DI void setm0(LAS char* dst) { asm volatile("s_mov_b32 m0, %0\n\ts_nop 0" :: "s"((u32)(uintptr_t)dst) : "memory"); }
template <int OFF>
DI void dma(u32x4 desc, u32 voff, u32 soff) { asm volatile("buffer_load_dwordx4 %0, %1, %2 offen offset:%3 lds" :: "v"(voff), "s"(desc), "s"(soff), "i"(OFF) : "memory"); }
DI void ldd(u32x4& v, u32x4 desc, u32 voff, u32 soff) { asm volatile("buffer_load_dwordx4 %0, %1, %2, %3 offen sc1" : "=v"(v) : "v"(voff), "s"(desc), "s"(soff) : "memory"); }
DI u32 ld_epoch(const char* p) { u32 v; asm volatile("global_load_dword %0, %1, %2 sc1" : "=v"(v) : "v"(0u), "s"(p) : "memory"); return v; }
DI u32 touch(u32x4 desc, u32 voff) { u32 v; asm volatile("buffer_load_dword %0, %1, %2, 0 offen" : "=v"(v) : "v"(voff), "s"(desc) : "memory"); return v; }
DI void touch_lds_dev(u32x4 desc, u32 voff) { asm volatile("buffer_load_dword %0, %1, 0 offen sc1 lds" :: "v"(voff), "s"(desc) : "memory"); }
DI void touch_lds(u32x4 desc, u32 voff) { asm volatile("buffer_load_dword %0, %1, 0 offen lds" :: "v"(voff), "s"(desc) : "memory"); }
template <int BIT> DI void reld(u32x4& v, u32x4 desc, u32 voff, u32 soff, u32 mask) {
    asm volatile("s_bitcmp1_b32 %4, %5\n\ts_cbranch_scc0 1f\n\tbuffer_load_dwordx4 %0, %1, %2, %3 offen sc1\n1:"
                 : "+v"(v) : "v"(voff), "s"(desc), "s"(soff), "s"(mask), "i"(BIT) : "scc", "memory");
}
template <int BIT> DI void reld32(u32& v, u32x4 desc, u32 voff, u32 soff, u32 mask) {
    asm volatile("s_bitcmp1_b32 %4, %5\n\ts_cbranch_scc0 1f\n\tbuffer_load_dword %0, %1, %2, %3 offen sc1\n1:"
                 : "+v"(v) : "v"(voff), "s"(desc), "s"(soff), "s"(mask), "i"(BIT) : "scc", "memory");
}
template <int N, class OK, class LD, class PIN>
DI void settle(u32 bad, OK ok, LD ld, PIN pn) {
#pragma unroll 1
    for (int sp = 0; bad != 0 && sp < SPIN_CAP; ++sp) {
        __builtin_amdgcn_s_sleep(2);
        sfor<N>([&](auto i) { ld(i, bad); });
        waitv<0>();
        sfor<N>([&](auto i) { pn(i.v); });
        sfor<N>([&](auto i) { if ((bad >> i.v & 1) && __all(ok(i.v))) bad &= ~(1u << i.v); });
    }
}
template <int N, class OK> DI u32 badmask(OK ok) { u32 bad = 0; sfor<N>([&](auto i) { if (!__all(ok(i.v))) bad |= 1u << i.v; }); return bad; }

DI u32 sum4(u32x4 v) { return v.x + v.y + v.z + v.w; }
DI u32 rowsum(u32 s) {
    s += __builtin_amdgcn_update_dpp(0, s, 0x128, 0xf, 0xf, false);
    s += __builtin_amdgcn_update_dpp(0, s, 0x124, 0xf, 0xf, false);
    s += __builtin_amdgcn_update_dpp(0, s, 0x122, 0xf, 0xf, false);
    s += __builtin_amdgcn_update_dpp(0, s, 0x121, 0xf, 0xf, false);
    return s;
}
DI u32 sum32(u32 s) { s = rowsum(s); auto t = __builtin_amdgcn_permlane16_swap(s, s, false, false); return t[0] + t[1]; }
DI u32 halfsum(u32 s) { auto r = __builtin_amdgcn_permlane32_swap(s, s, false, false); return r[0] + r[1]; }
DI u32 wavemax(u32 s) {
    s = max(s, (u32)__builtin_amdgcn_update_dpp(0, s, 0x128, 0xf, 0xf, false));
    s = max(s, (u32)__builtin_amdgcn_update_dpp(0, s, 0x124, 0xf, 0xf, false));
    s = max(s, (u32)__builtin_amdgcn_update_dpp(0, s, 0x122, 0xf, 0xf, false));
    s = max(s, (u32)__builtin_amdgcn_update_dpp(0, s, 0x121, 0xf, 0xf, false));
    auto r = __builtin_amdgcn_permlane32_swap(s, s, false, false);
    s = max(r[0], r[1]);
    auto t = __builtin_amdgcn_permlane16_swap(s, s, false, false);
    return max(t[0], t[1]);
}

DI float phi(float t) {
    const float e = __builtin_amdgcn_exp2f(t * 1.44269504089f);
    return __int_as_float(max(__float_as_int(t), 0x358637bd)) + __builtin_amdgcn_fmed3f(e, 0.f, 1.f);
}

constexpr int A_RED = 0, A_WO = 32768, A_WQ = 98304, A_KVB = 155648;
constexpr int PRO = 8;
constexpr int LA = 6;
constexpr int QS0 = 40;
constexpr int GK0 = 14;
constexpr int G2A = 2;
constexpr int issued_before(int gs) {
    constexpr int B0 = 16 - LA;
    int n = PRO + gs;
    int cap = 16 + (gs < B0 ? 0 : 4 * ((gs - B0) / 8) + ((gs - B0) % 8 < 4 ? (gs - B0) % 8 : 4));
    if (gs > B0 + 20) cap = 29;
    n = n < cap ? n : cap;
    if (n < 29) return n;
    const int q = gs - QS0;
    return 29 + (q < 0 ? 0 : q > 7 ? 7 : q);
}
static_assert(QS0 >= 40 - LA, "Wq overwrites ring slot 3");
constexpr int slot_ch(int gs) { return gs < 40 ? gs >> 3 : 5 + ((gs >> 2) & 1); }
constexpr int slot_kk(int gs) { return gs < 40 ? (gs & 7) >> 1 : gs & 3; }
constexpr int slot_sg(int gs) { return gs < 40 ? gs & 1 : gs >= 48; }

DI void attn_role(const Args& a, LAS char* sm, const int xcd, const int j) {
    STAMP_DECL;
    const int tid = threadIdx.x, lane = tid & 63, wave = __builtin_amdgcn_readfirstlane(tid >> 6);
    const int rw4 = wave & 3, hs = wave >> 2;
    const int hp = (xcd & 1) * 4 + (j & 3), rb = (xcd >> 1) * 4 + (j >> 2);
    const int h = 2 * hp + hs;
    const int c0 = (2 * rb + 2 * hp) % 7;
    const int r32 = lane & 31, hh = lane >> 5, blk = (lane >> 4) & 1, q = (lane >> 2) & 3, p = lane & 3;
    const u32x4 rx = mkdesc(a.x, 2048 * 896), rw = mkdesc(a.wqkv, 448 * 2688), rwo = mkdesc(a.wo, 448 * 896), dws = mkdesc(a.ws, WS_END);
    const rsrc_t rws = mkrsrc(a.ws, WS_END);
    STAMP(0);
    u32 tag = ld_epoch(a.ws + WS_EPA), tch = 0;
    const int widx = (lane & 15) ^ ((lane >> 4) << 2);
    const u32 wv = (8 * wave + (lane >> 4)) * 2688 + (1 + (widx >> 3)) * 896 + hp * 112 + min(widx & 7, 6) * 16;
    const u32 xv0 = (rb * 128 + wave * 16 + (lane >> 3)) * 896 + (((lane & 7) ^ (lane >> 4)) * 16);
    const u32 xv1 = (rb * 128 + wave * 16 + 8 + (lane >> 3)) * 896 + (((lane & 7) ^ (4 + (lane >> 4))) * 16) - 1024;
    const u32 qv = (8 * wave + (lane >> 3)) * 2688 + hp * 112 + min((lane & 7) ^ (blk << 2), 6) * 16;
    auto kchunk = [&](int i) { int kc = i + c0; return kc >= 7 ? kc - 7 : kc; };
    auto ldn = [&](int n) {
        if (n >= 29) { setm0(sm + A_WQ + (n - 29) * 8192 + wave * 1024); dma<0>(rw, qv, kchunk(n - 29) * 64 * 2688); return; }
        if (n == 28) {
            tch = touch(dws, OOB);
            return;
        }
        const int i = n >> 2, m = n & 3;
        if (m == 0) { setm0(sm + (i & 3) * 32768 + 16384 + wave * 2048); dma<0>(rx, xv0, kchunk(i) * 128); }
        if (m == 1) dma<1024>(rx, xv1, kchunk(i) * 128);
        if (m == 2) { setm0(sm + (i & 3) * 32768 + wave * 2048); dma<0>(rw, wv, kchunk(i) * 64 * 2688); }
        if (m == 3) dma<1024>(rw, wv, kchunk(i) * 64 * 2688 + 4 * 2688 - 1024);
    };
#pragma unroll
    for (int n = 0; n < PRO; ++n) ldn(n);

    const int wb = 56 * hs + 32 * blk + 8 * p;
    LAS char *wsrc[2], *xsrc[4];
#pragma unroll
    for (int sg = 0; sg < 2; ++sg) wsrc[sg] = sm + (32 * hh + q) * 256 + (((2 * sg + (wb >> 6)) ^ q) << 6) + (wb & 63);
#pragma unroll
    for (int kk = 0; kk < 4; ++kk) xsrc[kk] = sm + 16384 + (32 * rw4 + r32) * 128 + (((4 * hh + kk) ^ ((r32 >> 1) & 7)) * 16);

    const bool p3 = j < 14;
    const int r128 = p3 ? xcd * 2 + j / 7 : 0, nb = j % 7;
    f32x16 acc[2] = {zero16(), zero16()};
    u32x4 xf[28], wf[56];
    auto rdw = [&](int gs) { LAS char* s = wsrc[slot_sg(gs)] + (slot_ch(gs) & 3) * 32768 + slot_kk(gs) * 2048; wf[gs] = trfrag(s, s + 1024); };
    auto rdx = [&](int i, int kk) { xf[4 * i + kk] = lds128(xsrc[kk] + (i & 3) * 32768); };
    STAMP(13);
    waitv<PRO - 1>();
    STAMP(12);
    waitv<PRO - 4>();
    STAMP(14);
    wg_barrier();
    STAMP(1);
#pragma unroll
    for (int sl = 0; sl < LA; ++sl) { if (sl < 4) rdx(0, sl); rdw(sl); }
    u32 pkd[8];
    u32 vm = r32 == 28 ? 0u : ~0u, vo = r32 == 28 ? 0x3f803f80u : 0u;
    pin(vm); pin(vo);
    sfor<56>([&](auto gs) {
        constexpr int issued = issued_before(gs.v);
        constexpr int fu = gs.v + LA, c = fu == 44 ? 6 : (fu <= 40 && (fu & 7) == 0) ? fu >> 3 : 0;
        static_assert(!c || issued >= 4 * c + 4, "chunk not issued yet");
        if constexpr (c != 0) { waitv<issued - (4 * c + 4)>(); raw_barrier(); }
        SB();
        acc[slot_sg(gs.v)] = mfma32(xf[4 * slot_ch(gs.v) + slot_kk(gs.v)], wf[gs.v], acc[slot_sg(gs.v)]);
        SB();
        if constexpr (fu < 40 && fu >= 8 && (fu & 1) == 0) rdx(fu >> 3, (fu & 7) >> 1);
        if constexpr (fu >= 40 && fu < 48) rdx(slot_ch(fu), fu & 3);
        if constexpr (fu < 56) rdw(fu);
        if constexpr (gs.v == 44) raw_barrier();
        if constexpr (gs.v >= 49) { pkd[gs.v - 49] = pk(phi(acc[0][2 * (gs.v - 49)]), phi(acc[0][2 * (gs.v - 49) + 1])); pin(pkd[gs.v - 49]); }
#pragma unroll
        for (int n = issued; n < issued_before(gs.v + 1); ++n) ldn(n);
        SB();
    });
    STAMP(2);
    {
        pkd[7] = pk(phi(acc[0][14]), phi(acc[0][15]));
        pin(pkd[7]);
        f32x16 kv = mfma32(u32x4{pkd[0], pkd[1], pkd[2], pkd[3]}, (pk8(acc[1], 0) & vm) | vo, zero16());
        SB();
        kv = mfma32(u32x4{pkd[4], pkd[5], pkd[6], pkd[7]}, (pk8(acc[1], 1) & vm) | vo, kv);
#pragma unroll
        for (int rq = 0; rq < 4; ++rq) sts128(sm + A_RED + wave * 4096 + rq * 1024 + lane * 16, quad(kv, rq));
    }
    asm volatile("s_waitcnt vmcnt(8)" : "+v"(tag) :: "memory");
    tag += 1;
    wg_barrier();
    u32 hm = hh ? 0u : ~0u, cm = r32 == 31 ? 0u : ~0u;
    pin(hm); pin(cm);
    const bool red = (rw4 >> 1) == hs;
    if (red) {
        LAS char* rs = sm + A_RED + hs * 16384 + (rw4 & 1) * 2048 + lane * 16;
        f32x4 lo = (asf(lds128(rs)) + asf(lds128(rs + 4096))) + (asf(lds128(rs + 8192)) + asf(lds128(rs + 12288)));
        f32x4 hi = (asf(lds128(rs + 1024)) + asf(lds128(rs + 5120))) + (asf(lds128(rs + 9216)) + asf(lds128(rs + 13312)));
        u32x4 v = {pk(lo.x, lo.y), pk(lo.z, lo.w), pk(hi.x, hi.y), pk(hi.z, hi.w) & cm};
        v.w |= (tag - sum32(sum4(v))) & ~cm;
        bst_dev(v, rws, WS_KVP + (h * 16 + rb) * 2048 + (tid & 127) * 16);
    }
    cbar();
    STAMP(3);
    if (red) waitv<1>(); else waitv<0>();
    KEEP(tch);
    raw_barrier();
    STAMP(4);
    const u32 ov = (8 * wave + (lane >> 3)) * 896 + nb * 128 + (((lane & 7) ^ (blk << 2)) * 16);
    u32x4 gk[8];
    const u32 kbase = WS_KVP + (h * 16 + (rw4 >> 1) * 8 + (rb & 7)) * 2048, kvo = (tid & 127) * 16;
    f32x16 aq = zero16();
    {
        LAS char* qsrc = sm + A_WQ + (32 * hh + q) * 128 + (((wb >> 6) ^ (q >> 1)) << 6) + (wb & 63);
        u32x4 wq[28];
        auto rdq = [&](int k) { LAS char* s = qsrc + (k >> 2) * 8192 + (k & 3) * 1024; wq[k] = trfrag(s, s + 512); };
#pragma unroll
        for (int k = 0; k < 4; ++k) rdq(k);
        SB();
#pragma unroll
        for (int k = 0; k < 28; ++k) {
            aq = mfma32(wq[k], xf[k], aq);
            SB();
            if (k + 4 < 28) rdq(k + 4);
            if (k < 7) { setm0(sm + A_WO + k * 8192 + wave * 1024); dma<0>(rwo, ov, k * 64 * 896); }
            if (k >= GK0 && k < GK0 + 8) ldd(gk[k - GK0], dws, kvo, kbase ^ ((k - GK0) * 2048));
            SB();
        }
    }
    STAMP(5);
    u32 pqd[8];
#pragma unroll
    for (int k = 0; k < 8; ++k) {
        pqd[k] = pk(phi(aq[2 * k]), phi(aq[2 * k + 1]));
        if (k >= 6) pqd[k] &= hm;
        pin(pqd[k]);
    }
    {
        waitx<0>(gk); waitx<0>(gk + 4);
        u32 t = 0;
#pragma unroll
        for (int i = 0; i < 8; ++i) t += sum4(gk[i]);
        if (!__all(sum32(t) == 8u * tag)) {
            auto ok = [&](int i) { return sum32(sum4(gk[i])) == tag; };
            settle<8>(badmask<8>(ok), ok, [&](auto i, u32 bm) { reld<i.v>(gk[i.v], dws, kvo, kbase ^ (i.v * 2048), bm); }, [&](int i) { pin(gk[i]); });
        }
    }
    STAMP(6);
    {
        u32x4 v;
#pragma unroll
        for (int c = 0; c < 4; ++c) {
            const float lo = ((bflo(gk[0][c]) + bflo(gk[1][c])) + (bflo(gk[2][c]) + bflo(gk[3][c]))) + ((bflo(gk[4][c]) + bflo(gk[5][c])) + (bflo(gk[6][c]) + bflo(gk[7][c])));
            const float hi = ((bfhi(gk[0][c]) + bfhi(gk[1][c])) + (bfhi(gk[2][c]) + bfhi(gk[3][c]))) + ((bfhi(gk[4][c]) + bfhi(gk[5][c])) + (bfhi(gk[6][c]) + bfhi(gk[7][c])));
            v[c] = pk(lo, hi);
        }
        sts128(sm + A_KVB + tid * 16, v);
    }
    wg_barrier();
    STAMP(7);
    {
        u32x4 kf[4];
#pragma unroll
        for (int i = 0; i < 4; ++i) kf[i] = lds128(sm + A_KVB + hs * 4096 + i * 1024 + lane * 16);
        const u32x4 pq0 = {pqd[0], pqd[1], pqd[2], pqd[3]}, pq1 = {pqd[4], pqd[5], pqd[6], pqd[7]};
        f32x16 oa = mfma32(kf[3], pq1, mfma32(kf[2], pq0, mfma32(kf[1], pq1, mfma32(kf[0], pq0, zero16()))));
        const u32 dn = __float_as_uint(oa[12]);
        auto r = __builtin_amdgcn_permlane32_swap(dn, dn, false, false);
        oa *= __builtin_amdgcn_rcpf(__uint_as_float(r[1]) + 1e-6f);
        const u32x4 f0 = pk8(oa, 0);
        const u32 o = WS_OX + ((rb * 4 + rw4) * 16 + h) * 2048 + lane * 16;
        bst_dev(f0, rws, o);
        SB();
        u32x4 f1 = pk8(oa, 1);
        f1.z &= hm; f1.w &= hm;
        f1.w |= (tag - halfsum(sum4(f0) + sum4(f1))) & ~hm;
        bst_dev(f1, rws, o + 1024);
    }
    cbar();
    STAMP(8);
    if (p3) {
        LAS char* osrc[2];
#pragma unroll
        for (int nt = 0; nt < 2; ++nt) osrc[nt] = sm + A_WO + (224 * hs + 4 * hh + q) * 128 + ((nt ^ (q >> 1)) * 64) + blk * 32 + 8 * p;
        const int padoff = hh ? 0 : 1024;
        u32x4 wf3[8][2][2];
#pragma unroll
        for (int k = 0; k < 32; ++k) {
            const int hl = k >> 2, ks = (k >> 1) & 1, nt = k & 1;
            LAS char* s = osrc[nt] + (hl * 28 + 16 * ks) * 128;
            wf3[hl][ks][nt] = trfrag(s, s + (ks ? padoff : 1024));
        }
        SB();
        waitv<0>();
        u32x4 go[16];
        const u32 ob0 = WS_OX + ((r128 * 4 + rw4) * 16 + 8 * hs) * 2048;
        auto ldo = [&](int i) { ldd(go[i], dws, lane * 16, ob0 + i * 1024); };
#pragma unroll
        for (int i = 0; i < 4 * G2A; ++i) ldo(i);
        STAMP(9);
        f32x16 ac[2];
        sfor<4>([&](auto gi) {
            constexpr int i = 4 * gi.v, nis = gi.v + G2A < 4 ? gi.v + G2A : 4;
            waitx<4 * (nis - gi.v - 1)>(go + i);
            {
                auto r0 = __builtin_amdgcn_permlane32_swap(sum4(go[i]) + sum4(go[i + 1]), sum4(go[i + 2]) + sum4(go[i + 3]), false, false);
                const u64 ne = __ballot(r0[0] + r0[1] != tag);
                if (ne)
                    settle<2>(((u32)ne != 0) | ((ne >> 32) != 0) << 1, [&](int k) { return halfsum(sum4(go[i + 2 * k]) + sum4(go[i + 2 * k + 1])) == tag; },
                              [&](auto k, u32 bm) { reld<k.v>(go[i + 2 * k.v], dws, lane * 16, ob0 + (i + 2 * k.v) * 1024, bm);
                                                    reld<k.v>(go[i + 2 * k.v + 1], dws, lane * 16, ob0 + (i + 2 * k.v + 1) * 1024, bm); },
                              [&](int k) { pin(go[i + 2 * k]); pin(go[i + 2 * k + 1]); });
            }
            if (gi.v == 3) STAMP(10);
            go[i + 1].w &= hm; go[i + 3].w &= hm;
            SB();
#pragma unroll
            for (int k = 0; k < 8; ++k) {
                const int hl = 2 * gi.v + (k >> 2), ks = (k >> 1) & 1, nt = k & 1;
                ac[nt] = mfma32(wf3[hl][ks][nt], go[2 * hl + ks], (gi.v == 0 && k < 2) ? zero16() : ac[nt]);
                SB();
                if ((k & 1) == 0 && gi.v + G2A < 4) ldo(4 * (gi.v + G2A) + (k >> 1));
                SB();
            }
        });
        LAS char* xb = sm + wave * 4096 + lane * 16;
        if (hs) {
#pragma unroll
            for (int rq = 0; rq < 4; ++rq) sts128(xb + rq * 1024, quad(ac[0], rq));
            ac[0] = ac[1];
        } else {
#pragma unroll
            for (int rq = 0; rq < 4; ++rq) sts128(xb + rq * 1024, quad(ac[1], rq));
        }
        wg_barrier();
        LAS char* pb = sm + (wave ^ 4) * 4096 + lane * 16;
        LAS char* ob = sm + A_WQ + rw4 * 4608;
#pragma unroll
        for (int rq = 0; rq < 4; ++rq) {
            const f32x4 v = asf(quad(ac[0], rq)) + asf(lds128(pb + rq * 1024));
            sts64(ob + r32 * 144 + hs * 64 + rq * 16 + hh * 8, pk(v.x, v.y), pk(v.z, v.w));
        }
        wg_barrier();
        const rsrc_t ro = mkrsrc(a.out, 2048 * 1792);
        const int orow = 16 * hs + (lane >> 3);
        const u32 oo = (r128 * 128 + rw4 * 32 + orow) * 1792 + 896 + nb * 128 + (lane & 7) * 16;
        const u32x4 v0 = lds128(ob + orow * 144 + (lane & 7) * 16), v1 = lds128(ob + (8 + orow) * 144 + (lane & 7) * 16);
        bst_dev(v0, ro, oo);
        bst_dev(v1, ro, oo + 8 * 1792);
        if (xcd == 0 && j == 0 && tid == 0) st_epoch(a.ws + WS_EPA, tag);
        STAMP(11);
    }
    STAMP_FLUSH(j * 8 + xcd);
}

constexpr int S_TAB = 0;
constexpr int S_KC = 34816;
constexpr int S_P = 47104;
constexpr int S_UT = 52480;
constexpr int S_TR = 61696;

struct c2 { f32x2 r, i; };
struct hc { h2 r, i; };
DI c2 csq(c2 a) { return c2{a.r * a.r - a.i * a.i, 2.f * a.r * a.i}; }
DI hc hmul(hc a, hc b) { return hc{a.r * b.r - a.i * b.i, a.r * b.i + a.i * b.r}; }
DI hc toh(c2 a) { return hc{__builtin_convertvector(a.r, h2), __builtin_convertvector(a.i, h2)}; }
DI c2 cexp2(f32x2 lr, f32x2 rev, float pw) {
    f32x2 e = lr * pw, f = rev * pw;
    f32x2 m = {__builtin_amdgcn_exp2f(e.x), __builtin_amdgcn_exp2f(e.y)};
    f.x = __builtin_amdgcn_fractf(f.x); f.y = __builtin_amdgcn_fractf(f.y);
    f32x2 c = {__builtin_amdgcn_cosf(f.x), __builtin_amdgcn_cosf(f.y)}, s = {__builtin_amdgcn_sinf(f.x), __builtin_amdgcn_sinf(f.y)};
    return c2{m * c, m * s};
}
constexpr int tri_a(int sl) { int a = 0; while (sl >= 8 - a) { sl -= 8 - a; ++a; } return a; }
constexpr int tri_b(int sl) { int a = 0; while (sl >= 8 - a) { sl -= 8 - a; ++a; } return sl; }

DI void ssm_role(const Args& a, LAS char* sm0, const int sb) {
    STAMP_DECL;
    const int tid = threadIdx.x & 255, lane = tid & 63, wave8 = __builtin_amdgcn_readfirstlane(threadIdx.x >> 6), wave = wave8 & 3;
    LAS char* sm = sm0 + (wave8 >> 2) * 81920;
    const int ch = wave >> 1, hf = wave & 1, d = 4 * sb + (wave8 >> 1);
    const int g = lane >> 4, r16 = lane & 15;
    STAMP(0);
    const u32 po = (d * 128 + hf * 64 + lane) * 4;
    const u32 ldt = a.logdt[d];
    const u32 pA = *(const u32*)((const char*)a.logA + po), pI = *(const u32*)((const char*)a.Aim + po);
    const u32 pCr = *(const u32*)((const char*)a.Cre + po), pCi = *(const u32*)((const char*)a.Cim + po);
    const u32x2 dsk = ((const u32x2*)a.Dskip)[sb];
    cbar();
    const int G8 = (wave8 + sb) & 7, trow = 256 * G8 + 16 * r16 + 4 * g;
    const rsrc_t ru = mkrsrc(a.u, 2048 * 896);
    u32x2 uu[4];
    auto ldu = [&](int i) { SB(); uu[i] = __builtin_amdgcn_raw_buffer_load_b64(ru, trow * 896 + sb * 8 + i * 896, 0, 0); cbar(); SB(); };
    const int sidx = 2 * sb + (wave8 >> 2);
    const int ct = sidx >> 5, j = sidx & 31;
    u32 tag = ld_epoch(a.ws + WS_EPS + ct * 256);
    *(LAS u32*)(sm + S_UT + (tid >> 7) * 4608 + (tid & 127) * 4) = 0;
    const int w = 16 + r16 - 8 * g;
    u32x4 bE, bO, mkb, mkd;
    u32 sel[3][4], msk[4], pb[3];
    {
        u32 y[4];
#pragma unroll
        for (int e = 0; e < 4; ++e) {
            y[e] = (u32)(w + 64 - 2 * e) * 0x10001u - 0x10000u;
            msk[e] = hf ? 0u : (((y[e] >> 6) & 0x10001u) ^ 0x10001u) * 0xffffu;
        }
        pb[0] = w - 7;
#pragma unroll
        for (int s = 1; s < 3; ++s) {
            u32 r[4];
#pragma unroll
            for (int e = (s == 1 ? 2 : 0); e < 4; ++e)
                r[e] = ((y[e] + ((1u << (s - 1)) - 1u) * 0x10001u + ((y[e] >> s) & 0x10001u)) >> s) & 0x00ff00ffu;
            const u32 base = r[3] >> 16;
            pb[s] = base - (64 >> s);
#pragma unroll
            for (int e = (s == 1 ? 2 : 0); e < 4; ++e) sel[s][e] = (((r[e] - base * 0x10001u) ^ 0x10001u) & 0xfffffu) * 0x202u + 0x01000100u;
        }
#pragma unroll
        for (int e = 0; e < 4; ++e) {
            const int z = min(max(16 - w + 2 * e, 0), 20);
            auto two = [&](u32 m) { const u32 b2 = (m >> z) & 3u; return (b2 | (b2 << 15)) & 0x10001u; };
            bE[e] = two(0x1ff0u) * 0x3f80u; bO[e] = two(0x0fe0u) * 0x3f80u; mkb[e] = two(0x1f000u) * 0xffffu;
            mkd[e] = r16 - 4 * g - e >= 0 ? ~0u : 0u;
        }
        asm volatile("" : "+v"(sel[1][2]), "+v"(sel[1][3]), "+v"(sel[2][0]), "+v"(sel[2][1]), "+v"(sel[2][2]), "+v"(sel[2][3]), "+v"(bE), "+v"(bO), "+v"(mkb), "+v"(mkd));
        asm volatile("" : "+v"(msk[0]), "+v"(msk[1]), "+v"(msk[2]), "+v"(msk[3]));
    }
    LAS u32* const R = (LAS u32*)(sm + S_P + ch * 2688) + 655;
    LAS u32 *P0 = R - (128 * hf + (int)pb[0]) - 102, *P1 = R - (256 + 64 * hf + (int)pb[1]) - 132, *P2 = R - (384 + 32 * hf + (int)pb[2]) - 130,
            *P3 = R - (385 + 2 * (r16 - 4 * g)) - 224;
    LAS char* ubase = sm + S_UT + ch * 4608 + 480 + r16 * 32 + g * 16 - 256 * hf - 192;
    asm volatile("" : "+v"(P0), "+v"(P1), "+v"(P2), "+v"(P3), "+v"(ubase));
    LAS char* tab = sm + S_TAB + wave * 8704;
    LAS char* tw = tab + lane * 8;
    LAS char* tr = tab + r16 * 544 + g * 16;
    LAS char* kc = sm + S_KC + wave * 3072;
    SB();
#define EMIT1(row, v) sts64(tw + (row) * 544, __builtin_bit_cast(u32, (v).r), __builtin_bit_cast(u32, (v).i))
#define EMIT2(row, v0, v1) do { SB(); EMIT1(row, v0); EMIT1((row) + 1, v1); SB(); } while (0)
#define KRD() do { SB(); _Pragma("unroll") for (int ks = 0; ks < 8; ++ks) gf[ks] = lds128(tr + ks * 64); cbar(); SB(); } while (0)
#define KM2(k) do { ka = mfmah(gf[2 * (k)], ff[2 * (k)], (k) ? ka : zero4()); ka = mfmah(gf[2 * (k) + 1], ff[2 * (k) + 1], ka); } while (0)
    const float L2E = 1.44269504089f;
    const float edt = __builtin_amdgcn_exp2f(bflo(ldt) * L2E);
    const float dt = bflo(pk(edt, edt));
    f32x2 lr = unpk(pA) * L2E;
    lr = unpk(pk(__builtin_amdgcn_exp2f(lr.x), __builtin_amdgcn_exp2f(lr.y))) * dt * -L2E;
    const f32x2 rev = unpk(pI) * dt * 0.15915494309f;
    hc W[8], F[16], G[17], H, t0, t1;
    u32x4 ff[8], gf[8];
    f32x4 ka;
    c2 e = cexp2(lr, -rev, 8.f);
    F[1] = toh(e); e = csq(e); F[2] = toh(e);
    F[0] = hc{h2{1.f16, 1.f16}, h2{0.f16, 0.f16}};
    EMIT2(0, F[0], F[1]);
    e = csq(e); F[4] = toh(e); F[3] = hmul(F[1], F[2]);
    EMIT2(2, F[2], F[3]);
    e = csq(e); F[8] = toh(e); F[5] = hmul(F[4], F[1]);
    EMIT2(4, F[4], F[5]);
    F[6] = hmul(F[4], F[2]); F[7] = hmul(F[4], F[3]); F[9] = hmul(F[8], F[1]);
    EMIT2(6, F[6], F[7]);
    STAMP(1);
    {
        const int xcd = sb / 14, un = ((sb % 14) * 3 + wave8) * 64 + lane, v = un - 512, h0 = (xcd & 1) * 8, r0 = (xcd >> 1) * 4;
        setm0(sm0 + 163840 - 256);
        touch_lds_dev(mkdesc(a.ws, WS_END), wave8 >= 3 || un >= 2560 ? OOB
                      : un < 512 ? WS_KVP + ((h0 + (un >> 6)) * 16 + r0) * 2048 + (un & 63) * 128
                                 : WS_OX + ((r0 * 4 + (v >> 7)) * 16 + h0) * 2048 + (v & 127) * 128);
    }
    EMIT2(8, F[8], F[9]);
    F[10] = hmul(F[8], F[2]); F[11] = hmul(F[8], F[3]);
    e = cexp2(lr, rev, 1.f); W[1] = toh(e);
    EMIT2(10, F[10], F[11]);
    F[12] = hmul(F[8], F[4]); F[13] = hmul(F[8], F[5]);
    e = csq(e); W[2] = toh(e);
    EMIT2(12, F[12], F[13]);
    F[14] = hmul(F[8], F[6]); F[15] = hmul(F[8], F[7]);
    e = csq(e); W[4] = toh(e);
    EMIT2(14, F[14], F[15]);
    u32 ce = max(max(pCr & 0x7fffu, (pCr >> 16) & 0x7fffu), max(pCi & 0x7fffu, (pCi >> 16) & 0x7fffu));
    ce = min(max(wavemax(ce) >> 7, 1u), 253u);
    const float cdn = __uint_as_float((254u - ce) << 23), cup = __uint_as_float(ce << 23) * (2.f * dt);
    G[0] = toh(c2{unpk(pCr) * cdn, unpk(pCi) * cdn});
    t1 = hmul(G[0], W[1]);
    SB();
#pragma unroll
    for (int ks = 0; ks < 8; ++ks) ff[ks] = lds128(tr + ks * 64);
    cbar();
    ldu(0);
    EMIT2(0, G[0], t1);
    W[3] = hmul(W[1], W[2]); t0 = hmul(G[0], W[2]); t1 = hmul(G[0], W[3]);
    EMIT2(2, t0, t1);
    W[5] = hmul(W[4], W[1]); t0 = hmul(G[0], W[4]); t1 = hmul(G[0], W[5]);
    EMIT2(4, t0, t1);
    W[6] = hmul(W[4], W[2]); W[7] = hmul(W[4], W[3]); t0 = hmul(G[0], W[6]); t1 = hmul(G[0], W[7]);
    ldu(1);
    EMIT2(6, t0, t1);
    e = cexp2(lr, rev, 128.f); H = toh(e);
    G[1] = hmul(G[0], H); t1 = hmul(G[1], W[1]);
    EMIT2(8, G[1], t1);
    t0 = hmul(G[1], W[2]); t1 = hmul(G[1], W[3]);
    e = csq(e); H = toh(e);
    EMIT2(10, t0, t1);
    t0 = hmul(G[1], W[4]); t1 = hmul(G[1], W[5]); G[2] = hmul(G[0], H);
    ldu(2);
    EMIT2(12, t0, t1);
    t0 = hmul(G[1], W[6]); t1 = hmul(G[1], W[7]); G[3] = hmul(G[1], H);
    EMIT2(14, t0, t1);
    t1 = hmul(G[2], W[2]);
    e = csq(e); H = toh(e);
    KRD();
    EMIT2(0, G[2], t1);
    t0 = hmul(G[2], W[4]); t1 = hmul(G[2], W[6]); G[4] = hmul(G[0], H);
    ldu(3);
    EMIT2(2, t0, t1);
    t1 = hmul(G[3], W[2]); G[5] = hmul(G[1], H); KM2(0);
    EMIT2(4, G[3], t1);
    t0 = hmul(G[3], W[4]); t1 = hmul(G[3], W[6]); KM2(1);
    EMIT2(6, t0, t1);
    t1 = hmul(G[4], W[4]); G[6] = hmul(G[2], H); KM2(2);
    EMIT2(8, G[4], t1);
    t1 = hmul(G[5], W[4]); G[7] = hmul(G[3], H); KM2(3);
    EMIT2(10, G[5], t1);
    STAMP(2);
    t1 = hmul(G[6], W[4]);
    e = csq(e); H = toh(e);
    EMIT2(12, G[6], t1);
    t1 = hmul(G[7], W[4]); G[8] = hmul(G[0], H);
    sts128(kc + (128 * (g >> 1) + 4 * (g & 1) + 8 * r16) * 4, asu(ka * cup));
    EMIT2(14, G[7], t1);
    G[9] = hmul(G[1], H); G[10] = hmul(G[2], H);
    KRD();
    EMIT2(0, G[8], G[9]);
    G[11] = hmul(G[3], H); G[12] = hmul(G[4], H); G[13] = hmul(G[5], H);
    EMIT2(2, G[10], G[11]);
    G[14] = hmul(G[6], H); KM2(0); KM2(1);
    EMIT2(4, G[12], G[13]);
    G[15] = hmul(G[7], H); G[16] = hmul(G[8], H); KM2(2); KM2(3);
    EMIT2(6, G[14], G[15]);
    EMIT1(8, G[16]);
    KRD();
    {
        LAS char* k0 = kc + (g < 2 ? 256 + 64 * g + 4 * r16 : 384 + 64 * (g - 2) + 2 * r16) * 4;
        const u32x4 kb = asu(ka * cup);
        sts64(k0, kb.x, kb.y);
        sts64(k0 + (g < 2 ? 8 : 128), kb.z, kb.w);
    }
    SB();
    KM2(0); KM2(1); KM2(2); KM2(3);
    {
        LAS char* ud = sm0 + S_UT + 512 + trow * 2;
        sts64(ud, PERM(uu[1].x, uu[0].x, 0x05040100u), PERM(uu[3].x, uu[2].x, 0x05040100u));
        sts64(ud + 4608, PERM(uu[1].x, uu[0].x, 0x07060302u), PERM(uu[3].x, uu[2].x, 0x07060302u));
        sts64(ud + 81920, PERM(uu[1].y, uu[0].y, 0x05040100u), PERM(uu[3].y, uu[2].y, 0x05040100u));
        sts64(ud + 81920 + 4608, PERM(uu[1].y, uu[0].y, 0x07060302u), PERM(uu[3].y, uu[2].y, 0x07060302u));
    }
    {
        LAS float* k0 = (LAS float*)(kc + (512 + 64 * g + r16) * 4);
        k0[0] = ka.x * cup; k0[16] = ka.y * cup; k0[32] = ka.z * cup; k0[48] = ka.w * cup;
    }
    wg_barrier();
    STAMP(3);
    const rsrc_t rws = mkrsrc(a.ws, WS_END);
    const u32x4 dws = mkdesc(a.ws, WS_END);
    u32 tch = touch(dws, lane < 16 ? WS_YX + (2 * sb + (lane >> 3)) * 8192 + G8 * 1024 + (lane & 7) * 128 : lane == 16 ? WS_YC + (sb * 8 + G8) * 128 : OOB);
    u32x4 U[2][8];
    auto rdU = [&](int it, int bq) { U[it & 1][bq] = lds128(ubase + 512 * bq + 192 - 64 * it); };
    {
        LAS char* k0 = sm + S_KC + ch * 6144;
        LAS char* Rb = sm + S_P + ch * 2688;
        const int t = tid & 127, e1 = min(t + 128, 167);
        f32x4 a0 = asf(lds128(k0 + t * 16)), b0 = asf(lds128(k0 + 3072 + t * 16)), a1 = asf(lds128(k0 + e1 * 16)), b1 = asf(lds128(k0 + 3072 + e1 * 16));
        const float n0 = *(LAS float*)(k0 + t * 16 + 16) + *(LAS float*)(k0 + 3072 + t * 16 + 16);
        const float n1 = *(LAS float*)(k0 + e1 * 16 + 16) + *(LAS float*)(k0 + 3072 + e1 * 16 + 16);
#pragma unroll
        for (int bq = 0; bq < 8; ++bq) rdU(3, bq);
        a0 += b0; a1 += b1;
        float p0 = a0.x, p1 = a0.y, p2 = a0.z, p3 = a0.w, q0 = a1.x, q1 = a1.y, q2 = a1.z, q3 = a1.w;
        asm volatile("" : "+v"(p0), "+v"(p1), "+v"(p2), "+v"(p3), "+v"(q0), "+v"(q1), "+v"(q2), "+v"(q3));
        sts128(Rb + (163 - t) * 16, u32x4{pk(n0, p3), pk(p3, p2), pk(p2, p1), pk(p1, p0)});
        u32x4 v1 = {pk(n1, q3), pk(q3, q2), pk(q2, q1), pk(q1, q0)};
        if (t >= 36) v1 = u32x4{0, 0, 0, 0};
        if (t < 39) sts128(Rb + (t < 36 ? 35 - t : 129 + t) * 16, v1);
        if (t == 0) sts128(Rb + 164 * 16, u32x4{pk(p0, 0.f), 0, 0, 0});
    }
    wg_barrier();
    STAMP(4);
    f32x4 y[8];
    {
#define Q0(i) P0[102 - (i)]
#define Q1(i) P1[132 - (i)]
#define Q2(i) P2[130 - (i)]
#define Q3(i) P3[224 - (i)]
        u32x4 T[2][4], T32, TB[4], UW[4], WA[2];
        f32x4 wd[4];
        u32 raw[7], rx[10];
        auto rdT = [&](int it, int k) {
            u32x4& f0 = T[it & 1][0];
            if (k == 0) { f0.x = Q0(32 * it + 6); f0.y = Q0(32 * it + 4); }
            if (k == 1) { f0.z = Q0(32 * it + 2); f0.w = Q0(32 * it); }
            if (k == 2) { raw[0] = Q1(16 * it); raw[1] = Q1(16 * it + 2); raw[2] = Q1(16 * it + 4); }
            if (k == 3) { raw[3] = Q2(8 * it); raw[4] = Q2(8 * it + 2); }
            if (k == 4) { raw[5] = Q2(8 * it + 64); raw[6] = Q2(8 * it + 66); }
        };
        auto mkT = [&](int it, int aa, int e) {
            T[it & 1][aa][e] = aa == 1 ? (e < 2 ? PERM(raw[2], raw[1], sel[1][2 + e]) : PERM(raw[1], raw[0], sel[1][e])) : PERM(raw[2 * aa], raw[2 * aa - 1], sel[2][e]);
        };
        auto rdX = [&](int k) {
            if (k == 0) { rx[0] = Q0(262); rx[1] = Q0(260); }
            if (k == 1) { rx[2] = Q0(258); rx[3] = Q0(256); }
            if (k == 2) { rx[4] = Q1(128); rx[5] = Q1(130); rx[6] = Q1(132); }
            if (k == 3) { rx[7] = Q2(128); rx[8] = Q2(130); rx[9] = R[-512]; }
        };
        auto fixT = [&](int k, int e) {
            if (k < 2) {
                const u32 lo = k == 0 ? rx[e] : (e < 2 ? PERM(rx[6], rx[5], sel[1][2 + e]) : PERM(rx[5], rx[4], sel[1][e]));
                T[0][1 + k][e] = (lo & msk[e]) | (T[0][1 + k][e] & ~msk[e]);
            } else T32[e] = (PERM(rx[8], rx[7], sel[2][e]) & msk[e]) | (PERM(rx[9], rx[9], 0x03020302u) & mkb[e]);
        };
        LAS char* uw = ubase + 192 + 256 * hf + 1024 * (1 - hf);
        auto rdC = [&](int k) {
            if (k < 4) UW[k] = lds128(uw + 512 * (k >> 1) - 16 * (k & 1));
            else { const int dl = (k - 4) >> 1, e = 2 * (k & 1); TB[dl][e] = Q3(128 + 32 * dl - 2 * e); TB[dl][e + 1] = Q3(128 + 32 * dl - 2 * e - 2); }
        };
#pragma unroll
        for (int k = 0; k < 5; ++k) rdT(3, k);
        SB();
        sfor<104>([&](auto gs) {
            constexpr int it = 3 - gs.v / 26, sl = gs.v % 26, aa = tri_a(sl), bq = tri_b(sl);
            y[aa + bq] = mfma(T[it & 1][aa], U[it & 1][bq], (it == 3 && aa == 0) ? zero4() : y[aa + bq]);
            SB();
            if constexpr (it == 3) {
                if constexpr (sl < 6) { mkT(3, 1 + (sl >> 1), 2 * (sl & 1)); mkT(3, 1 + (sl >> 1), 2 * (sl & 1) + 1); }
                else if constexpr (sl < 14) { if (sl < 11) rdT(2, sl - 6); rdU(2, sl - 6); }
                else mkT(2, 1 + ((sl - 14) >> 2), (sl - 14) & 3);
            } else if constexpr (it > 0) {
                if constexpr (sl < 8) { if (sl < 5) rdT(it - 1, sl); rdU(it - 1, sl); }
                if constexpr (it == 1 && sl >= 5 && sl < 9) rdX(sl - 5);
                if constexpr (it == 2 && sl >= 10 && sl < 22) mkT(1, 1 + ((sl - 10) >> 2), (sl - 10) & 3);
                if constexpr (it == 1 && sl >= 10 && sl < 16) { mkT(0, 1 + ((sl - 10) >> 1), 2 * (sl & 1)); mkT(0, 1 + ((sl - 10) >> 1), 2 * (sl & 1) + 1); }
                if constexpr (it == 1 && sl >= 16 && sl < 24) fixT((sl - 16) >> 2, (sl - 16) & 3);
            } else {
                if constexpr (sl < 12) rdC(sl);
                if constexpr (sl < 4) fixT(2, sl);
                if constexpr (sl >= 10 && sl < 14) wd[sl - 10] = mfma(UW[sl - 10], (sl & 1) ? bE : bO, zero4());
                if constexpr (sl >= 16 && sl < 24) WA[(sl - 16) >> 2][sl & 3] = pk(wd[2 * ((sl - 16) >> 2) + 1][sl & 3], wd[2 * ((sl - 16) >> 2)][sl & 3]);
                if constexpr (sl == 24) TB[0] &= mkd;
            }
            SB();
        });
        if (hf) {
#pragma unroll
            for (int k = 0; k < 4; ++k) y[4 + k] = mfma(WA[0], TB[k], y[4 + k]);
#pragma unroll
            for (int k = 0; k < 3; ++k) y[5 + k] = mfma(WA[1], TB[k], y[5 + k]);
        } else {
#pragma unroll
            for (int k = 0; k < 4; ++k) y[4 + k] = mfma(T32, U[0][k], y[4 + k]);
            y[6] = mfma(WA[0], TB[0], y[6]); y[7] = mfma(WA[0], TB[1], y[7]); y[7] = mfma(WA[1], TB[0], y[7]);
        }
    }
    STAMP(5);
    {
        LAS char* yd = sm + S_TAB + wave * 8192 + lane * 16;
#pragma unroll
        for (int i = 0; i < 8; ++i) sts128(yd + i * 1024, asu(y[i]));
    }
    asm volatile("s_waitcnt vmcnt(0)" : "+v"(tag), "+v"(tch) :: "memory");
    tag += 1;
    wg_barrier();
    {
        LAS char* ys = sm0 + S_TAB + G8 * 1024 + lane * 16;
        u32x4 o[2];
#pragma unroll
        for (int c = 0; c < 2; ++c) {
            const f32x4 y0 = asf(lds128(ys + c * 81920)) + asf(lds128(ys + c * 81920 + 8192));
            const f32x4 y1 = asf(lds128(ys + c * 81920 + 16384)) + asf(lds128(ys + c * 81920 + 24576));
            const float d0 = bflo(dsk[c]), d1 = bfhi(dsk[c]);
#pragma unroll
            for (int i = 0; i < 4; ++i) o[c][i] = pk(y0[i] + bflo(uu[i][c]) * d0, y1[i] + bfhi(uu[i][c]) * d1);
            bst_dev(o[c], rws, WS_YX + (2 * sb + c) * 8192 + G8 * 1024 + lane * 16);
        }
        const u32 s0 = rowsum(sum4(o[0])), s1 = rowsum(sum4(o[1]));
        bst32_dev(tag - (r16 ? s1 : s0), rws, r16 < 2 ? WS_YC + (sb * 8 + G8) * 128 + g * 8 + r16 * 4 : OOB);
    }
    STAMP(6);
    {
        const u32 cofs = (ct * 32 + (tid >> 4)) * 8192 + j * 256 + (tid & 15) * 16;
        const u32 ccs = ((ct * 16 + (tid >> 5)) * 8 + (j >> 2)) * 128 + (j & 3) * 8 + ((tid >> 4) & 1) * 4;
        LAS char* trw = sm + S_TR + (tid & 7) * 288 + (tid >> 3) * 8;
        LAS char* trr = sm + S_TR + (((tid >> 4) & 3) * 8 + (tid >> 6)) * 288 + (tid & 15) * 16;
        const u32 oo = (256 * (j >> 2) + 16 * (tid >> 4) + 4 * (j & 3) + 2 * ((tid >> 3) & 1)) * 1792 + 128 * ct + (tid & 7) * 16;
        waitv<0>();
        STAMP(7);
        u32x4 v[2]; u32 vc[2];
#pragma unroll
        for (int i = 0; i < 2; ++i) { v[i] = bld_dev(rws, cofs, WS_YX + i * 16 * 8192); vc[i] = bld32_dev(rws, ccs, WS_YC + i * 8192); }
        cbar();
        auto ok = [&](int i) { return rowsum(sum4(v[i])) + vc[i] == tag; };
        settle<2>(badmask<2>(ok), ok,
                  [&](auto i, u32 bm) { reld<i.v>(v[i.v], dws, cofs, WS_YX + i.v * 16 * 8192, bm); reld32<i.v>(vc[i.v], dws, ccs, WS_YC + i.v * 8192, bm); },
                  [&](int i) { pin(v[i]); pin(vc[i]); });
        STAMP(8);
        sts128(trr, v[0]); sts128(trr + 4 * 288, v[1]);
        wg_barrier();
        const u32x2 s0 = *(LAS u32x2*)trw, s1 = *(LAS u32x2*)(trw + 8 * 288), s2 = *(LAS u32x2*)(trw + 16 * 288), s3 = *(LAS u32x2*)(trw + 24 * 288);
        const rsrc_t ro = mkrsrc(a.out, 2048 * 1792);
        bst_dev(u32x4{s0.x, s1.x, s2.x, s3.x}, ro, oo);
        bst_dev(u32x4{s0.y, s1.y, s2.y, s3.y}, ro, oo + 1792);
        if (j == 0 && tid == 0) st_epoch(a.ws + WS_EPS + ct * 256, tag);
    }
    STAMP(9);
    STAMP_FLUSH(256 + sb);
}

DI void warm_attn(const Args& a, LAS char* sm, int xcd, int hi) {
    const int lane = threadIdx.x & 63, wave8 = __builtin_amdgcn_readfirstlane(threadIdx.x >> 6);
    const int u = hi * 5 + wave8, idx = (u - 24) * 64 + lane;
    if (wave8 >= 5) return;
    setm0(sm + 163840 - 256);
    if (u < 24) {
        const int rbl = (xcd >> 1) * 4 + u / 6, kc = (2 * (rbl + (xcd & 1) * 4) + 1 + 2 * ((u % 6) >> 1)) % 7;
        touch_lds(mkdesc(a.x, 2048 * 896), (rbl * 128 + (u & 1) * 64 + lane) * 896 + kc * 128);
    } else touch_lds(mkdesc(a.wqkv, 448 * 2688), (idx >> 3) * 2688 + (1 + ((idx >> 2) & 1)) * 896 + ((xcd & 1) * 3 + (idx & 3)) * 128);
}

#if defined(__HIP_DEVICE_COMPILE__)
#define NOPK __attribute__((target("no-packed-fp32-ops")))
#else
#define NOPK
#endif
__global__ NOPK __launch_bounds__(512, 2) void fused_kernel(Args a) {
    __shared__ __attribute__((aligned(16))) char smem[163840];
    LAS char* sm = (LAS char*)smem;
    const int xcd = blockIdx.x & 7, j = blockIdx.x >> 3;
    const bool is_attn = (j & 1) == 0;
    if (__builtin_expect(!is_attn, 1)) warm_attn(a, sm, xcd, j >> 1);
    cbar();
    if (__builtin_expect(is_attn, 1)) attn_role(a, sm, xcd, j >> 1);
    else if (j < 28) ssm_role(a, sm, xcd * 14 + (j >> 1));
    else {
        const int t = (j >> 1 & 1) * 512 + threadIdx.x;
        setm0(sm);
#pragma unroll
        for (int i = 0; i < 2; ++i) touch_lds(mkdesc(a.wqkv, 448 * 2688), ((t + 1024 * i) >> 2) * 2688 + ((xcd & 1) * 3 + (t & 3)) * 128);
#pragma unroll
        for (int i = 0; i < 4; ++i) touch_lds(mkdesc(a.wo, 448 * 896), (t + 1024 * i) * 128);
        waitv<0>();
    }
}

extern "C" void kernel_launch(void* const* d_in, const int* in_sizes, int n_in,
                              void* d_out, int out_size, void* d_ws, size_t ws_size,
                              hipStream_t stream) {
    Args a;
    a.u = (const u16*)d_in[0]; a.logA = (const u16*)d_in[1]; a.Aim = (const u16*)d_in[2];
    a.Cre = (const u16*)d_in[3]; a.Cim = (const u16*)d_in[4]; a.logdt = (const u16*)d_in[5];
    a.Dskip = (const u16*)d_in[6]; a.x = (const u16*)d_in[7]; a.wqkv = (const u16*)d_in[8];
    a.wo = (const u16*)d_in[9]; a.out = (u16*)d_out; a.ws = (char*)d_ws;
    fused_kernel<<<256, 512, 0, stream>>>(a);
}
```

```cpp
#include <hip/hip_runtime.h>
#include <stdint.h>

typedef unsigned short u16;
typedef unsigned int u32;
typedef unsigned long long u64;
typedef __bf16 bf16x8 __attribute__((ext_vector_type(8)));
typedef __bf16 bf16x2 __attribute__((ext_vector_type(2)));
typedef _Float16 h2 __attribute__((ext_vector_type(2)));
typedef _Float16 h8 __attribute__((ext_vector_type(8)));
typedef short s16x4 __attribute__((ext_vector_type(4)));
typedef float f32x2 __attribute__((ext_vector_type(2)));
typedef float f32x4 __attribute__((ext_vector_type(4)));
typedef float f32x16 __attribute__((ext_vector_type(16)));
typedef u32 u32x2 __attribute__((ext_vector_type(2)));
typedef u32 u32x4 __attribute__((ext_vector_type(4)));
typedef __amdgpu_buffer_rsrc_t rsrc_t;
#define LAS __attribute__((address_space(3)))
#define GAS __attribute__((address_space(1)))
#define DI __device__ __forceinline__
#define SB() __builtin_amdgcn_sched_barrier(0)
#define PERM __builtin_amdgcn_perm

constexpr u32 WS_EPA = 0;
constexpr u32 WS_EPS = 256;
constexpr u32 WS_YC  = 65536;
constexpr u32 WS_KVP = 524288;
constexpr u32 WS_OX  = WS_KVP + (1u << 19);
constexpr u32 WS_YX  = WS_OX + (2u << 20);
constexpr u32 WS_END = WS_YX + (2u << 20);
constexpr u32 OOB = 0x80000000u;
constexpr int SPIN_CAP = 1 << 16;

struct Args {
    const u16 *u, *logA, *Aim, *Cre, *Cim, *logdt, *Dskip, *x, *wqkv, *wo;
    u16* out;
    char* ws;
};

DI f32x4 mfma(u32x4 a, u32x4 b, f32x4 c) { return __builtin_amdgcn_mfma_f32_16x16x32_bf16(__builtin_bit_cast(bf16x8, a), __builtin_bit_cast(bf16x8, b), c, 0, 0, 0); }
DI f32x4 mfmah(u32x4 a, u32x4 b, f32x4 c) { return __builtin_amdgcn_mfma_f32_16x16x32_f16(__builtin_bit_cast(h8, a), __builtin_bit_cast(h8, b), c, 0, 0, 0); }
DI f32x16 mfma32(u32x4 a, u32x4 b, f32x16 c) { return __builtin_amdgcn_mfma_f32_32x32x16_bf16(__builtin_bit_cast(bf16x8, a), __builtin_bit_cast(bf16x8, b), c, 0, 0, 0); }
DI u32 pk(float lo, float hi) { f32x2 v = {lo, hi}; return __builtin_bit_cast(u32, __builtin_convertvector(v, bf16x2)); }
DI float bflo(u32 v) { return __uint_as_float(v << 16); }
DI float bfhi(u32 v) { return __uint_as_float(v & 0xffff0000u); }
DI f32x2 unpk(u32 v) { return f32x2{bflo(v), bfhi(v)}; }
DI f32x4 asf(u32x4 v) { return __builtin_bit_cast(f32x4, v); }
DI u32x4 asu(f32x4 v) { return __builtin_bit_cast(u32x4, v); }
DI f32x4 zero4() { return f32x4{0.f, 0.f, 0.f, 0.f}; }
DI f32x16 zero16() { f32x16 z; for (int i = 0; i < 16; ++i) z[i] = 0.f; return z; }
DI u32x4 pk8(const f32x16& a, int s) { return u32x4{pk(a[8 * s], a[8 * s + 1]), pk(a[8 * s + 2], a[8 * s + 3]), pk(a[8 * s + 4], a[8 * s + 5]), pk(a[8 * s + 6], a[8 * s + 7])}; }
DI u32x4 quad(const f32x16& a, int r) { return asu(f32x4{a[4 * r], a[4 * r + 1], a[4 * r + 2], a[4 * r + 3]}); }
template <class T> DI void pin(T& v) { asm volatile("" : "+v"(v) :: "memory"); }
template <int I> struct IC { static constexpr int v = I; };
template <int N, int I = 0, class F> DI void sfor(F f) { if constexpr (I < N) { f(IC<I>{}); sfor<N, I + 1>(f); } }
DI void cbar() { asm volatile("" ::: "memory"); }
DI void raw_barrier() { asm volatile("s_barrier" ::: "memory"); }
DI void wg_barrier() { asm volatile("s_waitcnt lgkmcnt(0)\n\ts_barrier" ::: "memory"); }
template <int N> DI void waitv() { asm volatile("s_waitcnt vmcnt(%0)" :: "i"(N) : "memory"); }
template <int N> DI void waitx(u32x4* x) { asm volatile("s_waitcnt vmcnt(%4)" : "+v"(x[0]), "+v"(x[1]), "+v"(x[2]), "+v"(x[3]) : "i"(N) : "memory"); }

DI u32x4 lds128(LAS char* p) { return *(LAS u32x4*)p; }
DI void sts128(LAS char* p, u32x4 v) { *(LAS u32x4*)p = v; }
DI void sts64(LAS char* p, u32 lo, u32 hi) { u32x2 v = {lo, hi}; *(LAS u32x2*)p = v; }
DI u32x4 trfrag(LAS char* p0, LAS char* p1) {
    u32x2 a = __builtin_bit_cast(u32x2, __builtin_amdgcn_ds_read_tr16_b64_v4i16((LAS s16x4*)p0));
    u32x2 b = __builtin_bit_cast(u32x2, __builtin_amdgcn_ds_read_tr16_b64_v4i16((LAS s16x4*)p1));
    return u32x4{a.x, a.y, b.x, b.y};
}

DI rsrc_t mkrsrc(const void* p, u32 bytes) { return __builtin_amdgcn_make_buffer_rsrc((void*)p, 0, bytes, 0x00020000); }
DI u32x4 mkdesc(const void* p, u32 bytes) { u64 v = (u64)p; return u32x4{(u32)v, (u32)(v >> 32) & 0xffffu, bytes, 0x00020000u}; }
DI u32x4 bld_dev(rsrc_t r, u32 voff, u32 soff) { return __builtin_amdgcn_raw_buffer_load_b128(r, voff, soff, 16); }
DI u32 bld32_dev(rsrc_t r, u32 voff, u32 soff) { return __builtin_amdgcn_raw_buffer_load_b32(r, voff, soff, 16); }
DI void bst_dev(u32x4 v, rsrc_t r, u32 voff) { __builtin_amdgcn_raw_buffer_store_b128(v, r, voff, 0, 16); }
DI void bst32_dev(u32 v, rsrc_t r, u32 voff) { __builtin_amdgcn_raw_buffer_store_b32(v, r, voff, 0, 16); }
DI void st_epoch(char* p, u32 v) { __hip_atomic_store((GAS u32*)p, v, __ATOMIC_RELAXED, __HIP_MEMORY_SCOPE_AGENT); }
DI void setm0(LAS char* dst) { asm volatile("s_mov_b32 m0, %0\n\ts_nop 0" :: "s"((u32)(uintptr_t)dst) : "memory"); }
template <int OFF>
DI void dma(u32x4 desc, u32 voff, u32 soff) { asm volatile("buffer_load_dwordx4 %0, %1, %2 offen offset:%3 lds" :: "v"(voff), "s"(desc), "s"(soff), "i"(OFF) : "memory"); }
DI void ldd(u32x4& v, u32x4 desc, u32 voff, u32 soff) { asm volatile("buffer_load_dwordx4 %0, %1, %2, %3 offen sc1" : "=v"(v) : "v"(voff), "s"(desc), "s"(soff) : "memory"); }
DI u32 ld_epoch(const char* p) { u32 v; asm volatile("global_load_dword %0, %1, %2 sc1" : "=v"(v) : "v"(0u), "s"(p) : "memory"); return v; }
DI u32 touch(u32x4 desc, u32 voff) { u32 v; asm volatile("buffer_load_dword %0, %1, %2, 0 offen" : "=v"(v) : "v"(voff), "s"(desc) : "memory"); return v; }
DI void touch_lds_dev(u32x4 desc, u32 voff) { asm volatile("buffer_load_dword %0, %1, 0 offen sc1 lds" :: "v"(voff), "s"(desc) : "memory"); }
DI void touch_lds(u32x4 desc, u32 voff) { asm volatile("buffer_load_dword %0, %1, 0 offen lds" :: "v"(voff), "s"(desc) : "memory"); }
template <int BIT> DI void reld(u32x4& v, u32x4 desc, u32 voff, u32 soff, u32 mask) {
    asm volatile("s_bitcmp1_b32 %4, %5\n\ts_cbranch_scc0 1f\n\tbuffer_load_dwordx4 %0, %1, %2, %3 offen sc1\n1:"
                 : "+v"(v) : "v"(voff), "s"(desc), "s"(soff), "s"(mask), "i"(BIT) : "scc", "memory");
}
template <int BIT> DI void reld32(u32& v, u32x4 desc, u32 voff, u32 soff, u32 mask) {
    asm volatile("s_bitcmp1_b32 %4, %5\n\ts_cbranch_scc0 1f\n\tbuffer_load_dword %0, %1, %2, %3 offen sc1\n1:"
                 : "+v"(v) : "v"(voff), "s"(desc), "s"(soff), "s"(mask), "i"(BIT) : "scc", "memory");
}
template <int N, class OK, class LD, class PIN>
DI void settle(u32 bad, OK ok, LD ld, PIN pn) {
#pragma unroll 1
    for (int sp = 0; bad != 0 && sp < SPIN_CAP; ++sp) {
        __builtin_amdgcn_s_sleep(2);
        sfor<N>([&](auto i) { ld(i, bad); });
        waitv<0>();
        sfor<N>([&](auto i) { pn(i.v); });
        sfor<N>([&](auto i) { if ((bad >> i.v & 1) && __all(ok(i.v))) bad &= ~(1u << i.v); });
    }
}
template <int N, class OK> DI u32 badmask(OK ok) { u32 bad = 0; sfor<N>([&](auto i) { if (!__all(ok(i.v))) bad |= 1u << i.v; }); return bad; }

DI u32 sum4(u32x4 v) { return v.x + v.y + v.z + v.w; }
DI u32 rowsum(u32 s) {
    s += __builtin_amdgcn_update_dpp(0, s, 0x128, 0xf, 0xf, false);
    s += __builtin_amdgcn_update_dpp(0, s, 0x124, 0xf, 0xf, false);
    s += __builtin_amdgcn_update_dpp(0, s, 0x122, 0xf, 0xf, false);
    s += __builtin_amdgcn_update_dpp(0, s, 0x121, 0xf, 0xf, false);
    return s;
}
DI u32 sum32(u32 s) { s = rowsum(s); auto t = __builtin_amdgcn_permlane16_swap(s, s, false, false); return t[0] + t[1]; }
DI u32 halfsum(u32 s) { auto r = __builtin_amdgcn_permlane32_swap(s, s, false, false); return r[0] + r[1]; }
DI u32 wavemax(u32 s) {
    s = max(s, (u32)__builtin_amdgcn_update_dpp(0, s, 0x128, 0xf, 0xf, false));
    s = max(s, (u32)__builtin_amdgcn_update_dpp(0, s, 0x124, 0xf, 0xf, false));
    s = max(s, (u32)__builtin_amdgcn_update_dpp(0, s, 0x122, 0xf, 0xf, false));
    s = max(s, (u32)__builtin_amdgcn_update_dpp(0, s, 0x121, 0xf, 0xf, false));
    auto r = __builtin_amdgcn_permlane32_swap(s, s, false, false);
    s = max(r[0], r[1]);
    auto t = __builtin_amdgcn_permlane16_swap(s, s, false, false);
    return max(t[0], t[1]);
}

DI float phi(float t) {
    const float e = __builtin_amdgcn_exp2f(t * 1.44269504089f);
    return __int_as_float(max(__float_as_int(t), 0x358637bd)) + __builtin_amdgcn_fmed3f(e, 0.f, 1.f);
}

constexpr int A_RED = 0, A_WO = 32768, A_WQ = 98304, A_KVB = 155648;
constexpr int PRO = 8;
constexpr int LA = 6;
constexpr int QS0 = 40;
constexpr int GK0 = 14;
constexpr int G2A = 2;
constexpr int issued_before(int gs) {
    constexpr int B0 = 16 - LA;
    int n = PRO + gs;
    int cap = 16 + (gs < B0 ? 0 : 4 * ((gs - B0) / 8) + ((gs - B0) % 8 < 4 ? (gs - B0) % 8 : 4));
    n = n < cap ? n : cap;
    if (n < 28) return n;
    const int q = gs - QS0;
    return 28 + (q < 0 ? 0 : q > 7 ? 7 : q);
}
static_assert(QS0 >= 40 - LA, "Wq overwrites ring slot 3");
constexpr int slot_ch(int gs) { return gs < 40 ? gs >> 3 : 5 + ((gs >> 2) & 1); }
constexpr int slot_kk(int gs) { return gs < 40 ? (gs & 7) >> 1 : gs & 3; }
constexpr int slot_sg(int gs) { return gs < 40 ? gs & 1 : gs >= 48; }

DI void attn_role(const Args& a, LAS char* sm, const int xcd, const int j) {
    const int tid = threadIdx.x, lane = tid & 63, wave = __builtin_amdgcn_readfirstlane(tid >> 6);
    const int rw4 = wave & 3, hs = wave >> 2;
    const int hp = (xcd & 1) * 4 + (j & 3), rb = (xcd >> 1) * 4 + (j >> 2);
    const int h = 2 * hp + hs;
    const int c0 = (2 * rb + 2 * hp) % 7;
    const int r32 = lane & 31, hh = lane >> 5, blk = (lane >> 4) & 1, q = (lane >> 2) & 3, p = lane & 3;
    const u32x4 rx = mkdesc(a.x, 2048 * 896), rw = mkdesc(a.wqkv, 448 * 2688), rwo = mkdesc(a.wo, 448 * 896), dws = mkdesc(a.ws, WS_END);
    const rsrc_t rws = mkrsrc(a.ws, WS_END);
    u32 tag = ld_epoch(a.ws + WS_EPA);
    const int widx = (lane & 15) ^ ((lane >> 4) << 2);
    const u32 wv = (8 * wave + (lane >> 4)) * 2688 + (1 + (widx >> 3)) * 896 + hp * 112 + min(widx & 7, 6) * 16;
    const u32 xv0 = (rb * 128 + wave * 16 + (lane >> 3)) * 896 + (((lane & 7) ^ (lane >> 4)) * 16);
    const u32 xv1 = (rb * 128 + wave * 16 + 8 + (lane >> 3)) * 896 + (((lane & 7) ^ (4 + (lane >> 4))) * 16) - 1024;
    const u32 qv = (8 * wave + (lane >> 3)) * 2688 + hp * 112 + min((lane & 7) ^ (blk << 2), 6) * 16;
    auto kchunk = [&](int i) { int kc = i + c0; return kc >= 7 ? kc - 7 : kc; };
    auto ldn = [&](int n) {
        if (n >= 28) { setm0(sm + A_WQ + (n - 28) * 8192 + wave * 1024); dma<0>(rw, qv, kchunk(n - 28) * 64 * 2688); return; }
        const int i = n >> 2, m = n & 3;
        if (m == 0) { setm0(sm + (i & 3) * 32768 + 16384 + wave * 2048); dma<0>(rx, xv0, kchunk(i) * 128); }
        if (m == 1) dma<1024>(rx, xv1, kchunk(i) * 128);
        if (m == 2) { setm0(sm + (i & 3) * 32768 + wave * 2048); dma<0>(rw, wv, kchunk(i) * 64 * 2688); }
        if (m == 3) dma<1024>(rw, wv, kchunk(i) * 64 * 2688 + 4 * 2688 - 1024);
    };
#pragma unroll
    for (int n = 0; n < PRO; ++n) ldn(n);

    const int wb = 56 * hs + 32 * blk + 8 * p;
    LAS char *wsrc[2], *xsrc[4];
#pragma unroll
    for (int sg = 0; sg < 2; ++sg) wsrc[sg] = sm + (32 * hh + q) * 256 + (((2 * sg + (wb >> 6)) ^ q) << 6) + (wb & 63);
#pragma unroll
    for (int kk = 0; kk < 4; ++kk) xsrc[kk] = sm + 16384 + (32 * rw4 + r32) * 128 + (((4 * hh + kk) ^ ((r32 >> 1) & 7)) * 16);

    const bool p3 = j < 14;
    const int r128 = p3 ? xcd * 2 + j / 7 : 0, nb = j % 7;
    f32x16 acc[2] = {zero16(), zero16()};
    u32x4 xf[28], wf[56];
    auto rdw = [&](int gs) { LAS char* s = wsrc[slot_sg(gs)] + (slot_ch(gs) & 3) * 32768 + slot_kk(gs) * 2048; wf[gs] = trfrag(s, s + 1024); };
    auto rdx = [&](int i, int kk) { xf[4 * i + kk] = lds128(xsrc[kk] + (i & 3) * 32768); };
    waitv<PRO - 4>();
    wg_barrier();
#pragma unroll
    for (int sl = 0; sl < LA; ++sl) { if (sl < 4) rdx(0, sl); rdw(sl); }
    u32 pkd[8];
    u32 vm = r32 == 28 ? 0u : ~0u, vo = r32 == 28 ? 0x3f803f80u : 0u;
    pin(vm); pin(vo);
    sfor<56>([&](auto gs) {
        constexpr int issued = issued_before(gs.v);
        constexpr int fu = gs.v + LA, c = fu == 44 ? 6 : (fu <= 40 && (fu & 7) == 0) ? fu >> 3 : 0;
        static_assert(!c || issued >= 4 * c + 4, "chunk not issued yet");
        if constexpr (c != 0) { waitv<issued - (4 * c + 4)>(); raw_barrier(); }
        SB();
        acc[slot_sg(gs.v)] = mfma32(xf[4 * slot_ch(gs.v) + slot_kk(gs.v)], wf[gs.v], acc[slot_sg(gs.v)]);
        SB();
        if constexpr (fu < 40 && fu >= 8 && (fu & 1) == 0) rdx(fu >> 3, (fu & 7) >> 1);
        if constexpr (fu >= 40 && fu < 48) rdx(slot_ch(fu), fu & 3);
        if constexpr (fu < 56) rdw(fu);
        if constexpr (gs.v == 44) raw_barrier();
        if constexpr (gs.v >= 49) { pkd[gs.v - 49] = pk(phi(acc[0][2 * (gs.v - 49)]), phi(acc[0][2 * (gs.v - 49) + 1])); pin(pkd[gs.v - 49]); }
#pragma unroll
        for (int n = issued; n < issued_before(gs.v + 1); ++n) ldn(n);
        SB();
    });
    {
        pkd[7] = pk(phi(acc[0][14]), phi(acc[0][15]));
        pin(pkd[7]);
        f32x16 kv = mfma32(u32x4{pkd[0], pkd[1], pkd[2], pkd[3]}, (pk8(acc[1], 0) & vm) | vo, zero16());
        SB();
        kv = mfma32(u32x4{pkd[4], pkd[5], pkd[6], pkd[7]}, (pk8(acc[1], 1) & vm) | vo, kv);
#pragma unroll
        for (int rq = 0; rq < 4; ++rq) sts128(sm + A_RED + wave * 4096 + rq * 1024 + lane * 16, quad(kv, rq));
    }
    asm volatile("s_waitcnt vmcnt(7)" : "+v"(tag) :: "memory");
    tag += 1;
    wg_barrier();
    u32 hm = hh ? 0u : ~0u, cm = r32 == 31 ? 0u : ~0u;
    pin(hm); pin(cm);
    const bool red = (rw4 >> 1) == hs;
    if (red) {
        LAS char* rs = sm + A_RED + hs * 16384 + (rw4 & 1) * 2048 + lane * 16;
        f32x4 lo = (asf(lds128(rs)) + asf(lds128(rs + 4096))) + (asf(lds128(rs + 8192)) + asf(lds128(rs + 12288)));
        f32x4 hi = (asf(lds128(rs + 1024)) + asf(lds128(rs + 5120))) + (asf(lds128(rs + 9216)) + asf(lds128(rs + 13312)));
        u32x4 v = {pk(lo.x, lo.y), pk(lo.z, lo.w), pk(hi.x, hi.y), pk(hi.z, hi.w) & cm};
        v.w |= (tag - sum32(sum4(v))) & ~cm;
        bst_dev(v, rws, WS_KVP + (h * 16 + rb) * 2048 + (tid & 127) * 16);
    }
    cbar();
    if (red) waitv<1>(); else waitv<0>();
    raw_barrier();
    const u32 ov = (8 * wave + (lane >> 3)) * 896 + nb * 128 + (((lane & 7) ^ (blk << 2)) * 16);
    u32x4 gk[8];
    const u32 kbase = WS_KVP + (h * 16 + (rw4 >> 1) * 8 + (rb & 7)) * 2048, kvo = (tid & 127) * 16;
    f32x16 aq = zero16();
    {
        LAS char* qsrc = sm + A_WQ + (32 * hh + q) * 128 + (((wb >> 6) ^ (q >> 1)) << 6) + (wb & 63);
        u32x4 wq[28];
        auto rdq = [&](int k) { LAS char* s = qsrc + (k >> 2) * 8192 + (k & 3) * 1024; wq[k] = trfrag(s, s + 512); };
#pragma unroll
        for (int k = 0; k < 4; ++k) rdq(k);
        SB();
#pragma unroll
        for (int k = 0; k < 28; ++k) {
            aq = mfma32(wq[k], xf[k], aq);
            SB();
            if (k + 4 < 28) rdq(k + 4);
            if (k < 7) { setm0(sm + A_WO + k * 8192 + wave * 1024); dma<0>(rwo, ov, k * 64 * 896); }
            if (k >= GK0 && k < GK0 + 8) ldd(gk[k - GK0], dws, kvo, kbase ^ ((k - GK0) * 2048));
            SB();
        }
    }
    u32 pqd[8];
#pragma unroll
    for (int k = 0; k < 8; ++k) {
        pqd[k] = pk(phi(aq[2 * k]), phi(aq[2 * k + 1]));
        if (k >= 6) pqd[k] &= hm;
        pin(pqd[k]);
    }
    {
        waitx<0>(gk); waitx<0>(gk + 4);
        u32 t = 0;
#pragma unroll
        for (int i = 0; i < 8; ++i) t += sum4(gk[i]);
        if (!__all(sum32(t) == 8u * tag)) {
            auto ok = [&](int i) { return sum32(sum4(gk[i])) == tag; };
            settle<8>(badmask<8>(ok), ok, [&](auto i, u32 bm) { reld<i.v>(gk[i.v], dws, kvo, kbase ^ (i.v * 2048), bm); }, [&](int i) { pin(gk[i]); });
        }
    }
    {
        u32x4 v;
#pragma unroll
        for (int c = 0; c < 4; ++c) {
            const float lo = ((bflo(gk[0][c]) + bflo(gk[1][c])) + (bflo(gk[2][c]) + bflo(gk[3][c]))) + ((bflo(gk[4][c]) + bflo(gk[5][c])) + (bflo(gk[6][c]) + bflo(gk[7][c])));
            const float hi = ((bfhi(gk[0][c]) + bfhi(gk[1][c])) + (bfhi(gk[2][c]) + bfhi(gk[3][c]))) + ((bfhi(gk[4][c]) + bfhi(gk[5][c])) + (bfhi(gk[6][c]) + bfhi(gk[7][c])));
            v[c] = pk(lo, hi);
        }
        sts128(sm + A_KVB + tid * 16, v);
    }
    wg_barrier();
    {
        u32x4 kf[4];
#pragma unroll
        for (int i = 0; i < 4; ++i) kf[i] = lds128(sm + A_KVB + hs * 4096 + i * 1024 + lane * 16);
        const u32x4 pq0 = {pqd[0], pqd[1], pqd[2], pqd[3]}, pq1 = {pqd[4], pqd[5], pqd[6], pqd[7]};
        f32x16 oa = mfma32(kf[3], pq1, mfma32(kf[2], pq0, mfma32(kf[1], pq1, mfma32(kf[0], pq0, zero16()))));
        const u32 dn = __float_as_uint(oa[12]);
        auto r = __builtin_amdgcn_permlane32_swap(dn, dn, false, false);
        oa *= __builtin_amdgcn_rcpf(__uint_as_float(r[1]) + 1e-6f);
        const u32x4 f0 = pk8(oa, 0);
        const u32 o = WS_OX + ((rb * 4 + rw4) * 16 + h) * 2048 + lane * 16;
        bst_dev(f0, rws, o);
        SB();
        u32x4 f1 = pk8(oa, 1);
        f1.z &= hm; f1.w &= hm;
        f1.w |= (tag - halfsum(sum4(f0) + sum4(f1))) & ~hm;
        bst_dev(f1, rws, o + 1024);
    }
    cbar();
    if (p3) {
        LAS char* osrc[2];
#pragma unroll
        for (int nt = 0; nt < 2; ++nt) osrc[nt] = sm + A_WO + (224 * hs + 4 * hh + q) * 128 + ((nt ^ (q >> 1)) * 64) + blk * 32 + 8 * p;
        const int padoff = hh ? 0 : 1024;
        u32x4 wf3[8][2][2];
#pragma unroll
        for (int k = 0; k < 32; ++k) {
            const int hl = k >> 2, ks = (k >> 1) & 1, nt = k & 1;
            LAS char* s = osrc[nt] + (hl * 28 + 16 * ks) * 128;
            wf3[hl][ks][nt] = trfrag(s, s + (ks ? padoff : 1024));
        }
        SB();
        waitv<0>();
        u32x4 go[16];
        const u32 ob0 = WS_OX + ((r128 * 4 + rw4) * 16 + 8 * hs) * 2048;
        auto ldo = [&](int i) { ldd(go[i], dws, lane * 16, ob0 + i * 1024); };
#pragma unroll
        for (int i = 0; i < 4 * G2A; ++i) ldo(i);
        f32x16 ac[2];
        sfor<4>([&](auto gi) {
            constexpr int i = 4 * gi.v, nis = gi.v + G2A < 4 ? gi.v + G2A : 4;
            waitx<4 * (nis - gi.v - 1)>(go + i);
            {
                auto r0 = __builtin_amdgcn_permlane32_swap(sum4(go[i]) + sum4(go[i + 1]), sum4(go[i + 2]) + sum4(go[i + 3]), false, false);
                const u64 ne = __ballot(r0[0] + r0[1] != tag);
                if (ne)
                    settle<2>(((u32)ne != 0) | ((ne >> 32) != 0) << 1, [&](int k) { return halfsum(sum4(go[i + 2 * k]) + sum4(go[i + 2 * k + 1])) == tag; },
                              [&](auto k, u32 bm) { reld<k.v>(go[i + 2 * k.v], dws, lane * 16, ob0 + (i + 2 * k.v) * 1024, bm);
                                                    reld<k.v>(go[i + 2 * k.v + 1], dws, lane * 16, ob0 + (i + 2 * k.v + 1) * 1024, bm); },
                              [&](int k) { pin(go[i + 2 * k]); pin(go[i + 2 * k + 1]); });
            }
            go[i + 1].w &= hm; go[i + 3].w &= hm;
            SB();
#pragma unroll
            for (int k = 0; k < 8; ++k) {
                const int hl = 2 * gi.v + (k >> 2), ks = (k >> 1) & 1, nt = k & 1;
                ac[nt] = mfma32(wf3[hl][ks][nt], go[2 * hl + ks], (gi.v == 0 && k < 2) ? zero16() : ac[nt]);
                SB();
                if ((k & 1) == 0 && gi.v + G2A < 4) ldo(4 * (gi.v + G2A) + (k >> 1));
                SB();
            }
        });
        LAS char* xb = sm + wave * 4096 + lane * 16;
        if (hs) {
#pragma unroll
            for (int rq = 0; rq < 4; ++rq) sts128(xb + rq * 1024, quad(ac[0], rq));
            ac[0] = ac[1];
        } else {
#pragma unroll
            for (int rq = 0; rq < 4; ++rq) sts128(xb + rq * 1024, quad(ac[1], rq));
        }
        wg_barrier();
        LAS char* pb = sm + (wave ^ 4) * 4096 + lane * 16;
        LAS char* ob = sm + A_WQ + rw4 * 4608;
#pragma unroll
        for (int rq = 0; rq < 4; ++rq) {
            const f32x4 v = asf(quad(ac[0], rq)) + asf(lds128(pb + rq * 1024));
            sts64(ob + r32 * 144 + hs * 64 + rq * 16 + hh * 8, pk(v.x, v.y), pk(v.z, v.w));
        }
        wg_barrier();
        const rsrc_t ro = mkrsrc(a.out, 2048 * 1792);
        const int orow = 16 * hs + (lane >> 3);
        const u32 oo = (r128 * 128 + rw4 * 32 + orow) * 1792 + 896 + nb * 128 + (lane & 7) * 16;
        const u32x4 v0 = lds128(ob + orow * 144 + (lane & 7) * 16), v1 = lds128(ob + (8 + orow) * 144 + (lane & 7) * 16);
        bst_dev(v0, ro, oo);
        bst_dev(v1, ro, oo + 8 * 1792);
        if (xcd == 0 && j == 0 && tid == 0) st_epoch(a.ws + WS_EPA, tag);
    }
}

constexpr int S_TAB = 0;
constexpr int S_KC = 34816;
constexpr int S_P = 47104;
constexpr int S_UT = 52480;
constexpr int S_TR = 61696;

struct c2 { f32x2 r, i; };
struct hc { h2 r, i; };
DI c2 csq(c2 a) { return c2{a.r * a.r - a.i * a.i, 2.f * a.r * a.i}; }
DI hc hmul(hc a, hc b) { return hc{a.r * b.r - a.i * b.i, a.r * b.i + a.i * b.r}; }
DI hc toh(c2 a) { return hc{__builtin_convertvector(a.r, h2), __builtin_convertvector(a.i, h2)}; }
DI c2 cexp2(f32x2 lr, f32x2 rev, float pw) {
    f32x2 e = lr * pw, f = rev * pw;
    f32x2 m = {__builtin_amdgcn_exp2f(e.x), __builtin_amdgcn_exp2f(e.y)};
    f.x = __builtin_amdgcn_fractf(f.x); f.y = __builtin_amdgcn_fractf(f.y);
    f32x2 c = {__builtin_amdgcn_cosf(f.x), __builtin_amdgcn_cosf(f.y)}, s = {__builtin_amdgcn_sinf(f.x), __builtin_amdgcn_sinf(f.y)};
    return c2{m * c, m * s};
}
constexpr int tri_a(int sl) { int a = 0; while (sl >= 8 - a) { sl -= 8 - a; ++a; } return a; }
constexpr int tri_b(int sl) { int a = 0; while (sl >= 8 - a) { sl -= 8 - a; ++a; } return sl; }

DI void ssm_role(const Args& a, LAS char* sm0, const int sb) {
    const int tid = threadIdx.x & 255, lane = tid & 63, wave8 = __builtin_amdgcn_readfirstlane(threadIdx.x >> 6), wave = wave8 & 3;
    LAS char* sm = sm0 + (wave8 >> 2) * 81920;
    const int ch = wave >> 1, hf = wave & 1, d = 4 * sb + (wave8 >> 1);
    const int g = lane >> 4, r16 = lane & 15;
    const u32 po = (d * 128 + hf * 64 + lane) * 4;
    const u32 ldt = a.logdt[d];
    const u32 pA = *(const u32*)((const char*)a.logA + po), pI = *(const u32*)((const char*)a.Aim + po);
    const u32 pCr = *(const u32*)((const char*)a.Cre + po), pCi = *(const u32*)((const char*)a.Cim + po);
    const u32x2 dsk = ((const u32x2*)a.Dskip)[sb];
    cbar();
    const int G8 = (wave8 + sb) & 7, trow = 256 * G8 + 16 * r16 + 4 * g;
    const rsrc_t ru = mkrsrc(a.u, 2048 * 896);
    u32x2 uu[4];
    auto ldu = [&](int i) { SB(); uu[i] = __builtin_amdgcn_raw_buffer_load_b64(ru, trow * 896 + sb * 8 + i * 896, 0, 0); cbar(); SB(); };
    const int sidx = 2 * sb + (wave8 >> 2);
    const int ct = sidx >> 5, j = sidx & 31;
    u32 tag = ld_epoch(a.ws + WS_EPS + ct * 256);
    *(LAS u32*)(sm + S_UT + (tid >> 7) * 4608 + (tid & 127) * 4) = 0;
    const int w = 16 + r16 - 8 * g;
    u32x4 bE, bO, mkb, mkd;
    u32 sel[3][4], msk[4], pb[3];
    {
        u32 y[4];
#pragma unroll
        for (int e = 0; e < 4; ++e) {
            y[e] = (u32)(w + 64 - 2 * e) * 0x10001u - 0x10000u;
            msk[e] = hf ? 0u : (((y[e] >> 6) & 0x10001u) ^ 0x10001u) * 0xffffu;
        }
        pb[0] = w - 7;
#pragma unroll
        for (int s = 1; s < 3; ++s) {
            u32 r[4];
#pragma unroll
            for (int e = (s == 1 ? 2 : 0); e < 4; ++e)
                r[e] = ((y[e] + ((1u << (s - 1)) - 1u) * 0x10001u + ((y[e] >> s) & 0x10001u)) >> s) & 0x00ff00ffu;
            const u32 base = r[3] >> 16;
            pb[s] = base - (64 >> s);
#pragma unroll
            for (int e = (s == 1 ? 2 : 0); e < 4; ++e) sel[s][e] = (((r[e] - base * 0x10001u) ^ 0x10001u) & 0xfffffu) * 0x202u + 0x01000100u;
        }
#pragma unroll
        for (int e = 0; e < 4; ++e) {
            const int z = min(max(16 - w + 2 * e, 0), 20);
            auto two = [&](u32 m) { const u32 b2 = (m >> z) & 3u; return (b2 | (b2 << 15)) & 0x10001u; };
            bE[e] = two(0x1ff0u) * 0x3f80u; bO[e] = two(0x0fe0u) * 0x3f80u; mkb[e] = two(0x1f000u) * 0xffffu;
            mkd[e] = r16 - 4 * g - e >= 0 ? ~0u : 0u;
        }
        asm volatile("" : "+v"(sel[1][2]), "+v"(sel[1][3]), "+v"(sel[2][0]), "+v"(sel[2][1]), "+v"(sel[2][2]), "+v"(sel[2][3]), "+v"(bE), "+v"(bO), "+v"(mkb), "+v"(mkd));
        asm volatile("" : "+v"(msk[0]), "+v"(msk[1]), "+v"(msk[2]), "+v"(msk[3]));
    }
    LAS u32* const R = (LAS u32*)(sm + S_P + ch * 2688) + 655;
    LAS u32 *P0 = R - (128 * hf + (int)pb[0]) - 102, *P1 = R - (256 + 64 * hf + (int)pb[1]) - 132, *P2 = R - (384 + 32 * hf + (int)pb[2]) - 130,
            *P3 = R - (385 + 2 * (r16 - 4 * g)) - 224;
    LAS char* ubase = sm + S_UT + ch * 4608 + 480 + r16 * 32 + g * 16 - 256 * hf - 192;
    asm volatile("" : "+v"(P0), "+v"(P1), "+v"(P2), "+v"(P3), "+v"(ubase));
    LAS char* tab = sm + S_TAB + wave * 8704;
    LAS char* tw = tab + lane * 8;
    LAS char* tr = tab + r16 * 544 + g * 16;
    LAS char* kc = sm + S_KC + wave * 3072;
    SB();
#define EMIT1(row, v) sts64(tw + (row) * 544, __builtin_bit_cast(u32, (v).r), __builtin_bit_cast(u32, (v).i))
#define EMIT2(row, v0, v1) do { SB(); EMIT1(row, v0); EMIT1((row) + 1, v1); SB(); } while (0)
#define KRD() do { SB(); _Pragma("unroll") for (int ks = 0; ks < 8; ++ks) gf[ks] = lds128(tr + ks * 64); cbar(); SB(); } while (0)
#define KM2(k) do { ka = mfmah(gf[2 * (k)], ff[2 * (k)], (k) ? ka : zero4()); ka = mfmah(gf[2 * (k) + 1], ff[2 * (k) + 1], ka); } while (0)
    const float L2E = 1.44269504089f;
    const float edt = __builtin_amdgcn_exp2f(bflo(ldt) * L2E);
    const float dt = bflo(pk(edt, edt));
    f32x2 lr = unpk(pA) * L2E;
    lr = unpk(pk(__builtin_amdgcn_exp2f(lr.x), __builtin_amdgcn_exp2f(lr.y))) * dt * -L2E;
    const f32x2 rev = unpk(pI) * dt * 0.15915494309f;
    hc W[8], F[16], G[17], H, t0, t1;
    u32x4 ff[8], gf[8];
    f32x4 ka;
    c2 e = cexp2(lr, -rev, 8.f);
    F[1] = toh(e); e = csq(e); F[2] = toh(e);
    F[0] = hc{h2{1.f16, 1.f16}, h2{0.f16, 0.f16}};
    EMIT2(0, F[0], F[1]);
    e = csq(e); F[4] = toh(e); F[3] = hmul(F[1], F[2]);
    EMIT2(2, F[2], F[3]);
    e = csq(e); F[8] = toh(e); F[5] = hmul(F[4], F[1]);
    EMIT2(4, F[4], F[5]);
    F[6] = hmul(F[4], F[2]); F[7] = hmul(F[4], F[3]); F[9] = hmul(F[8], F[1]);
    EMIT2(6, F[6], F[7]);
    {
        const int xcd = sb / 14, un = ((sb % 14) * 3 + wave8) * 64 + lane, v = un - 512, h0 = (xcd & 1) * 8, r0 = (xcd >> 1) * 4;
        setm0(sm0 + 163840 - 256);
        touch_lds_dev(mkdesc(a.ws, WS_END), wave8 >= 3 || un >= 2560 ? OOB
                      : un < 512 ? WS_KVP + ((h0 + (un >> 6)) * 16 + r0) * 2048 + (un & 63) * 128
                                 : WS_OX + ((r0 * 4 + (v >> 7)) * 16 + h0) * 2048 + (v & 127) * 128);
    }
    EMIT2(8, F[8], F[9]);
    F[10] = hmul(F[8], F[2]); F[11] = hmul(F[8], F[3]);
    e = cexp2(lr, rev, 1.f); W[1] = toh(e);
    EMIT2(10, F[10], F[11]);
    F[12] = hmul(F[8], F[4]); F[13] = hmul(F[8], F[5]);
    e = csq(e); W[2] = toh(e);
    EMIT2(12, F[12], F[13]);
    F[14] = hmul(F[8], F[6]); F[15] = hmul(F[8], F[7]);
    e = csq(e); W[4] = toh(e);
    EMIT2(14, F[14], F[15]);
    u32 ce = max(max(pCr & 0x7fffu, (pCr >> 16) & 0x7fffu), max(pCi & 0x7fffu, (pCi >> 16) & 0x7fffu));
    ce = min(max(wavemax(ce) >> 7, 1u), 253u);
    const float cdn = __uint_as_float((254u - ce) << 23), cup = __uint_as_float(ce << 23) * (2.f * dt);
    G[0] = toh(c2{unpk(pCr) * cdn, unpk(pCi) * cdn});
    t1 = hmul(G[0], W[1]);
    SB();
#pragma unroll
    for (int ks = 0; ks < 8; ++ks) ff[ks] = lds128(tr + ks * 64);
    cbar();
    ldu(0);
    EMIT2(0, G[0], t1);
    W[3] = hmul(W[1], W[2]); t0 = hmul(G[0], W[2]); t1 = hmul(G[0], W[3]);
    EMIT2(2, t0, t1);
    W[5] = hmul(W[4], W[1]); t0 = hmul(G[0], W[4]); t1 = hmul(G[0], W[5]);
    EMIT2(4, t0, t1);
    W[6] = hmul(W[4], W[2]); W[7] = hmul(W[4], W[3]); t0 = hmul(G[0], W[6]); t1 = hmul(G[0], W[7]);
    ldu(1);
    EMIT2(6, t0, t1);
    e = cexp2(lr, rev, 128.f); H = toh(e);
    G[1] = hmul(G[0], H); t1 = hmul(G[1], W[1]);
    EMIT2(8, G[1], t1);
    t0 = hmul(G[1], W[2]); t1 = hmul(G[1], W[3]);
    e = csq(e); H = toh(e);
    EMIT2(10, t0, t1);
    t0 = hmul(G[1], W[4]); t1 = hmul(G[1], W[5]); G[2] = hmul(G[0], H);
    ldu(2);
    EMIT2(12, t0, t1);
    t0 = hmul(G[1], W[6]); t1 = hmul(G[1], W[7]); G[3] = hmul(G[1], H);
    EMIT2(14, t0, t1);
    t1 = hmul(G[2], W[2]);
    e = csq(e); H = toh(e);
    KRD();
    EMIT2(0, G[2], t1);
    t0 = hmul(G[2], W[4]); t1 = hmul(G[2], W[6]); G[4] = hmul(G[0], H);
    ldu(3);
    EMIT2(2, t0, t1);
    t1 = hmul(G[3], W[2]); G[5] = hmul(G[1], H); KM2(0);
    EMIT2(4, G[3], t1);
    t0 = hmul(G[3], W[4]); t1 = hmul(G[3], W[6]); KM2(1);
    EMIT2(6, t0, t1);
    t1 = hmul(G[4], W[4]); G[6] = hmul(G[2], H); KM2(2);
    EMIT2(8, G[4], t1);
    t1 = hmul(G[5], W[4]); G[7] = hmul(G[3], H); KM2(3);
    EMIT2(10, G[5], t1);
    t1 = hmul(G[6], W[4]);
    e = csq(e); H = toh(e);
    EMIT2(12, G[6], t1);
    t1 = hmul(G[7], W[4]); G[8] = hmul(G[0], H);
    sts128(kc + (128 * (g >> 1) + 4 * (g & 1) + 8 * r16) * 4, asu(ka * cup));
    EMIT2(14, G[7], t1);
    G[9] = hmul(G[1], H); G[10] = hmul(G[2], H);
    KRD();
    EMIT2(0, G[8], G[9]);
    G[11] = hmul(G[3], H); G[12] = hmul(G[4], H); G[13] = hmul(G[5], H);
    EMIT2(2, G[10], G[11]);
    G[14] = hmul(G[6], H); KM2(0); KM2(1);
    EMIT2(4, G[12], G[13]);
    G[15] = hmul(G[7], H); G[16] = hmul(G[8], H); KM2(2); KM2(3);
    EMIT2(6, G[14], G[15]);
    EMIT1(8, G[16]);
    KRD();
    {
        LAS char* k0 = kc + (g < 2 ? 256 + 64 * g + 4 * r16 : 384 + 64 * (g - 2) + 2 * r16) * 4;
        const u32x4 kb = asu(ka * cup);
        sts64(k0, kb.x, kb.y);
        sts64(k0 + (g < 2 ? 8 : 128), kb.z, kb.w);
    }
    SB();
    KM2(0); KM2(1); KM2(2); KM2(3);
    {
        LAS char* ud = sm0 + S_UT + 512 + trow * 2;
        sts64(ud, PERM(uu[1].x, uu[0].x, 0x05040100u), PERM(uu[3].x, uu[2].x, 0x05040100u));
        sts64(ud + 4608, PERM(uu[1].x, uu[0].x, 0x07060302u), PERM(uu[3].x, uu[2].x, 0x07060302u));
        sts64(ud + 81920, PERM(uu[1].y, uu[0].y, 0x05040100u), PERM(uu[3].y, uu[2].y, 0x05040100u));
        sts64(ud + 81920 + 4608, PERM(uu[1].y, uu[0].y, 0x07060302u), PERM(uu[3].y, uu[2].y, 0x07060302u));
    }
    {
        LAS float* k0 = (LAS float*)(kc + (512 + 64 * g + r16) * 4);
        k0[0] = ka.x * cup; k0[16] = ka.y * cup; k0[32] = ka.z * cup; k0[48] = ka.w * cup;
    }
    wg_barrier();
    const rsrc_t rws = mkrsrc(a.ws, WS_END);
    const u32x4 dws = mkdesc(a.ws, WS_END);
    u32 tch = touch(dws, lane < 16 ? WS_YX + (2 * sb + (lane >> 3)) * 8192 + G8 * 1024 + (lane & 7) * 128 : lane == 16 ? WS_YC + (sb * 8 + G8) * 128 : OOB);
    u32x4 U[2][8];
    auto rdU = [&](int it, int bq) { U[it & 1][bq] = lds128(ubase + 512 * bq + 192 - 64 * it); };
    {
        LAS char* k0 = sm + S_KC + ch * 6144;
        LAS char* Rb = sm + S_P + ch * 2688;
        const int t = tid & 127, e1 = min(t + 128, 167);
        f32x4 a0 = asf(lds128(k0 + t * 16)), b0 = asf(lds128(k0 + 3072 + t * 16)), a1 = asf(lds128(k0 + e1 * 16)), b1 = asf(lds128(k0 + 3072 + e1 * 16));
        const float n0 = *(LAS float*)(k0 + t * 16 + 16) + *(LAS float*)(k0 + 3072 + t * 16 + 16);
        const float n1 = *(LAS float*)(k0 + e1 * 16 + 16) + *(LAS float*)(k0 + 3072 + e1 * 16 + 16);
#pragma unroll
        for (int bq = 0; bq < 8; ++bq) rdU(3, bq);
        a0 += b0; a1 += b1;
        float p0 = a0.x, p1 = a0.y, p2 = a0.z, p3 = a0.w, q0 = a1.x, q1 = a1.y, q2 = a1.z, q3 = a1.w;
        asm volatile("" : "+v"(p0), "+v"(p1), "+v"(p2), "+v"(p3), "+v"(q0), "+v"(q1), "+v"(q2), "+v"(q3));
        sts128(Rb + (163 - t) * 16, u32x4{pk(n0, p3), pk(p3, p2), pk(p2, p1), pk(p1, p0)});
        u32x4 v1 = {pk(n1, q3), pk(q3, q2), pk(q2, q1), pk(q1, q0)};
        if (t >= 36) v1 = u32x4{0, 0, 0, 0};
        if (t < 39) sts128(Rb + (t < 36 ? 35 - t : 129 + t) * 16, v1);
        if (t == 0) sts128(Rb + 164 * 16, u32x4{pk(p0, 0.f), 0, 0, 0});
    }
    wg_barrier();
    f32x4 y[8];
    {
#define Q0(i) P0[102 - (i)]
#define Q1(i) P1[132 - (i)]
#define Q2(i) P2[130 - (i)]
#define Q3(i) P3[224 - (i)]
        u32x4 T[2][4], T32, TB[4], UW[4], WA[2];
        f32x4 wd[4];
        u32 raw[7], rx[10];
        auto rdT = [&](int it, int k) {
            u32x4& f0 = T[it & 1][0];
            if (k == 0) { f0.x = Q0(32 * it + 6); f0.y = Q0(32 * it + 4); }
            if (k == 1) { f0.z = Q0(32 * it + 2); f0.w = Q0(32 * it); }
            if (k == 2) { raw[0] = Q1(16 * it); raw[1] = Q1(16 * it + 2); raw[2] = Q1(16 * it + 4); }
            if (k == 3) { raw[3] = Q2(8 * it); raw[4] = Q2(8 * it + 2); }
            if (k == 4) { raw[5] = Q2(8 * it + 64); raw[6] = Q2(8 * it + 66); }
        };
        auto mkT = [&](int it, int aa, int e) {
            T[it & 1][aa][e] = aa == 1 ? (e < 2 ? PERM(raw[2], raw[1], sel[1][2 + e]) : PERM(raw[1], raw[0], sel[1][e])) : PERM(raw[2 * aa], raw[2 * aa - 1], sel[2][e]);
        };
        auto rdX = [&](int k) {
            if (k == 0) { rx[0] = Q0(262); rx[1] = Q0(260); }
            if (k == 1) { rx[2] = Q0(258); rx[3] = Q0(256); }
            if (k == 2) { rx[4] = Q1(128); rx[5] = Q1(130); rx[6] = Q1(132); }
            if (k == 3) { rx[7] = Q2(128); rx[8] = Q2(130); rx[9] = R[-512]; }
        };
        auto fixT = [&](int k, int e) {
            if (k < 2) {
                const u32 lo = k == 0 ? rx[e] : (e < 2 ? PERM(rx[6], rx[5], sel[1][2 + e]) : PERM(rx[5], rx[4], sel[1][e]));
                T[0][1 + k][e] = (lo & msk[e]) | (T[0][1 + k][e] & ~msk[e]);
            } else T32[e] = (PERM(rx[8], rx[7], sel[2][e]) & msk[e]) | (PERM(rx[9], rx[9], 0x03020302u) & mkb[e]);
        };
        LAS char* uw = ubase + 192 + 256 * hf + 1024 * (1 - hf);
        auto rdC = [&](int k) {
            if (k < 4) UW[k] = lds128(uw + 512 * (k >> 1) - 16 * (k & 1));
            else { const int dl = (k - 4) >> 1, e = 2 * (k & 1); TB[dl][e] = Q3(128 + 32 * dl - 2 * e); TB[dl][e + 1] = Q3(128 + 32 * dl - 2 * e - 2); }
        };
#pragma unroll
        for (int k = 0; k < 5; ++k) rdT(3, k);
        SB();
        sfor<104>([&](auto gs) {
            constexpr int it = 3 - gs.v / 26, sl = gs.v % 26, aa = tri_a(sl), bq = tri_b(sl);
            y[aa + bq] = mfma(T[it & 1][aa], U[it & 1][bq], (it == 3 && aa == 0) ? zero4() : y[aa + bq]);
            SB();
            if constexpr (it == 3) {
                if constexpr (sl < 6) { mkT(3, 1 + (sl >> 1), 2 * (sl & 1)); mkT(3, 1 + (sl >> 1), 2 * (sl & 1) + 1); }
                else if constexpr (sl < 14) { if (sl < 11) rdT(2, sl - 6); rdU(2, sl - 6); }
                else mkT(2, 1 + ((sl - 14) >> 2), (sl - 14) & 3);
            } else if constexpr (it > 0) {
                if constexpr (sl < 8) { if (sl < 5) rdT(it - 1, sl); rdU(it - 1, sl); }
                if constexpr (it == 1 && sl >= 5 && sl < 9) rdX(sl - 5);
                if constexpr (it == 2 && sl >= 10 && sl < 22) mkT(1, 1 + ((sl - 10) >> 2), (sl - 10) & 3);
                if constexpr (it == 1 && sl >= 10 && sl < 16) { mkT(0, 1 + ((sl - 10) >> 1), 2 * (sl & 1)); mkT(0, 1 + ((sl - 10) >> 1), 2 * (sl & 1) + 1); }
                if constexpr (it == 1 && sl >= 16 && sl < 24) fixT((sl - 16) >> 2, (sl - 16) & 3);
            } else {
                if constexpr (sl < 12) rdC(sl);
                if constexpr (sl < 4) fixT(2, sl);
                if constexpr (sl >= 10 && sl < 14) wd[sl - 10] = mfma(UW[sl - 10], (sl & 1) ? bE : bO, zero4());
                if constexpr (sl >= 16 && sl < 24) WA[(sl - 16) >> 2][sl & 3] = pk(wd[2 * ((sl - 16) >> 2) + 1][sl & 3], wd[2 * ((sl - 16) >> 2)][sl & 3]);
                if constexpr (sl == 24) TB[0] &= mkd;
            }
            SB();
        });
        if (hf) {
#pragma unroll
            for (int k = 0; k < 4; ++k) y[4 + k] = mfma(WA[0], TB[k], y[4 + k]);
#pragma unroll
            for (int k = 0; k < 3; ++k) y[5 + k] = mfma(WA[1], TB[k], y[5 + k]);
        } else {
#pragma unroll
            for (int k = 0; k < 4; ++k) y[4 + k] = mfma(T32, U[0][k], y[4 + k]);
            y[6] = mfma(WA[0], TB[0], y[6]); y[7] = mfma(WA[0], TB[1], y[7]); y[7] = mfma(WA[1], TB[0], y[7]);
        }
    }
    {
        LAS char* yd = sm + S_TAB + wave * 8192 + lane * 16;
#pragma unroll
        for (int i = 0; i < 8; ++i) sts128(yd + i * 1024, asu(y[i]));
    }
    asm volatile("s_waitcnt vmcnt(0)" : "+v"(tag), "+v"(tch) :: "memory");
    tag += 1;
    wg_barrier();
    {
        LAS char* ys = sm0 + S_TAB + G8 * 1024 + lane * 16;
        u32x4 o[2];
#pragma unroll
        for (int c = 0; c < 2; ++c) {
            const f32x4 y0 = asf(lds128(ys + c * 81920)) + asf(lds128(ys + c * 81920 + 8192));
            const f32x4 y1 = asf(lds128(ys + c * 81920 + 16384)) + asf(lds128(ys + c * 81920 + 24576));
            const float d0 = bflo(dsk[c]), d1 = bfhi(dsk[c]);
#pragma unroll
            for (int i = 0; i < 4; ++i) o[c][i] = pk(y0[i] + bflo(uu[i][c]) * d0, y1[i] + bfhi(uu[i][c]) * d1);
            bst_dev(o[c], rws, WS_YX + (2 * sb + c) * 8192 + G8 * 1024 + lane * 16);
        }
        const u32 s0 = rowsum(sum4(o[0])), s1 = rowsum(sum4(o[1]));
        bst32_dev(tag - (r16 ? s1 : s0), rws, r16 < 2 ? WS_YC + (sb * 8 + G8) * 128 + g * 8 + r16 * 4 : OOB);
    }
    {
        const u32 cofs = (ct * 32 + (tid >> 4)) * 8192 + j * 256 + (tid & 15) * 16;
        const u32 ccs = ((ct * 16 + (tid >> 5)) * 8 + (j >> 2)) * 128 + (j & 3) * 8 + ((tid >> 4) & 1) * 4;
        LAS char* trw = sm + S_TR + (tid & 7) * 288 + (tid >> 3) * 8;
        LAS char* trr = sm + S_TR + (((tid >> 4) & 3) * 8 + (tid >> 6)) * 288 + (tid & 15) * 16;
        const u32 oo = (256 * (j >> 2) + 16 * (tid >> 4) + 4 * (j & 3) + 2 * ((tid >> 3) & 1)) * 1792 + 128 * ct + (tid & 7) * 16;
        waitv<0>();
        u32x4 v[2]; u32 vc[2];
#pragma unroll
        for (int i = 0; i < 2; ++i) { v[i] = bld_dev(rws, cofs, WS_YX + i * 16 * 8192); vc[i] = bld32_dev(rws, ccs, WS_YC + i * 8192); }
        cbar();
        auto ok = [&](int i) { return rowsum(sum4(v[i])) + vc[i] == tag; };
        settle<2>(badmask<2>(ok), ok,
                  [&](auto i, u32 bm) { reld<i.v>(v[i.v], dws, cofs, WS_YX + i.v * 16 * 8192, bm); reld32<i.v>(vc[i.v], dws, ccs, WS_YC + i.v * 8192, bm); },
                  [&](int i) { pin(v[i]); pin(vc[i]); });
        sts128(trr, v[0]); sts128(trr + 4 * 288, v[1]);
        wg_barrier();
        const u32x2 s0 = *(LAS u32x2*)trw, s1 = *(LAS u32x2*)(trw + 8 * 288), s2 = *(LAS u32x2*)(trw + 16 * 288), s3 = *(LAS u32x2*)(trw + 24 * 288);
        const rsrc_t ro = mkrsrc(a.out, 2048 * 1792);
        bst_dev(u32x4{s0.x, s1.x, s2.x, s3.x}, ro, oo);
        bst_dev(u32x4{s0.y, s1.y, s2.y, s3.y}, ro, oo + 1792);
        if (j == 0 && tid == 0) st_epoch(a.ws + WS_EPS + ct * 256, tag);
    }
}

DI void warm_attn(const Args& a, LAS char* sm, int xcd, int hi) {
    const int lane = threadIdx.x & 63, wave8 = __builtin_amdgcn_readfirstlane(threadIdx.x >> 6);
    const int u = hi * 5 + wave8, idx = (u - 24) * 64 + lane;
    if (wave8 >= 5) return;
    setm0(sm + 163840 - 256);
    if (u < 24) {
        const int rbl = (xcd >> 1) * 4 + u / 6, kc = (2 * (rbl + (xcd & 1) * 4) + 1 + 2 * ((u % 6) >> 1)) % 7;
        touch_lds(mkdesc(a.x, 2048 * 896), (rbl * 128 + (u & 1) * 64 + lane) * 896 + kc * 128);
    } else touch_lds(mkdesc(a.wqkv, 448 * 2688), (idx >> 3) * 2688 + (1 + ((idx >> 2) & 1)) * 896 + ((xcd & 1) * 3 + (idx & 3)) * 128);
}

#if defined(__HIP_DEVICE_COMPILE__)
#define NOPK __attribute__((target("no-packed-fp32-ops")))
#else
#define NOPK
#endif
__global__ NOPK __launch_bounds__(512, 2) void fused_kernel(Args a) {
    __shared__ __attribute__((aligned(16))) char smem[163840];
    LAS char* sm = (LAS char*)smem;
    const int xcd = blockIdx.x & 7, j = blockIdx.x >> 3;
    const bool is_attn = (j & 1) == 0;
    if (__builtin_expect(!is_attn, 1)) warm_attn(a, sm, xcd, j >> 1);
    cbar();
    if (__builtin_expect(is_attn, 1)) attn_role(a, sm, xcd, j >> 1);
    else if (j < 28) ssm_role(a, sm, xcd * 14 + (j >> 1));
    else {
        const int t = (j >> 1 & 1) * 512 + threadIdx.x;
        setm0(sm);
#pragma unroll
        for (int i = 0; i < 2; ++i) touch_lds(mkdesc(a.wqkv, 448 * 2688), ((t + 1024 * i) >> 2) * 2688 + ((xcd & 1) * 3 + (t & 3)) * 128);
#pragma unroll
        for (int i = 0; i < 4; ++i) touch_lds(mkdesc(a.wo, 448 * 896), (t + 1024 * i) * 128);
        waitv<0>();
    }
}

extern "C" void kernel_launch(void* const* d_in, const int* in_sizes, int n_in,
                              void* d_out, int out_size, void* d_ws, size_t ws_size,
                              hipStream_t stream) {
    Args a;
    a.u = (const u16*)d_in[0]; a.logA = (const u16*)d_in[1]; a.Aim = (const u16*)d_in[2];
    a.Cre = (const u16*)d_in[3]; a.Cim = (const u16*)d_in[4]; a.logdt = (const u16*)d_in[5];
    a.Dskip = (const u16*)d_in[6]; a.x = (const u16*)d_in[7]; a.wqkv = (const u16*)d_in[8];
    a.wo = (const u16*)d_in[9]; a.out = (u16*)d_out; a.ws = (char*)d_ws;
    fused_kernel<<<256, 512, 0, stream>>>(a);
}
```

```cpp
#include <hip/hip_runtime.h>
#include <stdint.h>

typedef unsigned short u16;
typedef unsigned int u32;
typedef unsigned long long u64;
typedef __bf16 bf16x8 __attribute__((ext_vector_type(8)));
typedef __bf16 bf16x2 __attribute__((ext_vector_type(2)));
typedef _Float16 h2 __attribute__((ext_vector_type(2)));
typedef _Float16 h8 __attribute__((ext_vector_type(8)));
typedef short s16x4 __attribute__((ext_vector_type(4)));
typedef float f32x2 __attribute__((ext_vector_type(2)));
typedef float f32x4 __attribute__((ext_vector_type(4)));
typedef float f32x16 __attribute__((ext_vector_type(16)));
typedef u32 u32x2 __attribute__((ext_vector_type(2)));
typedef u32 u32x4 __attribute__((ext_vector_type(4)));
typedef __amdgpu_buffer_rsrc_t rsrc_t;
#define LAS __attribute__((address_space(3)))
#define GAS __attribute__((address_space(1)))
#define DI __device__ __forceinline__
#define SB() __builtin_amdgcn_sched_barrier(0)
#define PERM __builtin_amdgcn_perm

constexpr u32 WS_EPA = 0;
constexpr u32 WS_EPS = 256;
constexpr u32 WS_YC  = 65536;
constexpr u32 WS_KVP = 524288;
constexpr u32 WS_OX  = WS_KVP + (1u << 19);
constexpr u32 WS_YX  = WS_OX + (2u << 20);
constexpr u32 WS_END = WS_YX + (2u << 20);
constexpr u32 OOB = 0x80000000u;
constexpr int SPIN_CAP = 1 << 16;

struct Args {
    const u16 *u, *logA, *Aim, *Cre, *Cim, *logdt, *Dskip, *x, *wqkv, *wo;
    u16* out;
    char* ws;
};

DI f32x4 mfma(u32x4 a, u32x4 b, f32x4 c) { return __builtin_amdgcn_mfma_f32_16x16x32_bf16(__builtin_bit_cast(bf16x8, a), __builtin_bit_cast(bf16x8, b), c, 0, 0, 0); }
DI f32x4 mfmah(u32x4 a, u32x4 b, f32x4 c) { return __builtin_amdgcn_mfma_f32_16x16x32_f16(__builtin_bit_cast(h8, a), __builtin_bit_cast(h8, b), c, 0, 0, 0); }
DI f32x16 mfma32(u32x4 a, u32x4 b, f32x16 c) { return __builtin_amdgcn_mfma_f32_32x32x16_bf16(__builtin_bit_cast(bf16x8, a), __builtin_bit_cast(bf16x8, b), c, 0, 0, 0); }
DI u32 pk(float lo, float hi) { f32x2 v = {lo, hi}; return __builtin_bit_cast(u32, __builtin_convertvector(v, bf16x2)); }
DI float bflo(u32 v) { return __uint_as_float(v << 16); }
DI float bfhi(u32 v) { return __uint_as_float(v & 0xffff0000u); }
DI f32x2 unpk(u32 v) { return f32x2{bflo(v), bfhi(v)}; }
DI f32x4 asf(u32x4 v) { return __builtin_bit_cast(f32x4, v); }
DI u32x4 asu(f32x4 v) { return __builtin_bit_cast(u32x4, v); }
DI f32x4 zero4() { return f32x4{0.f, 0.f, 0.f, 0.f}; }
DI f32x16 zero16() { f32x16 z; for (int i = 0; i < 16; ++i) z[i] = 0.f; return z; }
DI u32x4 pk8(const f32x16& a, int s) { return u32x4{pk(a[8 * s], a[8 * s + 1]), pk(a[8 * s + 2], a[8 * s + 3]), pk(a[8 * s + 4], a[8 * s + 5]), pk(a[8 * s + 6], a[8 * s + 7])}; }
DI u32x4 quad(const f32x16& a, int r) { return asu(f32x4{a[4 * r], a[4 * r + 1], a[4 * r + 2], a[4 * r + 3]}); }
template <class T> DI void pin(T& v) { asm volatile("" : "+v"(v) :: "memory"); }
template <int I> struct IC { static constexpr int v = I; };
template <int N, int I = 0, class F> DI void sfor(F f) { if constexpr (I < N) { f(IC<I>{}); sfor<N, I + 1>(f); } }
DI void cbar() { asm volatile("" ::: "memory"); }
DI void raw_barrier() { asm volatile("s_barrier" ::: "memory"); }
DI void wg_barrier() { asm volatile("s_waitcnt lgkmcnt(0)\n\ts_barrier" ::: "memory"); }
template <int N> DI void waitv() { asm volatile("s_waitcnt vmcnt(%0)" :: "i"(N) : "memory"); }
template <int N> DI void waitx(u32x4* x) { asm volatile("s_waitcnt vmcnt(%4)" : "+v"(x[0]), "+v"(x[1]), "+v"(x[2]), "+v"(x[3]) : "i"(N) : "memory"); }

DI u32x4 lds128(LAS char* p) { return *(LAS u32x4*)p; }
DI void sts128(LAS char* p, u32x4 v) { *(LAS u32x4*)p = v; }
DI void sts64(LAS char* p, u32 lo, u32 hi) { u32x2 v = {lo, hi}; *(LAS u32x2*)p = v; }
DI u32x4 trfrag(LAS char* p0, LAS char* p1) {
    u32x2 a = __builtin_bit_cast(u32x2, __builtin_amdgcn_ds_read_tr16_b64_v4i16((LAS s16x4*)p0));
    u32x2 b = __builtin_bit_cast(u32x2, __builtin_amdgcn_ds_read_tr16_b64_v4i16((LAS s16x4*)p1));
    return u32x4{a.x, a.y, b.x, b.y};
}

DI rsrc_t mkrsrc(const void* p, u32 bytes) { return __builtin_amdgcn_make_buffer_rsrc((void*)p, 0, bytes, 0x00020000); }
DI u32x4 mkdesc(const void* p, u32 bytes) { u64 v = (u64)p; return u32x4{(u32)v, (u32)(v >> 32) & 0xffffu, bytes, 0x00020000u}; }
DI u32x4 bld_dev(rsrc_t r, u32 voff, u32 soff) { return __builtin_amdgcn_raw_buffer_load_b128(r, voff, soff, 16); }
DI u32 bld32_dev(rsrc_t r, u32 voff, u32 soff) { return __builtin_amdgcn_raw_buffer_load_b32(r, voff, soff, 16); }
DI void bst_dev(u32x4 v, rsrc_t r, u32 voff) { __builtin_amdgcn_raw_buffer_store_b128(v, r, voff, 0, 16); }
DI void bst32_dev(u32 v, rsrc_t r, u32 voff) { __builtin_amdgcn_raw_buffer_store_b32(v, r, voff, 0, 16); }
DI void st_epoch(char* p, u32 v) { __hip_atomic_store((GAS u32*)p, v, __ATOMIC_RELAXED, __HIP_MEMORY_SCOPE_AGENT); }
DI void setm0(LAS char* dst) { asm volatile("s_mov_b32 m0, %0\n\ts_nop 0" :: "s"((u32)(uintptr_t)dst) : "memory"); }
template <int OFF>
DI void dma(u32x4 desc, u32 voff, u32 soff) { asm volatile("buffer_load_dwordx4 %0, %1, %2 offen offset:%3 lds" :: "v"(voff), "s"(desc), "s"(soff), "i"(OFF) : "memory"); }
DI void ldd(u32x4& v, u32x4 desc, u32 voff, u32 soff) { asm volatile("buffer_load_dwordx4 %0, %1, %2, %3 offen sc1" : "=v"(v) : "v"(voff), "s"(desc), "s"(soff) : "memory"); }
DI u32 ld_epoch(const char* p) { u32 v; asm volatile("global_load_dword %0, %1, %2 sc1" : "=v"(v) : "v"(0u), "s"(p) : "memory"); return v; }
DI u32 touch(u32x4 desc, u32 voff) { u32 v; asm volatile("buffer_load_dword %0, %1, %2, 0 offen" : "=v"(v) : "v"(voff), "s"(desc) : "memory"); return v; }
DI void touch_lds_dev(u32x4 desc, u32 voff) { asm volatile("buffer_load_dword %0, %1, 0 offen sc1 lds" :: "v"(voff), "s"(desc) : "memory"); }
DI void touch_lds(u32x4 desc, u32 voff) { asm volatile("buffer_load_dword %0, %1, 0 offen lds" :: "v"(voff), "s"(desc) : "memory"); }
template <int BIT> DI void reld(u32x4& v, u32x4 desc, u32 voff, u32 soff, u32 mask) {
    asm volatile("s_bitcmp1_b32 %4, %5\n\ts_cbranch_scc0 1f\n\tbuffer_load_dwordx4 %0, %1, %2, %3 offen sc1\n1:"
                 : "+v"(v) : "v"(voff), "s"(desc), "s"(soff), "s"(mask), "i"(BIT) : "scc", "memory");
}
template <int BIT> DI void reld32(u32& v, u32x4 desc, u32 voff, u32 soff, u32 mask) {
    asm volatile("s_bitcmp1_b32 %4, %5\n\ts_cbranch_scc0 1f\n\tbuffer_load_dword %0, %1, %2, %3 offen sc1\n1:"
                 : "+v"(v) : "v"(voff), "s"(desc), "s"(soff), "s"(mask), "i"(BIT) : "scc", "memory");
}
template <int N, class OK, class LD, class PIN>
DI void settle(u32 bad, OK ok, LD ld, PIN pn) {
#pragma unroll 1
    for (int sp = 0; bad != 0 && sp < SPIN_CAP; ++sp) {
        __builtin_amdgcn_s_sleep(2);
        sfor<N>([&](auto i) { ld(i, bad); });
        waitv<0>();
        sfor<N>([&](auto i) { pn(i.v); });
        sfor<N>([&](auto i) { if ((bad >> i.v & 1) && __all(ok(i.v))) bad &= ~(1u << i.v); });
    }
}
template <int N, class OK> DI u32 badmask(OK ok) { u32 bad = 0; sfor<N>([&](auto i) { if (!__all(ok(i.v))) bad |= 1u << i.v; }); return bad; }

DI u32 sum4(u32x4 v) { return v.x + v.y + v.z + v.w; }
DI u32 rowsum(u32 s) {
    s += __builtin_amdgcn_update_dpp(0, s, 0x128, 0xf, 0xf, false);
    s += __builtin_amdgcn_update_dpp(0, s, 0x124, 0xf, 0xf, false);
    s += __builtin_amdgcn_update_dpp(0, s, 0x122, 0xf, 0xf, false);
    s += __builtin_amdgcn_update_dpp(0, s, 0x121, 0xf, 0xf, false);
    return s;
}
DI u32 sum32(u32 s) { s = rowsum(s); auto t = __builtin_amdgcn_permlane16_swap(s, s, false, false); return t[0] + t[1]; }
DI u32 halfsum(u32 s) { auto r = __builtin_amdgcn_permlane32_swap(s, s, false, false); return r[0] + r[1]; }
DI u32 wavemax(u32 s) {
    s = max(s, (u32)__builtin_amdgcn_update_dpp(0, s, 0x128, 0xf, 0xf, false));
    s = max(s, (u32)__builtin_amdgcn_update_dpp(0, s, 0x124, 0xf, 0xf, false));
    s = max(s, (u32)__builtin_amdgcn_update_dpp(0, s, 0x122, 0xf, 0xf, false));
    s = max(s, (u32)__builtin_amdgcn_update_dpp(0, s, 0x121, 0xf, 0xf, false));
    auto r = __builtin_amdgcn_permlane32_swap(s, s, false, false);
    s = max(r[0], r[1]);
    auto t = __builtin_amdgcn_permlane16_swap(s, s, false, false);
    return max(t[0], t[1]);
}

DI float phi(float t) {
    const float e = __builtin_amdgcn_exp2f(t * 1.44269504089f);
    return __int_as_float(max(__float_as_int(t), 0x358637bd)) + __builtin_amdgcn_fmed3f(e, 0.f, 1.f);
}

constexpr int A_RED = 0, A_WO = 32768, A_WQ = 98304, A_KVB = 155648;
constexpr int PRO = 8;
constexpr int LA = 6;
constexpr int QS0 = 40;
constexpr int GK0 = 14;
constexpr int G2A = 2;
constexpr int issued_before(int gs) {
    constexpr int B0 = 16 - LA;
    int n = PRO + gs;
    int cap = 16 + (gs < B0 ? 0 : 4 * ((gs - B0) / 8) + ((gs - B0) % 8 < 4 ? (gs - B0) % 8 : 4));
    n = n < cap ? n : cap;
    if (n < 28) return n;
    const int q = gs - QS0;
    return 28 + (q < 0 ? 0 : q > 7 ? 7 : q);
}
static_assert(QS0 >= 40 - LA, "Wq overwrites ring slot 3");
constexpr int slot_ch(int gs) { return gs < 40 ? gs >> 3 : 5 + ((gs >> 2) & 1); }
constexpr int slot_kk(int gs) { return gs < 40 ? (gs & 7) >> 1 : gs & 3; }
constexpr int slot_sg(int gs) { return gs < 40 ? gs & 1 : gs >= 48; }

DI void attn_role(const Args& a, LAS char* sm, const int xcd, const int j) {
    const int tid = threadIdx.x, lane = tid & 63, wave = __builtin_amdgcn_readfirstlane(tid >> 6);
    const int rw4 = wave & 3, hs = wave >> 2;
    const int hp = (xcd & 1) * 4 + (j & 3), rb = (xcd >> 1) * 4 + (j >> 2);
    const int h = 2 * hp + hs;
    const int c0 = (2 * rb + 2 * hp) % 7;
    const int r32 = lane & 31, hh = lane >> 5, blk = (lane >> 4) & 1, q = (lane >> 2) & 3, p = lane & 3;
    const u32x4 rx = mkdesc(a.x, 2048 * 896), rw = mkdesc(a.wqkv, 448 * 2688), rwo = mkdesc(a.wo, 448 * 896), dws = mkdesc(a.ws, WS_END);
    const rsrc_t rws = mkrsrc(a.ws, WS_END);
    u32 tag = ld_epoch(a.ws + WS_EPA);
    const int widx = (lane & 15) ^ ((lane >> 4) << 2);
    const u32 wv = (8 * wave + (lane >> 4)) * 2688 + (1 + (widx >> 3)) * 896 + hp * 112 + min(widx & 7, 6) * 16;
    const u32 xv0 = (rb * 128 + wave * 16 + (lane >> 3)) * 896 + (((lane & 7) ^ (lane >> 4)) * 16);
    const u32 xv1 = (rb * 128 + wave * 16 + 8 + (lane >> 3)) * 896 + (((lane & 7) ^ (4 + (lane >> 4))) * 16) - 1024;
    const u32 qv = (8 * wave + (lane >> 3)) * 2688 + hp * 112 + min((lane & 7) ^ (blk << 2), 6) * 16;
    auto kchunk = [&](int i) { int kc = i + c0; return kc >= 7 ? kc - 7 : kc; };
    auto ldn = [&](int n) {
        if (n >= 28) { setm0(sm + A_WQ + (n - 28) * 8192 + wave * 1024); dma<0>(rw, qv, kchunk(n - 28) * 64 * 2688); return; }
        const int i = n >> 2, m = n & 3;
        if (m == 0) { setm0(sm + (i & 3) * 32768 + 16384 + wave * 2048); dma<0>(rx, xv0, kchunk(i) * 128); }
        if (m == 1) dma<1024>(rx, xv1, kchunk(i) * 128);
        if (m == 2) { setm0(sm + (i & 3) * 32768 + wave * 2048); dma<0>(rw, wv, kchunk(i) * 64 * 2688); }
        if (m == 3) dma<1024>(rw, wv, kchunk(i) * 64 * 2688 + 4 * 2688 - 1024);
    };
#pragma unroll
    for (int n = 0; n < PRO; ++n) ldn(n);

    const int wb = 56 * hs + 32 * blk + 8 * p;
    LAS char *wsrc[4], *xsrc[8];
#pragma unroll
    for (int sg = 0; sg < 2; ++sg) { wsrc[sg] = sm + (32 * hh + q) * 256 + (((2 * sg + (wb >> 6)) ^ q) << 6) + (wb & 63); wsrc[2 + sg] = wsrc[sg] + 65536; pin(wsrc[2 + sg]); }
#pragma unroll
    for (int kk = 0; kk < 4; ++kk) { xsrc[kk] = sm + 16384 + (32 * rw4 + r32) * 128 + (((4 * hh + kk) ^ ((r32 >> 1) & 7)) * 16); xsrc[4 + kk] = xsrc[kk] + 65536; pin(xsrc[4 + kk]); }

    const bool p3 = j < 14;
    const int r128 = p3 ? xcd * 2 + j / 7 : 0, nb = j % 7;
    f32x16 acc[2] = {zero16(), zero16()};
    u32x4 xf[28], wf[56];
    auto rdw = [&](int gs) { LAS char* s = wsrc[slot_sg(gs) + (slot_ch(gs) & 2)] + (slot_ch(gs) & 1) * 32768 + slot_kk(gs) * 2048; wf[gs] = trfrag(s, s + 1024); };
    auto rdx = [&](int i, int kk) { xf[4 * i + kk] = lds128(xsrc[kk + 2 * (i & 2)] + (i & 1) * 32768); };
    waitv<PRO - 4>();
    wg_barrier();
#pragma unroll
    for (int sl = 0; sl < LA; ++sl) { if (sl < 4) rdx(0, sl); rdw(sl); }
    u32 pkd[8];
    u32 vm = r32 == 28 ? 0u : ~0u, vo = r32 == 28 ? 0x3f803f80u : 0u;
    pin(vm); pin(vo);
    sfor<56>([&](auto gs) {
        constexpr int issued = issued_before(gs.v);
        constexpr int fu = gs.v + LA, c = fu == 44 ? 6 : (fu <= 40 && (fu & 7) == 0) ? fu >> 3 : 0;
        static_assert(!c || issued >= 4 * c + 4, "chunk not issued yet");
        if constexpr (c != 0) { waitv<issued - (4 * c + 4)>(); raw_barrier(); }
        SB();
        acc[slot_sg(gs.v)] = mfma32(xf[4 * slot_ch(gs.v) + slot_kk(gs.v)], wf[gs.v], acc[slot_sg(gs.v)]);
        SB();
        if constexpr (fu < 40 && fu >= 8 && (fu & 1) == 0) rdx(fu >> 3, (fu & 7) >> 1);
        if constexpr (fu >= 40 && fu < 48) rdx(slot_ch(fu), fu & 3);
        if constexpr (fu < 56) rdw(fu);
        if constexpr (gs.v == 44) raw_barrier();
        if constexpr (gs.v >= 49) { pkd[gs.v - 49] = pk(phi(acc[0][2 * (gs.v - 49)]), phi(acc[0][2 * (gs.v - 49) + 1])); pin(pkd[gs.v - 49]); }
#pragma unroll
        for (int n = issued; n < issued_before(gs.v + 1); ++n) ldn(n);
        SB();
    });
    {
        pkd[7] = pk(phi(acc[0][14]), phi(acc[0][15]));
        pin(pkd[7]);
        f32x16 kv = mfma32(u32x4{pkd[0], pkd[1], pkd[2], pkd[3]}, (pk8(acc[1], 0) & vm) | vo, zero16());
        SB();
        kv = mfma32(u32x4{pkd[4], pkd[5], pkd[6], pkd[7]}, (pk8(acc[1], 1) & vm) | vo, kv);
#pragma unroll
        for (int rq = 0; rq < 4; ++rq) sts128(sm + A_RED + wave * 4096 + rq * 1024 + lane * 16, quad(kv, rq));
    }
    asm volatile("s_waitcnt vmcnt(7)" : "+v"(tag) :: "memory");
    tag += 1;
    wg_barrier();
    u32 hm = hh ? 0u : ~0u, cm = r32 == 31 ? 0u : ~0u;
    pin(hm); pin(cm);
    const bool red = (rw4 >> 1) == hs;
    if (red) {
        LAS char* rs = sm + A_RED + hs * 16384 + (rw4 & 1) * 2048 + lane * 16;
        f32x4 lo = (asf(lds128(rs)) + asf(lds128(rs + 4096))) + (asf(lds128(rs + 8192)) + asf(lds128(rs + 12288)));
        f32x4 hi = (asf(lds128(rs + 1024)) + asf(lds128(rs + 5120))) + (asf(lds128(rs + 9216)) + asf(lds128(rs + 13312)));
        u32x4 v = {pk(lo.x, lo.y), pk(lo.z, lo.w), pk(hi.x, hi.y), pk(hi.z, hi.w) & cm};
        v.w |= (tag - sum32(sum4(v))) & ~cm;
        bst_dev(v, rws, WS_KVP + (h * 16 + rb) * 2048 + (tid & 127) * 16);
    }
    cbar();
    if (red) waitv<1>(); else waitv<0>();
    raw_barrier();
    const u32 ov = (8 * wave + (lane >> 3)) * 896 + nb * 128 + (((lane & 7) ^ (blk << 2)) * 16);
    u32x4 gk[8];
    const u32 kbase = WS_KVP + (h * 16 + (rw4 >> 1) * 8 + (rb & 7)) * 2048, kvo = (tid & 127) * 16;
    f32x16 aq = zero16();
    {
        LAS char* qsrc = sm + A_WQ + (32 * hh + q) * 128 + (((wb >> 6) ^ (q >> 1)) << 6) + (wb & 63);
        pin(qsrc);
        u32x4 wq[28];
        auto rdq = [&](int k) { LAS char* s = qsrc + (k >> 2) * 8192 + (k & 3) * 1024; wq[k] = trfrag(s, s + 512); };
#pragma unroll
        for (int k = 0; k < 4; ++k) rdq(k);
        SB();
#pragma unroll
        for (int k = 0; k < 28; ++k) {
            aq = mfma32(wq[k], xf[k], aq);
            SB();
            if (k + 4 < 28) rdq(k + 4);
            if (k < 7) { setm0(sm + A_WO + k * 8192 + wave * 1024); dma<0>(rwo, ov, k * 64 * 896); }
            if (k >= GK0 && k < GK0 + 8) ldd(gk[k - GK0], dws, kvo, kbase ^ ((k - GK0) * 2048));
            SB();
        }
    }
    u32 pqd[8];
#pragma unroll
    for (int k = 0; k < 8; ++k) {
        pqd[k] = pk(phi(aq[2 * k]), phi(aq[2 * k + 1]));
        if (k >= 6) pqd[k] &= hm;
        pin(pqd[k]);
    }
    {
        waitx<0>(gk); waitx<0>(gk + 4);
        u32 t = 0;
#pragma unroll
        for (int i = 0; i < 8; ++i) t += sum4(gk[i]);
        if (!__all(sum32(t) == 8u * tag)) {
            auto ok = [&](int i) { return sum32(sum4(gk[i])) == tag; };
            settle<8>(badmask<8>(ok), ok, [&](auto i, u32 bm) { reld<i.v>(gk[i.v], dws, kvo, kbase ^ (i.v * 2048), bm); }, [&](int i) { pin(gk[i]); });
        }
    }
    {
        u32x4 v;
#pragma unroll
        for (int c = 0; c < 4; ++c) {
            const float lo = ((bflo(gk[0][c]) + bflo(gk[1][c])) + (bflo(gk[2][c]) + bflo(gk[3][c]))) + ((bflo(gk[4][c]) + bflo(gk[5][c])) + (bflo(gk[6][c]) + bflo(gk[7][c])));
            const float hi = ((bfhi(gk[0][c]) + bfhi(gk[1][c])) + (bfhi(gk[2][c]) + bfhi(gk[3][c]))) + ((bfhi(gk[4][c]) + bfhi(gk[5][c])) + (bfhi(gk[6][c]) + bfhi(gk[7][c])));
            v[c] = pk(lo, hi);
        }
        sts128(sm + A_KVB + tid * 16, v);
    }
    wg_barrier();
    {
        u32x4 kf[4];
#pragma unroll
        for (int i = 0; i < 4; ++i) kf[i] = lds128(sm + A_KVB + hs * 4096 + i * 1024 + lane * 16);
        const u32x4 pq0 = {pqd[0], pqd[1], pqd[2], pqd[3]}, pq1 = {pqd[4], pqd[5], pqd[6], pqd[7]};
        f32x16 oa = mfma32(kf[3], pq1, mfma32(kf[2], pq0, mfma32(kf[1], pq1, mfma32(kf[0], pq0, zero16()))));
        const u32 dn = __float_as_uint(oa[12]);
        auto r = __builtin_amdgcn_permlane32_swap(dn, dn, false, false);
        oa *= __builtin_amdgcn_rcpf(__uint_as_float(r[1]) + 1e-6f);
        const u32x4 f0 = pk8(oa, 0);
        const u32 o = WS_OX + ((rb * 4 + rw4) * 16 + h) * 2048 + lane * 16;
        bst_dev(f0, rws, o);
        SB();
        u32x4 f1 = pk8(oa, 1);
        f1.z &= hm; f1.w &= hm;
        f1.w |= (tag - halfsum(sum4(f0) + sum4(f1))) & ~hm;
        bst_dev(f1, rws, o + 1024);
    }
    cbar();
    if (p3) {
        LAS char* osrc[2];
#pragma unroll
        for (int nt = 0; nt < 2; ++nt) osrc[nt] = sm + A_WO + (224 * hs + 4 * hh + q) * 128 + ((nt ^ (q >> 1)) * 64) + blk * 32 + 8 * p;
        LAS char* osrp[2] = {osrc[0] + (hh ? 0 : 1024), osrc[1] + (hh ? 0 : 1024)};
        pin(osrc[0]); pin(osrc[1]); pin(osrp[0]); pin(osrp[1]);
        u32x4 wf3[8][2][2];
#pragma unroll
        for (int k = 0; k < 32; ++k) {
            const int hl = k >> 2, ks = (k >> 1) & 1, nt = k & 1;
            const int o = (hl * 28 + 16 * ks) * 128;
            wf3[hl][ks][nt] = trfrag(osrc[nt] + o, ks ? osrp[nt] + o : osrc[nt] + o + 1024);
        }
        SB();
        waitv<0>();
        u32x4 go[16];
        const u32 ob0 = WS_OX + ((r128 * 4 + rw4) * 16 + 8 * hs) * 2048;
        auto ldo = [&](int i) { ldd(go[i], dws, lane * 16, ob0 + i * 1024); };
#pragma unroll
        for (int i = 0; i < 4 * G2A; ++i) ldo(i);
        f32x16 ac[2];
        sfor<4>([&](auto gi) {
            constexpr int i = 4 * gi.v, nis = gi.v + G2A < 4 ? gi.v + G2A : 4;
            waitx<4 * (nis - gi.v - 1)>(go + i);
            {
                auto r0 = __builtin_amdgcn_permlane32_swap(sum4(go[i]) + sum4(go[i + 1]), sum4(go[i + 2]) + sum4(go[i + 3]), false, false);
                const u64 ne = __ballot(r0[0] + r0[1] != tag);
                if (ne)
                    settle<2>(((u32)ne != 0) | ((ne >> 32) != 0) << 1, [&](int k) { return halfsum(sum4(go[i + 2 * k]) + sum4(go[i + 2 * k + 1])) == tag; },
                              [&](auto k, u32 bm) { reld<k.v>(go[i + 2 * k.v], dws, lane * 16, ob0 + (i + 2 * k.v) * 1024, bm);
                                                    reld<k.v>(go[i + 2 * k.v + 1], dws, lane * 16, ob0 + (i + 2 * k.v + 1) * 1024, bm); },
                              [&](int k) { pin(go[i + 2 * k]); pin(go[i + 2 * k + 1]); });
            }
            go[i + 1].w &= hm; go[i + 3].w &= hm;
            SB();
#pragma unroll
            for (int k = 0; k < 8; ++k) {
                const int hl = 2 * gi.v + (k >> 2), ks = (k >> 1) & 1, nt = k & 1;
                ac[nt] = mfma32(wf3[hl][ks][nt], go[2 * hl + ks], (gi.v == 0 && k < 2) ? zero16() : ac[nt]);
                SB();
                if ((k & 1) == 0 && gi.v + G2A < 4) ldo(4 * (gi.v + G2A) + (k >> 1));
                SB();
            }
        });
        LAS char* xb = sm + wave * 4096 + lane * 16;
        if (hs) {
#pragma unroll
            for (int rq = 0; rq < 4; ++rq) sts128(xb + rq * 1024, quad(ac[0], rq));
            ac[0] = ac[1];
        } else {
#pragma unroll
            for (int rq = 0; rq < 4; ++rq) sts128(xb + rq * 1024, quad(ac[1], rq));
        }
        wg_barrier();
        LAS char* pb = sm + (wave ^ 4) * 4096 + lane * 16;
        LAS char* ob = sm + A_WQ + rw4 * 4608;
#pragma unroll
        for (int rq = 0; rq < 4; ++rq) {
            const f32x4 v = asf(quad(ac[0], rq)) + asf(lds128(pb + rq * 1024));
            sts64(ob + r32 * 144 + hs * 64 + rq * 16 + hh * 8, pk(v.x, v.y), pk(v.z, v.w));
        }
        wg_barrier();
        const rsrc_t ro = mkrsrc(a.out, 2048 * 1792);
        const int orow = 16 * hs + (lane >> 3);
        const u32 oo = (r128 * 128 + rw4 * 32 + orow) * 1792 + 896 + nb * 128 + (lane & 7) * 16;
        const u32x4 v0 = lds128(ob + orow * 144 + (lane & 7) * 16), v1 = lds128(ob + (8 + orow) * 144 + (lane & 7) * 16);
        bst_dev(v0, ro, oo);
        bst_dev(v1, ro, oo + 8 * 1792);
        if (xcd == 0 && j == 0 && tid == 0) st_epoch(a.ws + WS_EPA, tag);
    }
}

constexpr int S_TAB = 0;
constexpr int S_KC = 34816;
constexpr int S_P = 47104;
constexpr int S_UT = 52480;
constexpr int S_TR = 61696;

struct c2 { f32x2 r, i; };
struct hc { h2 r, i; };
DI c2 csq(c2 a) { return c2{a.r * a.r - a.i * a.i, 2.f * a.r * a.i}; }
DI hc hmul(hc a, hc b) { return hc{a.r * b.r - a.i * b.i, a.r * b.i + a.i * b.r}; }
DI hc toh(c2 a) { return hc{__builtin_convertvector(a.r, h2), __builtin_convertvector(a.i, h2)}; }
DI c2 cexp2(f32x2 lr, f32x2 rev, float pw) {
    f32x2 e = lr * pw, f = rev * pw;
    f32x2 m = {__builtin_amdgcn_exp2f(e.x), __builtin_amdgcn_exp2f(e.y)};
    f.x = __builtin_amdgcn_fractf(f.x); f.y = __builtin_amdgcn_fractf(f.y);
    f32x2 c = {__builtin_amdgcn_cosf(f.x), __builtin_amdgcn_cosf(f.y)}, s = {__builtin_amdgcn_sinf(f.x), __builtin_amdgcn_sinf(f.y)};
    return c2{m * c, m * s};
}
constexpr int tri_a(int sl) { int a = 0; while (sl >= 8 - a) { sl -= 8 - a; ++a; } return a; }
constexpr int tri_b(int sl) { int a = 0; while (sl >= 8 - a) { sl -= 8 - a; ++a; } return sl; }

DI void ssm_role(const Args& a, LAS char* sm0, const int sb) {
    const int tid = threadIdx.x & 255, lane = tid & 63, wave8 = __builtin_amdgcn_readfirstlane(threadIdx.x >> 6), wave = wave8 & 3;
    LAS char* sm = sm0 + (wave8 >> 2) * 81920;
    const int ch = wave >> 1, hf = wave & 1, d = 4 * sb + (wave8 >> 1);
    const int g = lane >> 4, r16 = lane & 15;
    const u32 po = (d * 128 + hf * 64 + lane) * 4;
    const u32 ldt = a.logdt[d];
    const u32 pA = *(const u32*)((const char*)a.logA + po), pI = *(const u32*)((const char*)a.Aim + po);
    const u32 pCr = *(const u32*)((const char*)a.Cre + po), pCi = *(const u32*)((const char*)a.Cim + po);
    const u32x2 dsk = ((const u32x2*)a.Dskip)[sb];
    cbar();
    const int G8 = (wave8 + sb) & 7, trow = 256 * G8 + 16 * r16 + 4 * g;
    const u32x4 du = mkdesc(a.u, 2048 * 896);
    const u32 uo = trow * 896 + sb * 8;
    u32x2 uu[4] = {};
    auto ldu = [&](int k) {
        SB();
        asm volatile("s_cmp_lg_u32 %6, 0\n\ts_cbranch_scc1 1f\n\tbuffer_load_dwordx2 %0, %4, %5, 0 offen\n"
                     "1:\ts_cmp_lg_u32 %6, 1\n\ts_cbranch_scc1 2f\n\tbuffer_load_dwordx2 %1, %4, %5, 0 offen offset:896\n"
                     "2:\ts_cmp_lg_u32 %6, 2\n\ts_cbranch_scc1 3f\n\tbuffer_load_dwordx2 %2, %4, %5, 0 offen offset:1792\n"
                     "3:\ts_cmp_lg_u32 %6, 3\n\ts_cbranch_scc1 4f\n\tbuffer_load_dwordx2 %3, %4, %5, 0 offen offset:2688\n4:"
                     : "+v"(uu[0]), "+v"(uu[1]), "+v"(uu[2]), "+v"(uu[3]) : "v"(uo), "s"(du), "s"((k + sb) & 3) : "scc", "memory");
        SB();
    };
    const int sidx = 2 * sb + (wave8 >> 2);
    const int ct = sidx >> 5, j = sidx & 31;
    u32 tag = ld_epoch(a.ws + WS_EPS + ct * 256);
    *(LAS u32*)(sm + S_UT + (tid >> 7) * 4608 + (tid & 127) * 4) = 0;
    const int w = 16 + r16 - 8 * g;
    u32x4 bE, bO, mkb, mkd;
    u32 sel[3][4], msk[4], pb[3];
    {
        u32 y[4];
#pragma unroll
        for (int e = 0; e < 4; ++e) {
            y[e] = (u32)(w + 64 - 2 * e) * 0x10001u - 0x10000u;
            msk[e] = hf ? 0u : (((y[e] >> 6) & 0x10001u) ^ 0x10001u) * 0xffffu;
        }
        pb[0] = w - 7;
#pragma unroll
        for (int s = 1; s < 3; ++s) {
            u32 r[4];
#pragma unroll
            for (int e = (s == 1 ? 2 : 0); e < 4; ++e)
                r[e] = ((y[e] + ((1u << (s - 1)) - 1u) * 0x10001u + ((y[e] >> s) & 0x10001u)) >> s) & 0x00ff00ffu;
            const u32 base = r[3] >> 16;
            pb[s] = base - (64 >> s);
#pragma unroll
            for (int e = (s == 1 ? 2 : 0); e < 4; ++e) sel[s][e] = (((r[e] - base * 0x10001u) ^ 0x10001u) & 0xfffffu) * 0x202u + 0x01000100u;
        }
#pragma unroll
        for (int e = 0; e < 4; ++e) {
            const int z = min(max(16 - w + 2 * e, 0), 20);
            auto two = [&](u32 m) { const u32 b2 = (m >> z) & 3u; return (b2 | (b2 << 15)) & 0x10001u; };
            bE[e] = two(0x1ff0u) * 0x3f80u; bO[e] = two(0x0fe0u) * 0x3f80u; mkb[e] = two(0x1f000u) * 0xffffu;
            mkd[e] = r16 - 4 * g - e >= 0 ? ~0u : 0u;
        }
        asm volatile("" : "+v"(sel[1][2]), "+v"(sel[1][3]), "+v"(sel[2][0]), "+v"(sel[2][1]), "+v"(sel[2][2]), "+v"(sel[2][3]), "+v"(bE), "+v"(bO), "+v"(mkb), "+v"(mkd));
        asm volatile("" : "+v"(msk[0]), "+v"(msk[1]), "+v"(msk[2]), "+v"(msk[3]));
    }
    LAS u32* const R = (LAS u32*)(sm + S_P + ch * 2688) + 655;
    LAS u32 *P0 = R - (128 * hf + (int)pb[0]) - 102, *P1 = R - (256 + 64 * hf + (int)pb[1]) - 132, *P2 = R - (384 + 32 * hf + (int)pb[2]) - 130,
            *P3 = R - (385 + 2 * (r16 - 4 * g)) - 224;
    LAS char* ubase = sm + S_UT + ch * 4608 + 480 + r16 * 32 + g * 16 - 256 * hf - 192;
    asm volatile("" : "+v"(P0), "+v"(P1), "+v"(P2), "+v"(P3), "+v"(ubase));
    LAS char* tab = sm + S_TAB + wave * 8704;
    LAS char* tw = tab + lane * 8;
    LAS char* tr = tab + r16 * 544 + g * 16;
    LAS char* kc = sm + S_KC + wave * 3072;
    SB();
#define EMIT1(row, v) sts64(tw + (row) * 544, __builtin_bit_cast(u32, (v).r), __builtin_bit_cast(u32, (v).i))
#define EMIT2(row, v0, v1) do { SB(); EMIT1(row, v0); EMIT1((row) + 1, v1); SB(); } while (0)
#define KRD() do { SB(); _Pragma("unroll") for (int ks = 0; ks < 8; ++ks) gf[ks] = lds128(tr + ks * 64); cbar(); SB(); } while (0)
#define KM2(k) do { ka = mfmah(gf[2 * (k)], ff[2 * (k)], (k) ? ka : zero4()); ka = mfmah(gf[2 * (k) + 1], ff[2 * (k) + 1], ka); } while (0)
    const float L2E = 1.44269504089f;
    const float edt = __builtin_amdgcn_exp2f(bflo(ldt) * L2E);
    const float dt = bflo(pk(edt, edt));
    f32x2 lr = unpk(pA) * L2E;
    lr = unpk(pk(__builtin_amdgcn_exp2f(lr.x), __builtin_amdgcn_exp2f(lr.y))) * dt * -L2E;
    const f32x2 rev = unpk(pI) * dt * 0.15915494309f;
    hc W[8], F[16], G[17], H, t0, t1;
    u32x4 ff[8], gf[8];
    f32x4 ka;
    c2 e = cexp2(lr, -rev, 8.f);
    F[1] = toh(e); e = csq(e); F[2] = toh(e);
    F[0] = hc{h2{1.f16, 1.f16}, h2{0.f16, 0.f16}};
    ldu(0);
    EMIT2(0, F[0], F[1]);
    e = csq(e); F[4] = toh(e); F[3] = hmul(F[1], F[2]);
    EMIT2(2, F[2], F[3]);
    e = csq(e); F[8] = toh(e); F[5] = hmul(F[4], F[1]);
    EMIT2(4, F[4], F[5]);
    F[6] = hmul(F[4], F[2]); F[7] = hmul(F[4], F[3]); F[9] = hmul(F[8], F[1]);
    EMIT2(6, F[6], F[7]);
    {
        const int xcd = sb / 14, un = ((sb % 14) * 3 + wave8) * 64 + lane, v = un - 512, h0 = (xcd & 1) * 8, r0 = (xcd >> 1) * 4;
        setm0(sm0 + 163840 - 256);
        touch_lds_dev(mkdesc(a.ws, WS_END), wave8 >= 3 || un >= 2560 ? OOB
                      : un < 512 ? WS_KVP + ((h0 + (un >> 6)) * 16 + r0) * 2048 + (un & 63) * 128
                                 : WS_OX + ((r0 * 4 + (v >> 7)) * 16 + h0) * 2048 + (v & 127) * 128);
    }
    EMIT2(8, F[8], F[9]);
    F[10] = hmul(F[8], F[2]); F[11] = hmul(F[8], F[3]);
    e = cexp2(lr, rev, 1.f); W[1] = toh(e);
    EMIT2(10, F[10], F[11]);
    F[12] = hmul(F[8], F[4]); F[13] = hmul(F[8], F[5]);
    e = csq(e); W[2] = toh(e);
    EMIT2(12, F[12], F[13]);
    F[14] = hmul(F[8], F[6]); F[15] = hmul(F[8], F[7]);
    e = csq(e); W[4] = toh(e);
    EMIT2(14, F[14], F[15]);
    u32 ce = max(max(pCr & 0x7fffu, (pCr >> 16) & 0x7fffu), max(pCi & 0x7fffu, (pCi >> 16) & 0x7fffu));
    ce = min(max(wavemax(ce) >> 7, 1u), 253u);
    const float cdn = __uint_as_float((254u - ce) << 23), cup = __uint_as_float(ce << 23) * (2.f * dt);
    G[0] = toh(c2{unpk(pCr) * cdn, unpk(pCi) * cdn});
    t1 = hmul(G[0], W[1]);
    SB();
#pragma unroll
    for (int ks = 0; ks < 8; ++ks) ff[ks] = lds128(tr + ks * 64);
    cbar();
    EMIT2(0, G[0], t1);
    W[3] = hmul(W[1], W[2]); t0 = hmul(G[0], W[2]); t1 = hmul(G[0], W[3]);
    EMIT2(2, t0, t1);
    W[5] = hmul(W[4], W[1]); t0 = hmul(G[0], W[4]); t1 = hmul(G[0], W[5]);
    EMIT2(4, t0, t1);
    W[6] = hmul(W[4], W[2]); W[7] = hmul(W[4], W[3]); t0 = hmul(G[0], W[6]); t1 = hmul(G[0], W[7]);
    ldu(1);
    EMIT2(6, t0, t1);
    e = cexp2(lr, rev, 128.f); H = toh(e);
    G[1] = hmul(G[0], H); t1 = hmul(G[1], W[1]);
    EMIT2(8, G[1], t1);
    t0 = hmul(G[1], W[2]); t1 = hmul(G[1], W[3]);
    e = csq(e); H = toh(e);
    EMIT2(10, t0, t1);
    t0 = hmul(G[1], W[4]); t1 = hmul(G[1], W[5]); G[2] = hmul(G[0], H);
    ldu(2);
    EMIT2(12, t0, t1);
    t0 = hmul(G[1], W[6]); t1 = hmul(G[1], W[7]); G[3] = hmul(G[1], H);
    EMIT2(14, t0, t1);
    t1 = hmul(G[2], W[2]);
    e = csq(e); H = toh(e);
    KRD();
    EMIT2(0, G[2], t1);
    t0 = hmul(G[2], W[4]); t1 = hmul(G[2], W[6]); G[4] = hmul(G[0], H);
    ldu(3);
    EMIT2(2, t0, t1);
    t1 = hmul(G[3], W[2]); G[5] = hmul(G[1], H); KM2(0);
    EMIT2(4, G[3], t1);
    t0 = hmul(G[3], W[4]); t1 = hmul(G[3], W[6]); KM2(1);
    EMIT2(6, t0, t1);
    t1 = hmul(G[4], W[4]); G[6] = hmul(G[2], H); KM2(2);
    EMIT2(8, G[4], t1);
    t1 = hmul(G[5], W[4]); G[7] = hmul(G[3], H); KM2(3);
    EMIT2(10, G[5], t1);
    t1 = hmul(G[6], W[4]);
    e = csq(e); H = toh(e);
    EMIT2(12, G[6], t1);
    t1 = hmul(G[7], W[4]); G[8] = hmul(G[0], H);
    sts128(kc + (128 * (g >> 1) + 4 * (g & 1) + 8 * r16) * 4, asu(ka * cup));
    EMIT2(14, G[7], t1);
    G[9] = hmul(G[1], H); G[10] = hmul(G[2], H);
    KRD();
    EMIT2(0, G[8], G[9]);
    G[11] = hmul(G[3], H); G[12] = hmul(G[4], H); G[13] = hmul(G[5], H);
    EMIT2(2, G[10], G[11]);
    G[14] = hmul(G[6], H); KM2(0); KM2(1);
    EMIT2(4, G[12], G[13]);
    G[15] = hmul(G[7], H); G[16] = hmul(G[8], H); KM2(2); KM2(3);
    EMIT2(6, G[14], G[15]);
    EMIT1(8, G[16]);
    KRD();
    {
        LAS char* k0 = kc + (g < 2 ? 256 + 64 * g + 4 * r16 : 384 + 64 * (g - 2) + 2 * r16) * 4;
        const u32x4 kb = asu(ka * cup);
        sts64(k0, kb.x, kb.y);
        sts64(k0 + (g < 2 ? 8 : 128), kb.z, kb.w);
    }
    SB();
    KM2(0); KM2(1); KM2(2); KM2(3);
    asm volatile("s_waitcnt vmcnt(0)" : "+v"(uu[0]), "+v"(uu[1]), "+v"(uu[2]), "+v"(uu[3]) :: "memory");
    {
        LAS char* ud = sm0 + S_UT + 512 + trow * 2;
        sts64(ud, PERM(uu[1].x, uu[0].x, 0x05040100u), PERM(uu[3].x, uu[2].x, 0x05040100u));
        sts64(ud + 4608, PERM(uu[1].x, uu[0].x, 0x07060302u), PERM(uu[3].x, uu[2].x, 0x07060302u));
        sts64(ud + 81920, PERM(uu[1].y, uu[0].y, 0x05040100u), PERM(uu[3].y, uu[2].y, 0x05040100u));
        sts64(ud + 81920 + 4608, PERM(uu[1].y, uu[0].y, 0x07060302u), PERM(uu[3].y, uu[2].y, 0x07060302u));
    }
    {
        LAS float* k0 = (LAS float*)(kc + (512 + 64 * g + r16) * 4);
        k0[0] = ka.x * cup; k0[16] = ka.y * cup; k0[32] = ka.z * cup; k0[48] = ka.w * cup;
    }
    wg_barrier();
    const rsrc_t rws = mkrsrc(a.ws, WS_END);
    const u32x4 dws = mkdesc(a.ws, WS_END);
    u32 tch = touch(dws, lane < 16 ? WS_YX + (2 * sb + (lane >> 3)) * 8192 + G8 * 1024 + (lane & 7) * 128 : lane == 16 ? WS_YC + (sb * 8 + G8) * 128 : OOB);
    u32x4 U[2][8];
    auto rdU = [&](int it, int bq) { U[it & 1][bq] = lds128(ubase + 512 * bq + 192 - 64 * it); };
    {
        LAS char* k0 = sm + S_KC + ch * 6144;
        LAS char* Rb = sm + S_P + ch * 2688;
        const int t = tid & 127, e1 = min(t + 128, 167);
        f32x4 a0 = asf(lds128(k0 + t * 16)), b0 = asf(lds128(k0 + 3072 + t * 16)), a1 = asf(lds128(k0 + e1 * 16)), b1 = asf(lds128(k0 + 3072 + e1 * 16));
        const float n0 = *(LAS float*)(k0 + t * 16 + 16) + *(LAS float*)(k0 + 3072 + t * 16 + 16);
        const float n1 = *(LAS float*)(k0 + e1 * 16 + 16) + *(LAS float*)(k0 + 3072 + e1 * 16 + 16);
#pragma unroll
        for (int bq = 0; bq < 8; ++bq) rdU(3, bq);
        a0 += b0; a1 += b1;
        float p0 = a0.x, p1 = a0.y, p2 = a0.z, p3 = a0.w, q0 = a1.x, q1 = a1.y, q2 = a1.z, q3 = a1.w;
        asm volatile("" : "+v"(p0), "+v"(p1), "+v"(p2), "+v"(p3), "+v"(q0), "+v"(q1), "+v"(q2), "+v"(q3));
        sts128(Rb + (163 - t) * 16, u32x4{pk(n0, p3), pk(p3, p2), pk(p2, p1), pk(p1, p0)});
        u32x4 v1 = {pk(n1, q3), pk(q3, q2), pk(q2, q1), pk(q1, q0)};
        if (t >= 36) v1 = u32x4{0, 0, 0, 0};
        if (t < 39) sts128(Rb + (t < 36 ? 35 - t : 129 + t) * 16, v1);
        if (t == 0) sts128(Rb + 164 * 16, u32x4{pk(p0, 0.f), 0, 0, 0});
    }
    wg_barrier();
    f32x4 y[8];
    {
#define Q0(i) P0[102 - (i)]
#define Q1(i) P1[132 - (i)]
#define Q2(i) P2[130 - (i)]
#define Q3(i) P3[224 - (i)]
        u32x4 T[2][4], T32, TB[4], UW[4], WA[2];
        f32x4 wd[4];
        u32 raw[7], rx[10];
        auto rdT = [&](int it, int k) {
            u32x4& f0 = T[it & 1][0];
            if (k == 0) { f0.x = Q0(32 * it + 6); f0.y = Q0(32 * it + 4); }
            if (k == 1) { f0.z = Q0(32 * it + 2); f0.w = Q0(32 * it); }
            if (k == 2) { raw[0] = Q1(16 * it); raw[1] = Q1(16 * it + 2); raw[2] = Q1(16 * it + 4); }
            if (k == 3) { raw[3] = Q2(8 * it); raw[4] = Q2(8 * it + 2); }
            if (k == 4) { raw[5] = Q2(8 * it + 64); raw[6] = Q2(8 * it + 66); }
        };
        auto mkT = [&](int it, int aa, int e) {
            T[it & 1][aa][e] = aa == 1 ? (e < 2 ? PERM(raw[2], raw[1], sel[1][2 + e]) : PERM(raw[1], raw[0], sel[1][e])) : PERM(raw[2 * aa], raw[2 * aa - 1], sel[2][e]);
        };
        auto rdX = [&](int k) {
            if (k == 0) { rx[0] = Q0(262); rx[1] = Q0(260); }
            if (k == 1) { rx[2] = Q0(258); rx[3] = Q0(256); }
            if (k == 2) { rx[4] = Q1(128); rx[5] = Q1(130); rx[6] = Q1(132); }
            if (k == 3) { rx[7] = Q2(128); rx[8] = Q2(130); rx[9] = R[-512]; }
        };
        auto fixT = [&](int k, int e) {
            if (k < 2) {
                const u32 lo = k == 0 ? rx[e] : (e < 2 ? PERM(rx[6], rx[5], sel[1][2 + e]) : PERM(rx[5], rx[4], sel[1][e]));
                T[0][1 + k][e] = (lo & msk[e]) | (T[0][1 + k][e] & ~msk[e]);
            } else T32[e] = (PERM(rx[8], rx[7], sel[2][e]) & msk[e]) | (PERM(rx[9], rx[9], 0x03020302u) & mkb[e]);
        };
        LAS char* uw = ubase + 192 + 256 * hf + 1024 * (1 - hf);
        auto rdC = [&](int k) {
            if (k < 4) UW[k] = lds128(uw + 512 * (k >> 1) - 16 * (k & 1));
            else { const int dl = (k - 4) >> 1, e = 2 * (k & 1); TB[dl][e] = Q3(128 + 32 * dl - 2 * e); TB[dl][e + 1] = Q3(128 + 32 * dl - 2 * e - 2); }
        };
#pragma unroll
        for (int k = 0; k < 5; ++k) rdT(3, k);
        SB();
        sfor<104>([&](auto gs) {
            constexpr int it = 3 - gs.v / 26, sl = gs.v % 26, aa = tri_a(sl), bq = tri_b(sl);
            y[aa + bq] = mfma(T[it & 1][aa], U[it & 1][bq], (it == 3 && aa == 0) ? zero4() : y[aa + bq]);
            SB();
            if constexpr (it == 3) {
                if constexpr (sl < 6) { mkT(3, 1 + (sl >> 1), 2 * (sl & 1)); mkT(3, 1 + (sl >> 1), 2 * (sl & 1) + 1); }
                else if constexpr (sl < 14) { if (sl < 11) rdT(2, sl - 6); rdU(2, sl - 6); }
                else mkT(2, 1 + ((sl - 14) >> 2), (sl - 14) & 3);
            } else if constexpr (it > 0) {
                if constexpr (sl < 8) { if (sl < 5) rdT(it - 1, sl); rdU(it - 1, sl); }
                if constexpr (it == 1 && sl >= 5 && sl < 9) rdX(sl - 5);
                if constexpr (it == 2 && sl >= 10 && sl < 22) mkT(1, 1 + ((sl - 10) >> 2), (sl - 10) & 3);
                if constexpr (it == 1 && sl >= 10 && sl < 16) { mkT(0, 1 + ((sl - 10) >> 1), 2 * (sl & 1)); mkT(0, 1 + ((sl - 10) >> 1), 2 * (sl & 1) + 1); }
                if constexpr (it == 1 && sl >= 16 && sl < 24) fixT((sl - 16) >> 2, (sl - 16) & 3);
            } else {
                if constexpr (sl < 12) rdC(sl);
                if constexpr (sl < 4) fixT(2, sl);
                if constexpr (sl >= 10 && sl < 14) wd[sl - 10] = mfma(UW[sl - 10], (sl & 1) ? bE : bO, zero4());
                if constexpr (sl >= 16 && sl < 24) WA[(sl - 16) >> 2][sl & 3] = pk(wd[2 * ((sl - 16) >> 2) + 1][sl & 3], wd[2 * ((sl - 16) >> 2)][sl & 3]);
                if constexpr (sl == 24) TB[0] &= mkd;
            }
            SB();
        });
        if (hf) {
#pragma unroll
            for (int k = 0; k < 4; ++k) y[4 + k] = mfma(WA[0], TB[k], y[4 + k]);
#pragma unroll
            for (int k = 0; k < 3; ++k) y[5 + k] = mfma(WA[1], TB[k], y[5 + k]);
        } else {
#pragma unroll
            for (int k = 0; k < 4; ++k) y[4 + k] = mfma(T32, U[0][k], y[4 + k]);
            y[6] = mfma(WA[0], TB[0], y[6]); y[7] = mfma(WA[0], TB[1], y[7]); y[7] = mfma(WA[1], TB[0], y[7]);
        }
    }
    {
        LAS char* yd = sm + S_TAB + wave * 8192 + lane * 16;
#pragma unroll
        for (int i = 0; i < 8; ++i) sts128(yd + i * 1024, asu(y[i]));
    }
    asm volatile("s_waitcnt vmcnt(0)" : "+v"(tag), "+v"(tch) :: "memory");
    tag += 1;
    wg_barrier();
    {
        LAS char* ys = sm0 + S_TAB + G8 * 1024 + lane * 16;
        u32x4 o[2];
#pragma unroll
        for (int c = 0; c < 2; ++c) {
            const f32x4 y0 = asf(lds128(ys + c * 81920)) + asf(lds128(ys + c * 81920 + 8192));
            const f32x4 y1 = asf(lds128(ys + c * 81920 + 16384)) + asf(lds128(ys + c * 81920 + 24576));
            const float d0 = bflo(dsk[c]), d1 = bfhi(dsk[c]);
#pragma unroll
            for (int i = 0; i < 4; ++i) o[c][i] = pk(y0[i] + bflo(uu[i][c]) * d0, y1[i] + bfhi(uu[i][c]) * d1);
            bst_dev(o[c], rws, WS_YX + (2 * sb + c) * 8192 + G8 * 1024 + lane * 16);
        }
        const u32 s0 = rowsum(sum4(o[0])), s1 = rowsum(sum4(o[1]));
        bst32_dev(tag - (r16 ? s1 : s0), rws, r16 < 2 ? WS_YC + (sb * 8 + G8) * 128 + g * 8 + r16 * 4 : OOB);
    }
    {
        const u32 cofs = (ct * 32 + (tid >> 4)) * 8192 + j * 256 + (tid & 15) * 16;
        const u32 ccs = ((ct * 16 + (tid >> 5)) * 8 + (j >> 2)) * 128 + (j & 3) * 8 + ((tid >> 4) & 1) * 4;
        LAS char* trw = sm + S_TR + (tid & 7) * 288 + (tid >> 3) * 8;
        LAS char* trr = sm + S_TR + (((tid >> 4) & 3) * 8 + (tid >> 6)) * 288 + (tid & 15) * 16;
        const u32 oo = (256 * (j >> 2) + 16 * (tid >> 4) + 4 * (j & 3) + 2 * ((tid >> 3) & 1)) * 1792 + 128 * ct + (tid & 7) * 16;
        waitv<0>();
        u32x4 v[2]; u32 vc[2];
#pragma unroll
        for (int i = 0; i < 2; ++i) { v[i] = bld_dev(rws, cofs, WS_YX + i * 16 * 8192); vc[i] = bld32_dev(rws, ccs, WS_YC + i * 8192); }
        cbar();
        auto ok = [&](int i) { return rowsum(sum4(v[i])) + vc[i] == tag; };
        settle<2>(badmask<2>(ok), ok,
                  [&](auto i, u32 bm) { reld<i.v>(v[i.v], dws, cofs, WS_YX + i.v * 16 * 8192, bm); reld32<i.v>(vc[i.v], dws, ccs, WS_YC + i.v * 8192, bm); },
                  [&](int i) { pin(v[i]); pin(vc[i]); });
        sts128(trr, v[0]); sts128(trr + 4 * 288, v[1]);
        wg_barrier();
        const u32x2 s0 = *(LAS u32x2*)trw, s1 = *(LAS u32x2*)(trw + 8 * 288), s2 = *(LAS u32x2*)(trw + 16 * 288), s3 = *(LAS u32x2*)(trw + 24 * 288);
        const rsrc_t ro = mkrsrc(a.out, 2048 * 1792);
        bst_dev(u32x4{s0.x, s1.x, s2.x, s3.x}, ro, oo);
        bst_dev(u32x4{s0.y, s1.y, s2.y, s3.y}, ro, oo + 1792);
        if (j == 0 && tid == 0) st_epoch(a.ws + WS_EPS + ct * 256, tag);
    }
}

DI void warm_attn(const Args& a, LAS char* sm, int xcd, int hi) {
    const int lane = threadIdx.x & 63, wave8 = __builtin_amdgcn_readfirstlane(threadIdx.x >> 6);
    const int u = hi * 5 + wave8, idx = (u - 24) * 64 + lane;
    if (wave8 >= 5) return;
    setm0(sm + 163840 - 256);
    if (u < 24) {
        const int rbl = (xcd >> 1) * 4 + u / 6, kc = (2 * (rbl + (xcd & 1) * 4) + 1 + 2 * ((u % 6) >> 1)) % 7;
        touch_lds(mkdesc(a.x, 2048 * 896), (rbl * 128 + (u & 1) * 64 + lane) * 896 + kc * 128);
    } else touch_lds(mkdesc(a.wqkv, 448 * 2688), (idx >> 3) * 2688 + (1 + ((idx >> 2) & 1)) * 896 + ((xcd & 1) * 3 + (idx & 3)) * 128);
}

#if defined(__HIP_DEVICE_COMPILE__)
#define NOPK __attribute__((target("no-packed-fp32-ops")))
#else
#define NOPK
#endif
__global__ NOPK __launch_bounds__(512, 2) void fused_kernel(Args a) {
    __shared__ __attribute__((aligned(16))) char smem[163840];
    LAS char* sm = (LAS char*)smem;
    const int xcd = blockIdx.x & 7, j = blockIdx.x >> 3;
    const bool is_attn = (j & 1) == 0;
    if (__builtin_expect(!is_attn, 1)) warm_attn(a, sm, xcd, j >> 1);
    cbar();
    if (__builtin_expect(is_attn, 1)) attn_role(a, sm, xcd, j >> 1);
    else if (j < 28) ssm_role(a, sm, xcd * 14 + (j >> 1));
    else {
        const int t = (j >> 1 & 1) * 512 + threadIdx.x;
        setm0(sm);
#pragma unroll
        for (int i = 0; i < 2; ++i) touch_lds(mkdesc(a.wqkv, 448 * 2688), ((t + 1024 * i) >> 2) * 2688 + ((xcd & 1) * 3 + (t & 3)) * 128);
#pragma unroll
        for (int i = 0; i < 4; ++i) touch_lds(mkdesc(a.wo, 448 * 896), (t + 1024 * i) * 128);
        waitv<0>();
    }
}

extern "C" void kernel_launch(void* const* d_in, const int* in_sizes, int n_in,
                              void* d_out, int out_size, void* d_ws, size_t ws_size,
                              hipStream_t stream) {
    Args a;
    a.u = (const u16*)d_in[0]; a.logA = (const u16*)d_in[1]; a.Aim = (const u16*)d_in[2];
    a.Cre = (const u16*)d_in[3]; a.Cim = (const u16*)d_in[4]; a.logdt = (const u16*)d_in[5];
    a.Dskip = (const u16*)d_in[6]; a.x = (const u16*)d_in[7]; a.wqkv = (const u16*)d_in[8];
    a.wo = (const u16*)d_in[9]; a.out = (u16*)d_out; a.ws = (char*)d_ws;
    fused_kernel<<<256, 512, 0, stream>>>(a);
}
```
